```python
import torch

def _softmax(x, axis=-1):
    return torch.softmax(x, dim=axis)

def _gelu(x):
    return torch.nn.functional.gelu(x, approximate='tanh')

def _silu(x):
    return torch.nn.functional.silu(x)

def _sigmoid(x):
    return torch.sigmoid(x)

def _layernorm(x, axis=-1, eps=1e-05):
    m = x.mean(dim=axis, keepdim=True)
    v = x.var(dim=axis, keepdim=True, unbiased=False)
    return (x - m) / torch.sqrt(v + eps)

def _scatter_add(tgt, idx, src, axis=0):
    return tgt.clone().index_add_(axis, idx, src)

def _segment_sum(src, seg, n):
    out = torch.zeros((n,) + src.shape[1:], dtype=src.dtype)
    return out.index_add_(0, seg, src)

def _pad(x, pw):
    flat = tuple((v for pair in reversed(pw) for v in pair))
    return torch.nn.functional.pad(x, flat)
kernel_name = 'standalone__conformer_family_zipformer__speech'

def setup_inputs():

    _w = lambda a: (lambda t: t.to(torch.bfloat16) if t.is_floating_point() else t)(torch.as_tensor(a))
    B, T, D, K, H = (2, 4000, 256, 5, 4)
    sc = 0.1
    return {'x': _w((lambda *s: torch.randn(*s, dtype=torch.bfloat16) * 0.8)(B, T, D)), 'w_ff1a': _w((lambda *s: torch.randn(*s, dtype=torch.bfloat16) * 0.8)(D, 4 * D) * sc), 'w_ff1b': _w((lambda *s: torch.randn(*s, dtype=torch.bfloat16) * 0.8)(4 * D, D) * sc), 'w_qkv': _w((lambda *s: torch.randn(*s, dtype=torch.bfloat16) * 0.8)(D, 3 * D) * sc), 'w_o': _w((lambda *s: torch.randn(*s, dtype=torch.bfloat16) * 1.6)(D, D) * sc), 'w_pw1': _w((lambda *s: torch.randn(*s, dtype=torch.bfloat16) * 1.6)(D, 2 * D) * sc), 'dw': _w((lambda *s: torch.randn(*s, dtype=torch.bfloat16) * 0.4)(K, D)), 'w_pw2': _w((lambda *s: torch.randn(*s, dtype=torch.bfloat16) * 0.4)(D, D) * sc), 'w_ff2a': _w((lambda *s: torch.randn(*s, dtype=torch.bfloat16) * 0.8)(D, 4 * D) * sc), 'w_ff2b': _w((lambda *s: torch.randn(*s, dtype=torch.bfloat16) * 0.8)(4 * D, D) * sc)}

def reference(x, w_ff1a, w_ff1b, w_qkv, w_o, w_pw1, dw, w_pw2, w_ff2a, w_ff2b):
    B, T, D = x.shape
    K = dw.shape[0]
    H = 4
    Dh = D // H
    h = _layernorm(x, axis=-1)
    h = _silu(h @ w_ff1a) @ w_ff1b
    x = x + 0.5 * h
    h = _layernorm(x, axis=-1)
    qkv = h @ w_qkv
    q = (lambda x, *d: x.permute(*d))(qkv[..., :D].reshape(B, T, H, Dh), 0, 2, 1, 3)
    k = (lambda x, *d: x.permute(*d))(qkv[..., D:2 * D].reshape(B, T, H, Dh), 0, 2, 1, 3)
    v = (lambda x, *d: x.permute(*d))(qkv[..., 2 * D:].reshape(B, T, H, Dh), 0, 2, 1, 3)
    scale = Dh ** (-0.5)
    scores = torch.einsum('bhqd,bhkd->bhqk', q, k) * scale
    p = _softmax(scores, axis=-1)
    att = torch.einsum('bhqk,bhkd->bhqd', p, v)
    att = (lambda x, *d: x.permute(*d))(att, 0, 2, 1, 3).reshape(B, T, D)
    x = x + att @ w_o
    h = _layernorm(x, axis=-1)
    h = h @ w_pw1
    a, b = (h[..., :D].clone(), h[..., D:].clone())
    h = a * _sigmoid(b)
    pad = K // 2
    hp = _pad(h, ((0, 0), (pad, pad), (0, 0)))
    c = torch.zeros_like(h)
    for i in range(K):
        c = c + hp[:, i:i + T, :] * dw[i][None, None, :]
    x = x + _silu(c) @ w_pw2
    h = _layernorm(x, axis=-1)
    h = _silu(h @ w_ff2a) @ w_ff2b
    x = x + 0.5 * h
    return _layernorm(x, axis=-1)
```

```cpp
#include <hip/hip_runtime.h>
#include <hip/hip_bf16.h>
#include <stdint.h>


typedef short bf16x8 __attribute__((ext_vector_type(8)));
typedef float f32x16 __attribute__((ext_vector_type(16)));
typedef float f32x2 __attribute__((ext_vector_type(2)));
typedef float f32x4 __attribute__((ext_vector_type(4)));
typedef unsigned u32x4 __attribute__((ext_vector_type(4)));
typedef unsigned u32x2 __attribute__((ext_vector_type(2)));
typedef __bf16 bf16v2 __attribute__((ext_vector_type(2)));

#define DI __device__ __forceinline__
#define SB() __builtin_amdgcn_sched_barrier(0)
#define MFMA(a, b, c) __builtin_amdgcn_mfma_f32_32x32x16_bf16(__builtin_bit_cast(bf16x8, (a)), __builtin_bit_cast(bf16x8, (b)), (c), 0, 0, 0)

constexpr int T_SEQ = 4000, DM = 256, NTOK = 8000, NTILE = 250, TPB = 125;
constexpr int NFA = 176;
constexpr int NFC = 192;
constexpr int RING = 16;

constexpr size_t WS_WA = 0;
constexpr size_t WS_WC = WS_WA + (size_t)8 * NFA * 64;
constexpr size_t WS_X1 = WS_WC + (size_t)8 * NFC * 64;
constexpr size_t WS_Q  = WS_X1 + (size_t)NTILE * 16 * 64;
constexpr size_t WS_KV = WS_Q + (size_t)8 * TPB * 4 * 64;
constexpr size_t WS_AT = WS_KV + (size_t)8 * TPB * 8 * 64 + 4096;

DI unsigned pack2(float lo, float hi) {
    f32x2 v = {lo, hi};
    bf16v2 r = __builtin_convertvector(v, bf16v2);
    return __builtin_bit_cast(unsigned, r);
}
DI float bflo(unsigned u) { return __uint_as_float(u << 16); }
DI float bfhi(unsigned u) { return __uint_as_float(u & 0xffff0000u); }
DI u32x4 pack8(const f32x16& a, int s) {
    u32x4 r;
    r[0] = pack2(a[8 * s + 0], a[8 * s + 1]);
    r[1] = pack2(a[8 * s + 2], a[8 * s + 3]);
    r[2] = pack2(a[8 * s + 4], a[8 * s + 5]);
    r[3] = pack2(a[8 * s + 6], a[8 * s + 7]);
    return r;
}
DI void unpack16(f32x16& a, const u32x4& f0, const u32x4& f1) {
#pragma unroll
    for (int i = 0; i < 4; ++i) {
        a[2 * i] = bflo(f0[i]); a[2 * i + 1] = bfhi(f0[i]);
        a[8 + 2 * i] = bflo(f1[i]); a[8 + 2 * i + 1] = bfhi(f1[i]);
    }
}
DI float xor32(float v) { return __shfl_xor(v, 32); }
DI float sigmoidf_(float x) { return __builtin_amdgcn_rcpf(1.0f + __builtin_amdgcn_exp2f(-1.4426950408889634f * x)); }
DI float siluf_(float x) { return x * sigmoidf_(x); }

struct PackArgs {
    const uint16_t* w1a; const uint16_t* w1b; const uint16_t* wqkv; const uint16_t* wo;
    const uint16_t* wp1; const uint16_t* wp2; const uint16_t* w2a; const uint16_t* w2b;
    u32x4* ws;
};

__global__ __launch_bounds__(256) void k_pack(PackArgs p) {
    const int gid = blockIdx.x * 256 + threadIdx.x;
    const int lane = gid & 63, fg = gid >> 6;
    if (fg >= 8 * (NFA + NFC)) return;
    const uint16_t* W; int N, nt, ks;
    if (fg < 8 * NFA) {
        const int w = fg / NFA, f = fg % NFA;
        if (f < 64)       { W = p.w1a; N = 1024; ks = f >> 2; nt = 4 * w + (f & 3); }
        else if (f < 128) { W = p.w1b; N = 256; ks = f - 64; nt = w; }
        else              { const int g = f - 128; W = p.wqkv; N = 768; ks = g / 3; nt = w + 8 * (g % 3); }
    } else {
        const int fg2 = fg - 8 * NFA, w = fg2 / NFC, f = fg2 % NFC;
        if (f < 16)       { W = p.wo; N = 256; ks = f; nt = w; }
        else if (f < 48)  { const int g = f - 16; W = p.wp1; N = 512; ks = g >> 1; nt = w + 8 * (g & 1); }
        else if (f < 64)  { W = p.wp2; N = 256; ks = f - 48; nt = w; }
        else if (f < 128) { const int g = f - 64; W = p.w2a; N = 1024; ks = g >> 2; nt = 4 * w + (g & 3); }
        else              { W = p.w2b; N = 256; ks = f - 128; nt = w; }
    }
    const int r = lane & 31, h = lane >> 5;
    const uint16_t* src = W + (size_t)(16 * ks + 4 * h) * N + 32 * nt + r;
    unsigned e[8];
#pragma unroll
    for (int j = 0; j < 8; ++j) e[j] = src[(size_t)(8 * (j >> 2) + (j & 3)) * N];
    u32x4 o;
#pragma unroll
    for (int i = 0; i < 4; ++i) o[i] = e[2 * i] | (e[2 * i + 1] << 16);
    p.ws[gid] = o;
}

#define RING_NEXT(F, NF) do { if ((F) + RING < (NF)) ring[(F) % RING] = wp[((F) + RING) * 64]; } while (0)

DI void ln_stats(const f32x16& v, f32x2* stats, int w, int lane, float& mean, float& rstd) {
    float s = 0.f;
#pragma unroll
    for (int i = 0; i < 16; ++i) s += v[i];
    s += xor32(s);
    const float mw = s * (1.0f / 32.0f);
    float q = 0.f;
#pragma unroll
    for (int i = 0; i < 16; ++i) { const float d = v[i] - mw; q += d * d; }
    q += xor32(q);
    if (lane < 32) stats[lane * 8 + w] = (f32x2){mw, q};
    __syncthreads();
    const f32x4* sp = (const f32x4*)(stats + (lane & 31) * 8);
    f32x4 a0 = sp[0], a1 = sp[1], a2 = sp[2], a3 = sp[3];
    const float m = ((a0[0] + a0[2]) + (a1[0] + a1[2]) + (a2[0] + a2[2]) + (a3[0] + a3[2])) * 0.125f;
    float M2 = (a0[1] + a0[3]) + (a1[1] + a1[3]) + (a2[1] + a2[3]) + (a3[1] + a3[3]);
    float dd = 0.f, d;
    d = a0[0] - m; dd += d * d; d = a0[2] - m; dd += d * d;
    d = a1[0] - m; dd += d * d; d = a1[2] - m; dd += d * d;
    d = a2[0] - m; dd += d * d; d = a2[2] - m; dd += d * d;
    d = a3[0] - m; dd += d * d; d = a3[2] - m; dd += d * d;
    M2 += 32.0f * dd;
    mean = m;
    rstd = __builtin_amdgcn_rsqf(M2 * (1.0f / 256.0f) + 1e-5f);
}

DI void ln_stats2(const f32x16& v, const f32x16& u, f32x2* stats, f32x2* stats2, int w, int lane,
                  float& mean, float& rstd, float& mean2, float& rstd2) {
    float s = 0.f, s2 = 0.f;
#pragma unroll
    for (int i = 0; i < 16; ++i) { s += v[i]; s2 += u[i]; }
    s += xor32(s); s2 += xor32(s2);
    const float mw = s * (1.0f / 32.0f), mw2 = s2 * (1.0f / 32.0f);
    float q = 0.f, q2 = 0.f;
#pragma unroll
    for (int i = 0; i < 16; ++i) { const float d = v[i] - mw; q += d * d; const float e = u[i] - mw2; q2 += e * e; }
    q += xor32(q); q2 += xor32(q2);
    if (lane < 32) { stats[lane * 8 + w] = (f32x2){mw, q}; stats2[lane * 8 + w] = (f32x2){mw2, q2}; }
    __syncthreads();
#pragma unroll
    for (int t = 0; t < 2; ++t) {
        const f32x4* sp = (const f32x4*)((t ? stats2 : stats) + (lane & 31) * 8);
        f32x4 a0 = sp[0], a1 = sp[1], a2 = sp[2], a3 = sp[3];
        const float m = ((a0[0] + a0[2]) + (a1[0] + a1[2]) + (a2[0] + a2[2]) + (a3[0] + a3[2])) * 0.125f;
        float M2 = (a0[1] + a0[3]) + (a1[1] + a1[3]) + (a2[1] + a2[3]) + (a3[1] + a3[3]);
        float dd = 0.f, d;
        d = a0[0] - m; dd += d * d; d = a0[2] - m; dd += d * d;
        d = a1[0] - m; dd += d * d; d = a1[2] - m; dd += d * d;
        d = a2[0] - m; dd += d * d; d = a2[2] - m; dd += d * d;
        d = a3[0] - m; dd += d * d; d = a3[2] - m; dd += d * d;
        M2 += 32.0f * dd;
        const float r = __builtin_amdgcn_rsqf(M2 * (1.0f / 256.0f) + 1e-5f);
        if (t) { mean2 = m; rstd2 = r; } else { mean = m; rstd = r; }
    }
}

DI void norm_store(const f32x16& v, float mean, float rstd, u32x4* dst  , int w, int lane) {
    f32x16 n;
#pragma unroll
    for (int i = 0; i < 16; ++i) n[i] = (v[i] - mean) * rstd;
    dst[(2 * w) * 64 + lane] = pack8(n, 0);
    dst[(2 * w + 1) * 64 + lane] = pack8(n, 1);
}

DI float rbf(float x) { return bflo(pack2(x, 0.f) & 0xffffu); }

__global__ __launch_bounds__(512, 2) void k_stageA(const uint16_t* __restrict__ x, u32x4* __restrict__ ws) {
    __shared__ u32x4 lds[16 * 64 + 64 * 64 + 128];
    u32x4* fx = lds;
    u32x4* fh = lds + 16 * 64;
    f32x2* stats = (f32x2*)(lds + 16 * 64 + 64 * 64);
    const int tile = blockIdx.x, tid = threadIdx.x, lane = tid & 63, h = lane >> 5, m = lane & 31;
    const int w = __builtin_amdgcn_readfirstlane(tid >> 6);
    const u32x4* wp = ws + WS_WA + (size_t)w * NFA * 64 + lane;
    u32x4 ring[RING];
#pragma unroll
    for (int i = 0; i < RING; ++i) ring[i] = wp[i * 64];

    f32x16 xv;
    {
        const uint16_t* xp = x + (size_t)(tile * 32 + m) * DM + 32 * w + 4 * h;
#pragma unroll
        for (int g = 0; g < 4; ++g) {
            const u32x2 t = *(const u32x2*)(xp + 8 * g);
            xv[4 * g + 0] = bflo(t[0]); xv[4 * g + 1] = bfhi(t[0]); xv[4 * g + 2] = bflo(t[1]); xv[4 * g + 3] = bfhi(t[1]);
        }
    }
    float mean, rstd;
    ln_stats(xv, stats, w, lane, mean, rstd);
    norm_store(xv, mean, rstd, fx, w, lane);
    __syncthreads();

    {
        f32x16 acc[4];
#pragma unroll
        for (int i = 0; i < 4; ++i) acc[i] = (f32x16){};
        u32x4 bn = fx[lane];
#pragma unroll
        for (int ks = 0; ks < 16; ++ks) {
            const u32x4 b = bn;
            if (ks + 1 < 16) bn = fx[(ks + 1) * 64 + lane];
#pragma unroll
            for (int i = 0; i < 4; ++i) {
                const int F = ks * 4 + i;
                acc[i] = MFMA(ring[F % RING], b, acc[i]);
                RING_NEXT(F, NFA);
            }
            SB();
        }
#pragma unroll
        for (int i = 0; i < 4; ++i) {
#pragma unroll
            for (int r = 0; r < 16; ++r) acc[i][r] = siluf_(acc[i][r]);
            fh[((4 * w + i) * 2 + 0) * 64 + lane] = pack8(acc[i], 0);
            fh[((4 * w + i) * 2 + 1) * 64 + lane] = pack8(acc[i], 1);
        }
    }
    __syncthreads();
    {
        f32x16 acc = (f32x16){};
        u32x4 bq[4];
#pragma unroll
        for (int i = 0; i < 4; ++i) bq[i] = fh[i * 64 + lane];
#pragma unroll
        for (int ks = 0; ks < 64; ++ks) {
            const int F = 64 + ks;
            acc = MFMA(ring[F % RING], bq[ks & 3], acc);
            if (ks + 4 < 64) bq[ks & 3] = fh[(ks + 4) * 64 + lane];
            RING_NEXT(F, NFA);
            SB();
        }
#pragma unroll
        for (int r = 0; r < 16; ++r) xv[r] = rbf(xv[r] + 0.5f * acc[r]);
    }
    {
        u32x4* xo = ws + WS_X1 + (size_t)tile * 16 * 64;
        xo[(2 * w) * 64 + lane] = pack8(xv, 0);
        xo[(2 * w + 1) * 64 + lane] = pack8(xv, 1);
    }
    ln_stats(xv, stats, w, lane, mean, rstd);
    norm_store(xv, mean, rstd, fx, w, lane);
    __syncthreads();
    {
        f32x16 aq = (f32x16){}, ak = (f32x16){}, av = (f32x16){};
        u32x4 bn = fx[lane];
#pragma unroll
        for (int ks = 0; ks < 16; ++ks) {
            const u32x4 b = bn;
            if (ks + 1 < 16) bn = fx[(ks + 1) * 64 + lane];
            const int F = 128 + ks * 3;
            aq = MFMA(ring[F % RING], b, aq); RING_NEXT(F, NFA);
            ak = MFMA(ring[(F + 1) % RING], b, ak); RING_NEXT(F + 1, NFA);
            av = MFMA(b, ring[(F + 2) % RING], av); RING_NEXT(F + 2, NFA);
            SB();
        }
        const float qs = 0.125f * 1.4426950408889634f;
#pragma unroll
        for (int r = 0; r < 16; ++r) aq[r] *= qs;
        const int b = tile / TPB, tt = tile % TPB, bh = b * 4 + (w >> 1), t2 = w & 1;
        u32x4* qo = ws + WS_Q + ((size_t)(bh * TPB + tt) * 4 + 2 * t2) * 64 + lane;
        qo[0] = pack8(aq, 0); qo[64] = pack8(aq, 1);
        u32x4* ko = ws + WS_KV + ((size_t)(bh * TPB + tt) * 8 + 2 * t2) * 64 + lane;
        ko[0] = pack8(ak, 0); ko[64] = pack8(ak, 1);
        ko[4 * 64] = pack8(av, 0); ko[5 * 64] = pack8(av, 1);
    }
}

DI void glds16(const void* g, unsigned lds_base) {
    unsigned sv;
    asm volatile("s_mov_b32 %0, m0\n\ts_mov_b32 m0, %2\n\ts_nop 0\n\tglobal_load_lds_dwordx4 %1, off\n\ts_mov_b32 m0, %0"
                 : "=&s"(sv) : "v"(g), "s"(lds_base) : "memory");
}
#define WAIT_BAR(N) asm volatile("s_waitcnt vmcnt(" #N ") lgkmcnt(0)\n\ts_barrier" ::: "memory")

constexpr int AT_SLOT = 2048;
constexpr float AT_BIG = 1073741824.0f;

__global__ __launch_bounds__(512, 2) void k_attn(const u32x4* __restrict__ ws_in, u32x4* __restrict__ ws) {
    __shared__ u32x4 lds[3 * AT_SLOT];
    const int tid = threadIdx.x, lane = tid & 63, h = lane >> 5;
    const int w = __builtin_amdgcn_readfirstlane(tid >> 6);
    const int bh = blockIdx.x & 7, qb = blockIdx.x >> 3, qi = w & 3, g = w >> 2;
    int qt = 4 * qb + qi; const bool qvalid = qt < TPB; if (!qvalid) qt = TPB - 1;
    const u32x4* kv = ws_in + WS_KV + (size_t)bh * TPB * 8 * 64;
    const unsigned lds0 = (unsigned)(uintptr_t)lds;
    const u32x4* src = kv + w * 256 + lane;
#define DMA(step, slot) do { _Pragma("unroll") for (int c_ = 0; c_ < 4; ++c_) \
        glds16(src + (size_t)(step) * AT_SLOT + c_ * 64, (unsigned)__builtin_amdgcn_readfirstlane(lds0 + ((slot) * AT_SLOT + w * 256 + c_ * 64) * 16)); } while (0)
    DMA(0, 0); DMA(1, 1);
    u32x4 q[4];
    {
        const u32x4* qp = ws_in + WS_Q + (size_t)(bh * TPB + qt) * 4 * 64 + lane;
#pragma unroll
        for (int f = 0; f < 4; ++f) q[f] = qp[f * 64];
    }
    f32x16 o0 = (f32x16){}, o1 = (f32x16){};
    float l = 0.f, negm = 0.f;
    f32x16 cm = (f32x16){};
    asm volatile("s_waitcnt vmcnt(4)" ::: "memory");
    WAIT_BAR(0);
    constexpr int NSTEP = 32;
    int slot = 0;
    for (int i = 0; i < NSTEP; ++i) {
        if (i + 2 < NSTEP) { const int s2 = slot == 0 ? 2 : slot - 1; DMA(i + 2, s2); }
        const bool last = (i == NSTEP - 1);
        if (!(last && g == 1)) {
            const u32x4* kb = lds + slot * AT_SLOT + (2 * g) * 512 + lane;
            f32x16 sa, sb;
            sa = MFMA(kb[0], q[0], cm); sa = MFMA(kb[64], q[1], sa); sa = MFMA(kb[128], q[2], sa); sa = MFMA(kb[192], q[3], sa);
            if (!last) { sb = MFMA(kb[512], q[0], cm); sb = MFMA(kb[512 + 64], q[1], sb); sb = MFMA(kb[512 + 128], q[2], sb); sb = MFMA(kb[512 + 192], q[3], sb); }
            else {
#pragma unroll
                for (int r = 0; r < 16; ++r) sb[r] = -INFINITY;
            }
            if (i == 0) {
                float mx = sa[0];
#pragma unroll
                for (int r = 0; r < 16; ++r) { mx = fmaxf(mx, sa[r]); mx = fmaxf(mx, sb[r]); }
                mx = fmaxf(mx, xor32(mx));
                negm = -mx;
#pragma unroll
                for (int r = 0; r < 16; ++r) { sa[r] += negm; sb[r] += negm; cm[r] = negm; }
            }
            f32x16 pa, pb; float sum = 0.f;
#pragma unroll
            for (int r = 0; r < 16; ++r) { pa[r] = __builtin_amdgcn_exp2f(sa[r]); pb[r] = __builtin_amdgcn_exp2f(sb[r]); sum += pa[r] + pb[r]; }
            if (__builtin_expect(__any(!(sum <= AT_BIG)), 0)) {
                float mx = sa[0];
#pragma unroll
                for (int r = 0; r < 16; ++r) { mx = fmaxf(mx, sa[r]); mx = fmaxf(mx, sb[r]); }
                mx = fmaxf(mx, xor32(mx));
                const float dl = fmaxf(mx, 0.f);
                const float f = __builtin_amdgcn_exp2f(-dl);
                negm -= dl; l *= f; sum = 0.f;
#pragma unroll
                for (int r = 0; r < 16; ++r) {
                    o0[r] *= f; o1[r] *= f; cm[r] = negm;
                    pa[r] = __builtin_amdgcn_exp2f(sa[r] - dl); pb[r] = __builtin_amdgcn_exp2f(sb[r] - dl); sum += pa[r] + pb[r];
                }
            }
            l += sum;
            const u32x4 pa0 = pack8(pa, 0), pa1 = pack8(pa, 1), pb0 = pack8(pb, 0), pb1 = pack8(pb, 1);
            const u32x4* vb = kb + 256;
            o0 = MFMA(vb[0], pa0, o0); o1 = MFMA(vb[128], pa0, o1);
            o0 = MFMA(vb[64], pa1, o0); o1 = MFMA(vb[192], pa1, o1);
            if (!last) {
                o0 = MFMA(vb[512], pb0, o0); o1 = MFMA(vb[512 + 128], pb0, o1);
                o0 = MFMA(vb[512 + 64], pb1, o0); o1 = MFMA(vb[512 + 192], pb1, o1);
            }
        }
        if (i + 2 < NSTEP) { WAIT_BAR(4); } else { WAIT_BAR(0); }
        slot = slot == 2 ? 0 : slot + 1;
    }
#undef DMA
    l += xor32(l);
    float* cf = (float*)lds;
    if (g == 1) {
        float* dst = cf + qi * 34 * 64 + lane;
#pragma unroll
        for (int r = 0; r < 16; ++r) { dst[r * 64] = o0[r]; dst[(16 + r) * 64] = o1[r]; }
        dst[32 * 64] = negm; dst[33 * 64] = l;
    }
    __syncthreads();
    if (g == 0) {
        const float* s = cf + qi * 34 * 64 + lane;
        const float negm1 = s[32 * 64], l1 = s[33 * 64];
        const float nm = fminf(negm, negm1);
        const float f0 = __builtin_amdgcn_exp2f(nm - negm), f1 = __builtin_amdgcn_exp2f(nm - negm1);
        const float inv = __builtin_amdgcn_rcpf(l * f0 + l1 * f1);
        const float c0 = f0 * inv, c1 = f1 * inv;
#pragma unroll
        for (int r = 0; r < 16; ++r) { o0[r] = o0[r] * c0 + s[r * 64] * c1; o1[r] = o1[r] * c0 + s[(16 + r) * 64] * c1; }
        if (qvalid) {
            const int b = bh >> 2, hd = bh & 3;
            u32x4* ao = ws + WS_AT + ((size_t)(b * TPB + qt) * 16 + 4 * hd) * 64 + lane;
            ao[0] = pack8(o0, 0); ao[64] = pack8(o0, 1); ao[128] = pack8(o1, 0); ao[192] = pack8(o1, 1);
        }
    }
}

__global__ __launch_bounds__(512, 2) void k_stageC(const u32x4* __restrict__ ws, const uint16_t* __restrict__ dw, uint16_t* __restrict__ out) {
    __shared__ u32x4 lds[32 * 64 + 64 * 64 + 256];
    u32x4* fx = lds;
    u32x4* fy = lds + 16 * 64;
    u32x4* fh = lds + 32 * 64;
    f32x2* stats = (f32x2*)(lds + 32 * 64 + 64 * 64);
    f32x2* stats2 = stats + 256;
    const int tile = blockIdx.x, tid = threadIdx.x, lane = tid & 63, h = lane >> 5, m = lane & 31;
    const int w = __builtin_amdgcn_readfirstlane(tid >> 6);
    const u32x4* wp = ws + WS_WC + (size_t)w * NFC * 64 + lane;
    u32x4 ring[RING];
#pragma unroll
    for (int i = 0; i < RING; ++i) ring[i] = wp[i * 64];

    const int tt = tile % TPB;
    const int hm = m & 3;
    int toff = hm < 2 ? hm - 2 : 30 + hm;
    const int tl = tt * 32 + toff;
    const bool hvalid = (tl >= 0) && (tl < T_SEQ);
    if (!hvalid) toff = 0;
    const int gtok = tile * 32 + toff;
    const size_t hbase = (size_t)(gtok >> 5) * 16 * 64 + h * 32 + (gtok & 31);

    f32x16 xv, yv;
    {
        const u32x4* at = ws + WS_AT;
        const u32x4* x1 = ws + WS_X1;
        const size_t mb = (size_t)tile * 16 * 64 + lane;
        fx[(2 * w) * 64 + lane] = at[mb + (2 * w) * 64];
        fx[(2 * w + 1) * 64 + lane] = at[mb + (2 * w + 1) * 64];
        fy[(2 * w) * 64 + lane] = at[hbase + (2 * w) * 64];
        fy[(2 * w + 1) * 64 + lane] = at[hbase + (2 * w + 1) * 64];
        unpack16(xv, x1[mb + (2 * w) * 64], x1[mb + (2 * w + 1) * 64]);
        unpack16(yv, x1[hbase + (2 * w) * 64], x1[hbase + (2 * w + 1) * 64]);
    }
    __syncthreads();
    {
        f32x16 a = (f32x16){}, b = (f32x16){};
        u32x4 bx[2], by[2];
        bx[0] = fx[lane]; by[0] = fy[lane]; bx[1] = fx[64 + lane]; by[1] = fy[64 + lane];
#pragma unroll
        for (int ks = 0; ks < 16; ++ks) {
            const int F = ks;
            a = MFMA(ring[F % RING], bx[ks & 1], a);
            b = MFMA(ring[F % RING], by[ks & 1], b);
            if (ks + 2 < 16) { bx[ks & 1] = fx[(ks + 2) * 64 + lane]; by[ks & 1] = fy[(ks + 2) * 64 + lane]; }
            RING_NEXT(F, NFC);
            SB();
        }
#pragma unroll
        for (int r = 0; r < 16; ++r) { xv[r] = rbf(xv[r] + rbf(a[r])); yv[r] = rbf(yv[r] + rbf(b[r])); }
    }
    float mean, rstd, mean2, rstd2;
    ln_stats2(xv, yv, stats, stats2, w, lane, mean, rstd, mean2, rstd2);
    norm_store(xv, mean, rstd, fx, w, lane);
    norm_store(yv, mean2, rstd2, fy, w, lane);
    __syncthreads();
    {
        f32x16 a = (f32x16){}, b = (f32x16){}, c = (f32x16){}, d = (f32x16){};
        u32x4 bxn = fx[lane], byn = fy[lane];
#pragma unroll
        for (int ks = 0; ks < 16; ++ks) {
            const int F = 16 + 2 * ks;
            const u32x4 bx = bxn, by = byn;
            if (ks + 1 < 16) { bxn = fx[(ks + 1) * 64 + lane]; byn = fy[(ks + 1) * 64 + lane]; }
            a = MFMA(ring[F % RING], bx, a); c = MFMA(ring[F % RING], by, c); RING_NEXT(F, NFC);
            b = MFMA(ring[(F + 1) % RING], bx, b); d = MFMA(ring[(F + 1) % RING], by, d); RING_NEXT(F + 1, NFC);
            SB();
        }
#pragma unroll
        for (int r = 0; r < 16; ++r) {
            a[r] = rbf(rbf(a[r]) * rbf(sigmoidf_(rbf(b[r]))));
            c[r] = hvalid ? rbf(rbf(c[r]) * rbf(sigmoidf_(rbf(d[r])))) : 0.f;
        }
        f32x4* cs = (f32x4*)fh + w * (4 * 2 * 36);
#pragma unroll
        for (int g = 0; g < 4; ++g) {
            cs[(g * 2 + h) * 36 + m + 2] = (f32x4){a[4 * g], a[4 * g + 1], a[4 * g + 2], a[4 * g + 3]};
            if (m < 4) cs[(g * 2 + h) * 36 + (m < 2 ? m : 32 + m)] = (f32x4){c[4 * g], c[4 * g + 1], c[4 * g + 2], c[4 * g + 3]};
        }
        __builtin_amdgcn_fence(__ATOMIC_RELEASE, "wavefront");
        __builtin_amdgcn_wave_barrier();
        __builtin_amdgcn_fence(__ATOMIC_ACQUIRE, "wavefront");
        const uint16_t* dp = dw + 32 * w + 4 * h;
#pragma unroll
        for (int g = 0; g < 4; ++g) {
            f32x4 acc = (f32x4){0.f, 0.f, 0.f, 0.f};
#pragma unroll
            for (int i = 0; i < 5; ++i) {
                const f32x4 hv = cs[(g * 2 + h) * 36 + m + i];
                const u32x2 t = *(const u32x2*)(dp + i * DM + 8 * g);
                acc[0] += hv[0] * bflo(t[0]); acc[1] += hv[1] * bfhi(t[0]); acc[2] += hv[2] * bflo(t[1]); acc[3] += hv[3] * bfhi(t[1]);
            }
#pragma unroll
            for (int i = 0; i < 4; ++i) a[4 * g + i] = siluf_(acc[i]);
        }
        __syncthreads();
        fx[(2 * w) * 64 + lane] = pack8(a, 0);
        fx[(2 * w + 1) * 64 + lane] = pack8(a, 1);
    }
    __syncthreads();
    {
        f32x16 a = (f32x16){};
        u32x4 bq[4];
#pragma unroll
        for (int i = 0; i < 4; ++i) bq[i] = fx[i * 64 + lane];
#pragma unroll
        for (int ks = 0; ks < 16; ++ks) {
            const int F = 48 + ks;
            a = MFMA(ring[F % RING], bq[ks & 3], a);
            if (ks + 4 < 16) bq[ks & 3] = fx[(ks + 4) * 64 + lane];
            RING_NEXT(F, NFC);
            SB();
        }
#pragma unroll
        for (int r = 0; r < 16; ++r) xv[r] = rbf(xv[r] + rbf(a[r]));
    }
    ln_stats(xv, stats, w, lane, mean, rstd);
    norm_store(xv, mean, rstd, fx, w, lane);
    __syncthreads();
    {
        f32x16 acc[4];
#pragma unroll
        for (int i = 0; i < 4; ++i) acc[i] = (f32x16){};
        u32x4 bn = fx[lane];
#pragma unroll
        for (int ks = 0; ks < 16; ++ks) {
            const u32x4 b = bn;
            if (ks + 1 < 16) bn = fx[(ks + 1) * 64 + lane];
#pragma unroll
            for (int i = 0; i < 4; ++i) {
                const int F = 64 + ks * 4 + i;
                acc[i] = MFMA(ring[F % RING], b, acc[i]);
                RING_NEXT(F, NFC);
            }
            SB();
        }
#pragma unroll
        for (int i = 0; i < 4; ++i) {
#pragma unroll
            for (int r = 0; r < 16; ++r) acc[i][r] = siluf_(acc[i][r]);
            fh[((4 * w + i) * 2 + 0) * 64 + lane] = pack8(acc[i], 0);
            fh[((4 * w + i) * 2 + 1) * 64 + lane] = pack8(acc[i], 1);
        }
    }
    __syncthreads();
    {
        f32x16 acc = (f32x16){};
        u32x4 bq[4];
#pragma unroll
        for (int i = 0; i < 4; ++i) bq[i] = fh[i * 64 + lane];
#pragma unroll
        for (int ks = 0; ks < 64; ++ks) {
            const int F = 128 + ks;
            acc = MFMA(ring[F % RING], bq[ks & 3], acc);
            if (ks + 4 < 64) bq[ks & 3] = fh[(ks + 4) * 64 + lane];
            RING_NEXT(F, NFC);
            SB();
        }
#pragma unroll
        for (int r = 0; r < 16; ++r) xv[r] = rbf(xv[r] + 0.5f * rbf(acc[r]));
    }
    ln_stats(xv, stats2, w, lane, mean, rstd);
    {
        uint16_t* op = out + (size_t)(tile * 32 + m) * DM + 32 * w + 4 * h;
#pragma unroll
        for (int g = 0; g < 4; ++g) {
            u32x2 t;
            t[0] = pack2((xv[4 * g] - mean) * rstd, (xv[4 * g + 1] - mean) * rstd);
            t[1] = pack2((xv[4 * g + 2] - mean) * rstd, (xv[4 * g + 3] - mean) * rstd);
            *(u32x2*)(op + 8 * g) = t;
        }
    }
}

extern "C" void kernel_launch(void* const* d_in, const int* in_sizes, int n_in,
                              void* d_out, int out_size, void* d_ws, size_t ws_size,
                              hipStream_t stream) {
    PackArgs p;
    p.w1a = (const uint16_t*)d_in[1]; p.w1b = (const uint16_t*)d_in[2]; p.wqkv = (const uint16_t*)d_in[3]; p.wo = (const uint16_t*)d_in[4];
    p.wp1 = (const uint16_t*)d_in[5]; p.wp2 = (const uint16_t*)d_in[7]; p.w2a = (const uint16_t*)d_in[8]; p.w2b = (const uint16_t*)d_in[9];
    p.ws = (u32x4*)d_ws;
    u32x4* ws = (u32x4*)d_ws;
    const int nchunk = 8 * (NFA + NFC) * 64;
    k_pack<<<(nchunk + 255) / 256, 256, 0, stream>>>(p);
    k_stageA<<<NTILE, 512, 0, stream>>>((const uint16_t*)d_in[0], ws);
    k_attn<<<256, 512, 0, stream>>>(ws, ws);
    k_stageC<<<NTILE, 512, 0, stream>>>(ws, (const uint16_t*)d_in[6], (uint16_t*)d_out);
}
```

```cpp
#include <hip/hip_runtime.h>
#include <hip/hip_bf16.h>
#include <stdint.h>

typedef short bf16x8 __attribute__((ext_vector_type(8)));
typedef float f32x16 __attribute__((ext_vector_type(16)));
typedef float f32x2 __attribute__((ext_vector_type(2)));
typedef float f32x4 __attribute__((ext_vector_type(4)));
typedef unsigned u32x4 __attribute__((ext_vector_type(4)));
typedef unsigned u32x2 __attribute__((ext_vector_type(2)));
typedef __bf16 bf16v2 __attribute__((ext_vector_type(2)));

#define DI __device__ __forceinline__
#define SB() __builtin_amdgcn_sched_barrier(0)
#define STAMPR(base, i) do {} while (0)
#define STAMP(base, i) do {} while (0)
#define MFMA(a, b, c) __builtin_amdgcn_mfma_f32_32x32x16_bf16(__builtin_bit_cast(bf16x8, (a)), __builtin_bit_cast(bf16x8, (b)), (c), 0, 0, 0)

constexpr int T_SEQ = 4000, DM = 256, NTOK = 8000, NTILE = 250, TPB = 125;
constexpr int NFA = 176;
constexpr int NFC = 192;
constexpr int RING = 16;

constexpr size_t WS_WA = 0;
constexpr size_t WS_WC = WS_WA + (size_t)8 * NFA * 64;
constexpr size_t WS_X1 = WS_WC + (size_t)8 * NFC * 64;
constexpr size_t WS_Q  = WS_X1 + (size_t)NTILE * 16 * 64;
constexpr size_t WS_KV = WS_Q + (size_t)8 * TPB * 4 * 64;
constexpr size_t WS_AT = WS_KV + (size_t)8 * TPB * 8 * 64 + 4096;

DI unsigned pack2(float lo, float hi) {
    f32x2 v = {lo, hi};
    bf16v2 r = __builtin_convertvector(v, bf16v2);
    return __builtin_bit_cast(unsigned, r);
}
DI float bflo(unsigned u) { return __uint_as_float(u << 16); }
DI float bfhi(unsigned u) { return __uint_as_float(u & 0xffff0000u); }
DI u32x4 pack8(const f32x16& a, int s) {
    u32x4 r;
    r[0] = pack2(a[8 * s + 0], a[8 * s + 1]);
    r[1] = pack2(a[8 * s + 2], a[8 * s + 3]);
    r[2] = pack2(a[8 * s + 4], a[8 * s + 5]);
    r[3] = pack2(a[8 * s + 6], a[8 * s + 7]);
    return r;
}
DI void unpack16(f32x16& a, const u32x4& f0, const u32x4& f1) {
#pragma unroll
    for (int i = 0; i < 4; ++i) {
        a[2 * i] = bflo(f0[i]); a[2 * i + 1] = bfhi(f0[i]);
        a[8 + 2 * i] = bflo(f1[i]); a[8 + 2 * i + 1] = bfhi(f1[i]);
    }
}
DI float xor32(float v) { return __shfl_xor(v, 32); }
DI float sigmoidf_(float x) { return __builtin_amdgcn_rcpf(1.0f + __builtin_amdgcn_exp2f(-1.4426950408889634f * x)); }
DI float siluf_(float x) { return x * sigmoidf_(x); }

struct PackArgs {
    const uint16_t* w1a; const uint16_t* w1b; const uint16_t* wqkv; const uint16_t* wo;
    const uint16_t* wp1; const uint16_t* wp2; const uint16_t* w2a; const uint16_t* w2b;
    u32x4* ws;
};

__global__ __launch_bounds__(256) void k_pack(PackArgs p) {
    const int gid = blockIdx.x * 256 + threadIdx.x;
    const int lane = gid & 63, fg = gid >> 6;
    if (fg >= 8 * (NFA + NFC)) return;
    const uint16_t* W; int N, nt, ks;
    if (fg < 8 * NFA) {
        const int w = fg / NFA, f = fg % NFA;
        if (f < 64)       { W = p.w1a; N = 1024; ks = f >> 2; nt = 4 * w + (f & 3); }
        else if (f < 128) { W = p.w1b; N = 256; ks = f - 64; nt = w; }
        else              { const int g = f - 128; W = p.wqkv; N = 768; ks = g / 3; nt = w + 8 * (g % 3); }
    } else {
        const int fg2 = fg - 8 * NFA, w = fg2 / NFC, f = fg2 % NFC;
        if (f < 16)       { W = p.wo; N = 256; ks = f; nt = w; }
        else if (f < 48)  { const int g = f - 16; W = p.wp1; N = 512; ks = g >> 1; nt = w + 8 * (g & 1); }
        else if (f < 64)  { W = p.wp2; N = 256; ks = f - 48; nt = w; }
        else if (f < 128) { const int g = f - 64; W = p.w2a; N = 1024; ks = g >> 2; nt = 4 * w + (g & 3); }
        else              { W = p.w2b; N = 256; ks = f - 128; nt = w; }
    }
    const int r = lane & 31, h = lane >> 5;
    const uint16_t* src = W + (size_t)(16 * ks + 4 * h) * N + 32 * nt + r;
    unsigned e[8];
#pragma unroll
    for (int j = 0; j < 8; ++j) e[j] = src[(size_t)(8 * (j >> 2) + (j & 3)) * N];
    u32x4 o;
#pragma unroll
    for (int i = 0; i < 4; ++i) o[i] = e[2 * i] | (e[2 * i + 1] << 16);
    p.ws[gid] = o;
}

#define RING_NEXT(F, NF) do { if ((F) + RING < (NF)) ring[(F) % RING] = wp[((F) + RING) * 64]; } while (0)

DI void ln_stats(const f32x16& v, f32x2* stats, int w, int lane, float& mean, float& rstd) {
    float s = 0.f;
#pragma unroll
    for (int i = 0; i < 16; ++i) s += v[i];
    s += xor32(s);
    const float mw = s * (1.0f / 32.0f);
    float q = 0.f;
#pragma unroll
    for (int i = 0; i < 16; ++i) { const float d = v[i] - mw; q += d * d; }
    q += xor32(q);
    if (lane < 32) stats[lane * 8 + w] = (f32x2){mw, q};
    __syncthreads();
    const f32x4* sp = (const f32x4*)(stats + (lane & 31) * 8);
    f32x4 a0 = sp[0], a1 = sp[1], a2 = sp[2], a3 = sp[3];
    const float m = ((a0[0] + a0[2]) + (a1[0] + a1[2]) + (a2[0] + a2[2]) + (a3[0] + a3[2])) * 0.125f;
    float M2 = (a0[1] + a0[3]) + (a1[1] + a1[3]) + (a2[1] + a2[3]) + (a3[1] + a3[3]);
    float dd = 0.f, d;
    d = a0[0] - m; dd += d * d; d = a0[2] - m; dd += d * d;
    d = a1[0] - m; dd += d * d; d = a1[2] - m; dd += d * d;
    d = a2[0] - m; dd += d * d; d = a2[2] - m; dd += d * d;
    d = a3[0] - m; dd += d * d; d = a3[2] - m; dd += d * d;
    M2 += 32.0f * dd;
    mean = m;
    rstd = __builtin_amdgcn_rsqf(M2 * (1.0f / 256.0f) + 1e-5f);
}

DI void ln_stats2(const f32x16& v, const f32x16& u, f32x2* stats, f32x2* stats2, int w, int lane,
                  float& mean, float& rstd, float& mean2, float& rstd2) {
    float s = 0.f, s2 = 0.f;
#pragma unroll
    for (int i = 0; i < 16; ++i) { s += v[i]; s2 += u[i]; }
    s += xor32(s); s2 += xor32(s2);
    const float mw = s * (1.0f / 32.0f), mw2 = s2 * (1.0f / 32.0f);
    float q = 0.f, q2 = 0.f;
#pragma unroll
    for (int i = 0; i < 16; ++i) { const float d = v[i] - mw; q += d * d; const float e = u[i] - mw2; q2 += e * e; }
    q += xor32(q); q2 += xor32(q2);
    if (lane < 32) { stats[lane * 8 + w] = (f32x2){mw, q}; stats2[lane * 8 + w] = (f32x2){mw2, q2}; }
    __syncthreads();
#pragma unroll
    for (int t = 0; t < 2; ++t) {
        const f32x4* sp = (const f32x4*)((t ? stats2 : stats) + (lane & 31) * 8);
        f32x4 a0 = sp[0], a1 = sp[1], a2 = sp[2], a3 = sp[3];
        const float m = ((a0[0] + a0[2]) + (a1[0] + a1[2]) + (a2[0] + a2[2]) + (a3[0] + a3[2])) * 0.125f;
        float M2 = (a0[1] + a0[3]) + (a1[1] + a1[3]) + (a2[1] + a2[3]) + (a3[1] + a3[3]);
        float dd = 0.f, d;
        d = a0[0] - m; dd += d * d; d = a0[2] - m; dd += d * d;
        d = a1[0] - m; dd += d * d; d = a1[2] - m; dd += d * d;
        d = a2[0] - m; dd += d * d; d = a2[2] - m; dd += d * d;
        d = a3[0] - m; dd += d * d; d = a3[2] - m; dd += d * d;
        M2 += 32.0f * dd;
        const float r = __builtin_amdgcn_rsqf(M2 * (1.0f / 256.0f) + 1e-5f);
        if (t) { mean2 = m; rstd2 = r; } else { mean = m; rstd = r; }
    }
}

DI void norm_store(const f32x16& v, float mean, float rstd, u32x4* dst  , int w, int lane) {
    f32x16 n;
#pragma unroll
    for (int i = 0; i < 16; ++i) n[i] = (v[i] - mean) * rstd;
    dst[(2 * w) * 64 + lane] = pack8(n, 0);
    dst[(2 * w + 1) * 64 + lane] = pack8(n, 1);
}

DI float rbf(float x) { return bflo(pack2(x, 0.f) & 0xffffu); }

__global__ __launch_bounds__(512, 2) void k_stageA(const uint16_t* __restrict__ x, u32x4* __restrict__ ws) {
    __shared__ u32x4 lds[16 * 64 + 64 * 64 + 128];
    u32x4* fx = lds;
    u32x4* fh = lds + 16 * 64;
    f32x2* stats = (f32x2*)(lds + 16 * 64 + 64 * 64);
    const int tile = blockIdx.x, tid = threadIdx.x, lane = tid & 63, h = lane >> 5, m = lane & 31;
    const int w = __builtin_amdgcn_readfirstlane(tid >> 6);
    STAMPR((ws + (8u<<20)), 14); STAMP((ws + (8u<<20)), 0);
    const u32x4* wp = ws + WS_WA + (size_t)w * NFA * 64 + lane;
    u32x4 ring[RING];
#pragma unroll
    for (int i = 0; i < RING; ++i) ring[i] = wp[i * 64];

    f32x16 xv;
    {
        const uint16_t* xp = x + (size_t)(tile * 32 + m) * DM + 32 * w + 4 * h;
#pragma unroll
        for (int g = 0; g < 4; ++g) {
            const u32x2 t = *(const u32x2*)(xp + 8 * g);
            xv[4 * g + 0] = bflo(t[0]); xv[4 * g + 1] = bfhi(t[0]); xv[4 * g + 2] = bflo(t[1]); xv[4 * g + 3] = bfhi(t[1]);
        }
    }
    float mean, rstd;
    ln_stats(xv, stats, w, lane, mean, rstd);
    norm_store(xv, mean, rstd, fx, w, lane);
    __syncthreads();

    STAMP((ws + (8u<<20)), 1);
    {
        f32x16 acc[4];
#pragma unroll
        for (int i = 0; i < 4; ++i) acc[i] = (f32x16){};
        u32x4 bn = fx[lane];
#pragma unroll
        for (int ks = 0; ks < 16; ++ks) {
            const u32x4 b = bn;
            if (ks + 1 < 16) bn = fx[(ks + 1) * 64 + lane];
#pragma unroll
            for (int i = 0; i < 4; ++i) {
                const int F = ks * 4 + i;
                acc[i] = MFMA(ring[F % RING], b, acc[i]);
                RING_NEXT(F, NFA);
            }
            SB();
        }
        STAMP((ws + (8u<<20)), 2);
#pragma unroll
        for (int i = 0; i < 4; ++i) {
#pragma unroll
            for (int r = 0; r < 16; ++r) acc[i][r] = siluf_(acc[i][r]);
            fh[((4 * w + i) * 2 + 0) * 64 + lane] = pack8(acc[i], 0);
            fh[((4 * w + i) * 2 + 1) * 64 + lane] = pack8(acc[i], 1);
        }
    }
    __syncthreads();
    STAMP((ws + (8u<<20)), 3);
    {
        f32x16 acc = (f32x16){};
        u32x4 bq[4];
#pragma unroll
        for (int i = 0; i < 4; ++i) bq[i] = fh[i * 64 + lane];
#pragma unroll
        for (int ks = 0; ks < 64; ++ks) {
            const int F = 64 + ks;
            acc = MFMA(ring[F % RING], bq[ks & 3], acc);
            if (ks + 4 < 64) bq[ks & 3] = fh[(ks + 4) * 64 + lane];
            RING_NEXT(F, NFA);
            SB();
        }
        STAMP((ws + (8u<<20)), 4);
#pragma unroll
        for (int r = 0; r < 16; ++r) xv[r] = rbf(xv[r] + 0.5f * acc[r]);
    }
    {
        u32x4* xo = ws + WS_X1 + (size_t)tile * 16 * 64;
        xo[(2 * w) * 64 + lane] = pack8(xv, 0);
        xo[(2 * w + 1) * 64 + lane] = pack8(xv, 1);
    }
    ln_stats(xv, stats, w, lane, mean, rstd);
    norm_store(xv, mean, rstd, fx, w, lane);
    __syncthreads();
    STAMP((ws + (8u<<20)), 5);
    {
        f32x16 aq = (f32x16){}, ak = (f32x16){}, av = (f32x16){};
        u32x4 bn = fx[lane];
#pragma unroll
        for (int ks = 0; ks < 16; ++ks) {
            const u32x4 b = bn;
            if (ks + 1 < 16) bn = fx[(ks + 1) * 64 + lane];
            const int F = 128 + ks * 3;
            aq = MFMA(ring[F % RING], b, aq); RING_NEXT(F, NFA);
            ak = MFMA(ring[(F + 1) % RING], b, ak); RING_NEXT(F + 1, NFA);
            av = MFMA(b, ring[(F + 2) % RING], av); RING_NEXT(F + 2, NFA);
            SB();
        }
        STAMP((ws + (8u<<20)), 6);
        const float qs = 0.125f * 1.4426950408889634f;
#pragma unroll
        for (int r = 0; r < 16; ++r) aq[r] *= qs;
        const int b = tile / TPB, tt = tile % TPB, bh = b * 4 + (w >> 1), t2 = w & 1;
        u32x4* qo = ws + WS_Q + ((size_t)(bh * TPB + tt) * 4 + 2 * t2) * 64 + lane;
        qo[0] = pack8(aq, 0); qo[64] = pack8(aq, 1);
        u32x4* ko = ws + WS_KV + ((size_t)(bh * TPB + tt) * 8 + 2 * t2) * 64 + lane;
        ko[0] = pack8(ak, 0); ko[64] = pack8(ak, 1);
        ko[4 * 64] = pack8(av, 0); ko[5 * 64] = pack8(av, 1);
    }
    STAMP((ws + (8u<<20)), 7); STAMPR((ws + (8u<<20)), 15);
}

DI void glds16(const void* g, unsigned lds_base) {
    unsigned sv;
    asm volatile("s_mov_b32 %0, m0\n\ts_mov_b32 m0, %2\n\ts_nop 0\n\tglobal_load_lds_dwordx4 %1, off\n\ts_mov_b32 m0, %0"
                 : "=&s"(sv) : "v"(g), "s"(lds_base) : "memory");
}
#define WAIT_BAR(N) asm volatile("s_waitcnt vmcnt(" #N ") lgkmcnt(0)\n\ts_barrier" ::: "memory")

constexpr int AT_SLOT = 1024;
constexpr float AT_BIG = 1.0e30f;
#define PIN(x) asm volatile("" : "+v"(x))
DI float vadd(float a, float b) { float r; asm("v_add_f32 %0, %1, %2" : "=v"(r) : "v"(a), "v"(b)); return r; }
#define EX2(x) __builtin_amdgcn_exp2f(x)

__global__ __launch_bounds__(512, 2) void k_attn(const u32x4* __restrict__ ws_in, u32x4* __restrict__ ws) {
    __shared__ u32x4 lds[6 * AT_SLOT];
    const int tid = threadIdx.x, lane = tid & 63;
    const int w = __builtin_amdgcn_readfirstlane(tid >> 6);
    const int bh = blockIdx.x & 7, qb = blockIdx.x >> 3, qi = w & 3, g = w >> 2;
    int qt = 4 * qb + qi; const bool qvalid = qt < TPB; if (!qvalid) qt = TPB - 1;
    const u32x4* kv = ws_in + WS_KV + (size_t)bh * TPB * 8 * 64;
    const unsigned lds0 = (unsigned)(uintptr_t)lds;
    const u32x4* src = kv + (w >> 1) * 512 + (w & 1) * 128 + lane;
    const unsigned dstk = lds0 + ((w >> 1) * 256 + (w & 1) * 128) * 16, dstv = dstk + 3 * AT_SLOT * 16;
#define DMA_K(step, slotoff) do { const u32x4* s_ = src + (size_t)(step) * 2048; \
        glds16(s_, (unsigned)__builtin_amdgcn_readfirstlane(dstk + (slotoff))); glds16(s_ + 64, (unsigned)__builtin_amdgcn_readfirstlane(dstk + (slotoff) + 1024)); } while (0)
#define DMA_V(step, slotoff) do { const u32x4* s_ = src + (size_t)(step) * 2048 + 256; \
        glds16(s_, (unsigned)__builtin_amdgcn_readfirstlane(dstv + (slotoff))); glds16(s_ + 64, (unsigned)__builtin_amdgcn_readfirstlane(dstv + (slotoff) + 1024)); } while (0)
    constexpr int SLB = AT_SLOT * 16;
    DMA_K(0, 0); DMA_V(0, 0); DMA_K(1, SLB); DMA_K(2, 2 * SLB); DMA_V(1, SLB);
    u32x4 q[4];
    {
        const u32x4* qp = ws_in + WS_Q + (size_t)(bh * TPB + qt) * 4 * 64 + lane;
#pragma unroll
        for (int f = 0; f < 4; ++f) q[f] = qp[f * 64];
    }
    const char* kbase = (const char*)lds + ((2 * g) * 256 + lane) * 16;
    const char* vbase = kbase + 3 * SLB;
#define KF(off, i) (*(const u32x4*)(kbase + (off) + ((i) >> 2) * 4096 + ((i) & 3) * 1024))
#define VF(off, i) (*(const u32x4*)(vbase + (off) + ((i) >> 2) * 4096 + ((i) & 3) * 1024))
    f32x16 o0 = (f32x16){}, o1 = (f32x16){};
    float l = 0.f, negm;
    f32x16 cm, sA0, sA1, sB0, sB1;
    u32x4 kf[8], vf[8], pk0, pk1, pk2, pk3;
    WAIT_BAR(0);
    {
#pragma unroll
        for (int i = 0; i < 8; ++i) kf[i] = KF(0, i);
        const f32x16 z = (f32x16){};
        sA0 = MFMA(kf[0], q[0], z); sA1 = MFMA(kf[4], q[0], z);
        sA0 = MFMA(kf[1], q[1], sA0); sA1 = MFMA(kf[5], q[1], sA1);
        sA0 = MFMA(kf[2], q[2], sA0); sA1 = MFMA(kf[6], q[2], sA1);
        sA0 = MFMA(kf[3], q[3], sA0); sA1 = MFMA(kf[7], q[3], sA1);
        float mx = sA0[0];
#pragma unroll
        for (int r = 0; r < 16; ++r) { mx = fmaxf(mx, sA0[r]); mx = fmaxf(mx, sA1[r]); }
        mx = fmaxf(mx, xor32(mx));
        negm = -mx;
#pragma unroll
        for (int r = 0; r < 16; ++r) { sA0[r] = EX2(sA0[r] + negm); sA1[r] = EX2(sA1[r] + negm); cm[r] = negm; }
#pragma unroll
        for (int i = 0; i < 8; ++i) kf[i] = KF(SLB, i);
    }
    WAIT_BAR(0);
    DMA_K(3, 0);
    int s_prev = 0, s_cur = SLB, s_next = 2 * SLB;
#define ROT() do { const int t_ = s_prev; s_prev = s_cur; s_cur = s_next; s_next = t_; } while (0)
#define GA(MF, VI, P, i0, PK, e0) do { if (VI >= 0) { vf[VI < 0 ? 0 : VI] = VF(s_prev, VI < 0 ? 0 : VI); SB(); } MF; \
        su0 = vadd(su0, P[i0]); su1 = vadd(su1, P[i0 + 1]); su0 = vadd(su0, P[i0 + 2]); su1 = vadd(su1, P[i0 + 3]); \
        PK[e0] = pack2(P[i0], P[i0 + 1]); PK[e0 + 1] = pack2(P[i0 + 2], P[i0 + 3]); PIN(PK); PIN(su0); PIN(su1); SB(); } while (0)
#define GB(MF, KI, VI, C, i0) do { kf[KI] = KF(s_next, KI); if (VI >= 0) vf[VI < 0 ? 0 : VI] = VF(s_prev, VI < 0 ? 0 : VI); SB(); MF; \
        C[i0] = EX2(C[i0]); C[i0 + 1] = EX2(C[i0 + 1]); C[i0 + 2] = EX2(C[i0 + 2]); C[i0 + 3] = EX2(C[i0 + 3]); PIN(C); SB(); } while (0)
#define STEP(C0, C1, P0, P1, t) do { \
        DMA_K(((t) + 3 < 32 ? (t) + 3 : 31), s_cur); DMA_V(((t) + 1 < 32 ? (t) + 1 : 31), s_next); \
        float su0 = 0.f, su1 = 0.f; SB(); \
        GA(C0 = MFMA(kf[0], q[0], cm), -1, P0, 0,  pk0, 0); \
        GA(C1 = MFMA(kf[4], q[0], cm), -1, P0, 4,  pk0, 2); \
        GA(C0 = MFMA(kf[1], q[1], C0), -1, P0, 8,  pk1, 0); \
        GA(C1 = MFMA(kf[5], q[1], C1), -1, P0, 12, pk1, 2); \
        GA(C0 = MFMA(kf[2], q[2], C0), -1, P1, 0,  pk2, 0); \
        GA(C1 = MFMA(kf[6], q[2], C1), -1, P1, 4,  pk2, 2); \
        GA(C0 = MFMA(kf[3], q[3], C0), 0, P1, 8,  pk3, 0); \
        GA(C1 = MFMA(kf[7], q[3], C1), 2, P1, 12, pk3, 2); \
        l += su0 + su1; \
        if ((t) == 31) { _Pragma("unroll") for (int r_ = 0; r_ < 16; ++r_) { C1[r_] = -INFINITY; if (g == 1) C0[r_] = -INFINITY; } } \
        SB(); \
        GB(o0 = MFMA(vf[0], pk0, o0), 0, 1, C0, 0); \
        GB(o1 = MFMA(vf[2], pk0, o1), 4, 3, C0, 4); \
        GB(o0 = MFMA(vf[1], pk1, o0), 1, 4, C0, 8); \
        GB(o1 = MFMA(vf[3], pk1, o1), 5, 6, C0, 12); \
        GB(o0 = MFMA(vf[4], pk2, o0), 2, 5, C1, 0); \
        GB(o1 = MFMA(vf[6], pk2, o1), 6, 7, C1, 4); \
        GB(o0 = MFMA(vf[5], pk3, o0), 3, -1, C1, 8); \
        GB(o1 = MFMA(vf[7], pk3, o1), 7, -1, C1, 12); \
        WAIT_BAR(4); ROT(); \
    } while (0)
    int t = 1;
    for (; t < 31; t += 2) {
        STEP(sB0, sB1, sA0, sA1, t);
        STEP(sA0, sA1, sB0, sB1, t + 1);
    }
    STEP(sB0, sB1, sA0, sA1, 31);
    {
        float sum = 0.f;
#pragma unroll
        for (int r = 0; r < 16; ++r) sum += sB0[r] + sB1[r];
        pk0 = pack8(sB0, 0); pk1 = pack8(sB0, 1);
        l += sum;
        if (g == 0) {
#pragma unroll
            for (int i = 0; i < 4; ++i) vf[i] = VF(s_prev, i);
            o0 = MFMA(vf[0], pk0, o0); o1 = MFMA(vf[2], pk0, o1);
            o0 = MFMA(vf[1], pk1, o0); o1 = MFMA(vf[3], pk1, o1);
        }
    }
    WAIT_BAR(0);
#undef DMA_K
#undef DMA_V
#undef STEP
#undef GA
#undef GB
#undef ROT
#undef KF
#undef VF
    {
        bool bad = !(l < AT_BIG);
#pragma unroll
        for (int r = 0; r < 16; ++r) bad = bad || !(fabsf(o0[r]) < INFINITY) || !(fabsf(o1[r]) < INFINITY);
        if (__builtin_expect(__any(bad), 0)) {
            o0 = (f32x16){}; o1 = (f32x16){}; l = 0.f; float mrun = -INFINITY;
            for (int kt = 0; kt < TPB; ++kt) {
                if (((kt >> 1) & 1) != g) continue;
                const u32x4* kg = kv + (size_t)kt * 512 + lane;
                f32x16 sc = (f32x16){};
#pragma unroll
                for (int f = 0; f < 4; ++f) sc = MFMA(kg[f * 64], q[f], sc);
                float mx = sc[0];
#pragma unroll
                for (int r = 1; r < 16; ++r) mx = fmaxf(mx, sc[r]);
                mx = fmaxf(mx, xor32(mx));
                const float mn = fmaxf(mrun, mx), f = EX2(mrun - mn);
                mrun = mn; l *= f;
#pragma unroll
                for (int r = 0; r < 16; ++r) { o0[r] *= f; o1[r] *= f; sc[r] = EX2(sc[r] - mn); l += sc[r]; }
                const u32x4 p0 = pack8(sc, 0), p1 = pack8(sc, 1);
                o0 = MFMA(kg[256], p0, o0); o1 = MFMA(kg[256 + 128], p0, o1);
                o0 = MFMA(kg[256 + 64], p1, o0); o1 = MFMA(kg[256 + 192], p1, o1);
            }
            negm = -mrun;
        }
    }
    l += xor32(l);
    float* cf = (float*)lds;
    if (g == 1) {
        float* dst = cf + qi * 34 * 64 + lane;
#pragma unroll
        for (int r = 0; r < 16; ++r) { dst[r * 64] = o0[r]; dst[(16 + r) * 64] = o1[r]; }
        dst[32 * 64] = negm; dst[33 * 64] = l;
    }
    __syncthreads();
    if (g == 0) {
        const float* s = cf + qi * 34 * 64 + lane;
        const float negm1 = s[32 * 64], l1 = s[33 * 64];
        const float nm = fminf(negm, negm1);
        const float f0 = __builtin_amdgcn_exp2f(nm - negm), f1 = __builtin_amdgcn_exp2f(nm - negm1);
        const float inv = __builtin_amdgcn_rcpf(l * f0 + l1 * f1);
        const float c0 = f0 * inv, c1 = f1 * inv;
#pragma unroll
        for (int r = 0; r < 16; ++r) { o0[r] = o0[r] * c0 + s[r * 64] * c1; o1[r] = o1[r] * c0 + s[(16 + r) * 64] * c1; }
        if (qvalid) {
            const int b = bh >> 2, hd = bh & 3;
            u32x4* ao = ws + WS_AT + ((size_t)(b * TPB + qt) * 16 + 4 * hd) * 64 + lane;
            ao[0] = pack8(o0, 0); ao[64] = pack8(o0, 1); ao[128] = pack8(o1, 0); ao[192] = pack8(o1, 1);
        }
    }
}

__global__ __launch_bounds__(512, 2) void k_stageC(const u32x4* __restrict__ ws, const uint16_t* __restrict__ dw, uint16_t* __restrict__ out) {
    __shared__ u32x4 lds[32 * 64 + 64 * 64 + 256];
    u32x4* fx = lds;
    u32x4* fy = lds + 16 * 64;
    u32x4* fh = lds + 32 * 64;
    f32x2* stats = (f32x2*)(lds + 32 * 64 + 64 * 64);
    f32x2* stats2 = stats + 256;
    const int tile = blockIdx.x, tid = threadIdx.x, lane = tid & 63, h = lane >> 5, m = lane & 31;
    const int w = __builtin_amdgcn_readfirstlane(tid >> 6);
    STAMPR(((u32x4*)ws + (9u<<20)), 14); STAMP(((u32x4*)ws + (9u<<20)), 0);
    const u32x4* wp = ws + WS_WC + (size_t)w * NFC * 64 + lane;
    u32x4 ring[RING];
#pragma unroll
    for (int i = 0; i < RING; ++i) ring[i] = wp[i * 64];

    const int tt = tile % TPB;
    const int hm = m & 3;
    int toff = hm < 2 ? hm - 2 : 30 + hm;
    const int tl = tt * 32 + toff;
    const bool hvalid = (tl >= 0) && (tl < T_SEQ);
    if (!hvalid) toff = 0;
    const int gtok = tile * 32 + toff;
    const size_t hbase = (size_t)(gtok >> 5) * 16 * 64 + h * 32 + (gtok & 31);

    f32x16 xv, yv;
    {
        const u32x4* at = ws + WS_AT;
        const u32x4* x1 = ws + WS_X1;
        const size_t mb = (size_t)tile * 16 * 64 + lane;
        fx[(2 * w) * 64 + lane] = at[mb + (2 * w) * 64];
        fx[(2 * w + 1) * 64 + lane] = at[mb + (2 * w + 1) * 64];
        fy[(2 * w) * 64 + lane] = at[hbase + (2 * w) * 64];
        fy[(2 * w + 1) * 64 + lane] = at[hbase + (2 * w + 1) * 64];
        unpack16(xv, x1[mb + (2 * w) * 64], x1[mb + (2 * w + 1) * 64]);
        unpack16(yv, x1[hbase + (2 * w) * 64], x1[hbase + (2 * w + 1) * 64]);
    }
    __syncthreads();
    STAMP(((u32x4*)ws + (9u<<20)), 1);
    {
        f32x16 a = (f32x16){}, b = (f32x16){};
        u32x4 bx[2], by[2];
        bx[0] = fx[lane]; by[0] = fy[lane]; bx[1] = fx[64 + lane]; by[1] = fy[64 + lane];
#pragma unroll
        for (int ks = 0; ks < 16; ++ks) {
            const int F = ks;
            a = MFMA(ring[F % RING], bx[ks & 1], a);
            b = MFMA(ring[F % RING], by[ks & 1], b);
            if (ks + 2 < 16) { bx[ks & 1] = fx[(ks + 2) * 64 + lane]; by[ks & 1] = fy[(ks + 2) * 64 + lane]; }
            RING_NEXT(F, NFC);
            SB();
        }
        STAMP(((u32x4*)ws + (9u<<20)), 2);
#pragma unroll
        for (int r = 0; r < 16; ++r) { xv[r] = rbf(xv[r] + rbf(a[r])); yv[r] = rbf(yv[r] + rbf(b[r])); }
    }
    float mean, rstd, mean2, rstd2;
    ln_stats2(xv, yv, stats, stats2, w, lane, mean, rstd, mean2, rstd2);
    norm_store(xv, mean, rstd, fx, w, lane);
    norm_store(yv, mean2, rstd2, fy, w, lane);
    __syncthreads();
    STAMP(((u32x4*)ws + (9u<<20)), 3);
    {
        f32x16 a = (f32x16){}, b = (f32x16){}, c = (f32x16){}, d = (f32x16){};
        u32x4 bxn = fx[lane], byn = fy[lane];
#pragma unroll
        for (int ks = 0; ks < 16; ++ks) {
            const int F = 16 + 2 * ks;
            const u32x4 bx = bxn, by = byn;
            if (ks + 1 < 16) { bxn = fx[(ks + 1) * 64 + lane]; byn = fy[(ks + 1) * 64 + lane]; }
            a = MFMA(ring[F % RING], bx, a); c = MFMA(ring[F % RING], by, c); RING_NEXT(F, NFC);
            b = MFMA(ring[(F + 1) % RING], bx, b); d = MFMA(ring[(F + 1) % RING], by, d); RING_NEXT(F + 1, NFC);
            SB();
        }
        STAMP(((u32x4*)ws + (9u<<20)), 4);
#pragma unroll
        for (int r = 0; r < 16; ++r) {
            a[r] = rbf(rbf(a[r]) * rbf(sigmoidf_(rbf(b[r]))));
            c[r] = hvalid ? rbf(rbf(c[r]) * rbf(sigmoidf_(rbf(d[r])))) : 0.f;
        }
        f32x4* cs = (f32x4*)fh + w * (4 * 2 * 36);
#pragma unroll
        for (int g = 0; g < 4; ++g) {
            cs[(g * 2 + h) * 36 + m + 2] = (f32x4){a[4 * g], a[4 * g + 1], a[4 * g + 2], a[4 * g + 3]};
            if (m < 4) cs[(g * 2 + h) * 36 + (m < 2 ? m : 32 + m)] = (f32x4){c[4 * g], c[4 * g + 1], c[4 * g + 2], c[4 * g + 3]};
        }
        __builtin_amdgcn_fence(__ATOMIC_RELEASE, "wavefront");
        __builtin_amdgcn_wave_barrier();
        __builtin_amdgcn_fence(__ATOMIC_ACQUIRE, "wavefront");
        const uint16_t* dp = dw + 32 * w + 4 * h;
#pragma unroll
        for (int g = 0; g < 4; ++g) {
            f32x4 acc = (f32x4){0.f, 0.f, 0.f, 0.f};
#pragma unroll
            for (int i = 0; i < 5; ++i) {
                const f32x4 hv = cs[(g * 2 + h) * 36 + m + i];
                const u32x2 t = *(const u32x2*)(dp + i * DM + 8 * g);
                acc[0] += hv[0] * bflo(t[0]); acc[1] += hv[1] * bfhi(t[0]); acc[2] += hv[2] * bflo(t[1]); acc[3] += hv[3] * bfhi(t[1]);
            }
#pragma unroll
            for (int i = 0; i < 4; ++i) a[4 * g + i] = siluf_(acc[i]);
        }
        __syncthreads();
        fx[(2 * w) * 64 + lane] = pack8(a, 0);
        fx[(2 * w + 1) * 64 + lane] = pack8(a, 1);
    }
    __syncthreads();
    STAMP(((u32x4*)ws + (9u<<20)), 5);
    {
        f32x16 a = (f32x16){};
        u32x4 bq[4];
#pragma unroll
        for (int i = 0; i < 4; ++i) bq[i] = fx[i * 64 + lane];
#pragma unroll
        for (int ks = 0; ks < 16; ++ks) {
            const int F = 48 + ks;
            a = MFMA(ring[F % RING], bq[ks & 3], a);
            if (ks + 4 < 16) bq[ks & 3] = fx[(ks + 4) * 64 + lane];
            RING_NEXT(F, NFC);
            SB();
        }
        STAMP(((u32x4*)ws + (9u<<20)), 6);
#pragma unroll
        for (int r = 0; r < 16; ++r) xv[r] = rbf(xv[r] + rbf(a[r]));
    }
    ln_stats(xv, stats, w, lane, mean, rstd);
    norm_store(xv, mean, rstd, fx, w, lane);
    __syncthreads();
    STAMP(((u32x4*)ws + (9u<<20)), 7);
    {
        f32x16 acc[4];
#pragma unroll
        for (int i = 0; i < 4; ++i) acc[i] = (f32x16){};
        u32x4 bn = fx[lane];
#pragma unroll
        for (int ks = 0; ks < 16; ++ks) {
            const u32x4 b = bn;
            if (ks + 1 < 16) bn = fx[(ks + 1) * 64 + lane];
#pragma unroll
            for (int i = 0; i < 4; ++i) {
                const int F = 64 + ks * 4 + i;
                acc[i] = MFMA(ring[F % RING], b, acc[i]);
                RING_NEXT(F, NFC);
            }
            SB();
        }
        STAMP(((u32x4*)ws + (9u<<20)), 8);
#pragma unroll
        for (int i = 0; i < 4; ++i) {
#pragma unroll
            for (int r = 0; r < 16; ++r) acc[i][r] = siluf_(acc[i][r]);
            fh[((4 * w + i) * 2 + 0) * 64 + lane] = pack8(acc[i], 0);
            fh[((4 * w + i) * 2 + 1) * 64 + lane] = pack8(acc[i], 1);
        }
    }
    __syncthreads();
    STAMP(((u32x4*)ws + (9u<<20)), 9);
    {
        f32x16 acc = (f32x16){};
        u32x4 bq[4];
#pragma unroll
        for (int i = 0; i < 4; ++i) bq[i] = fh[i * 64 + lane];
#pragma unroll
        for (int ks = 0; ks < 64; ++ks) {
            const int F = 128 + ks;
            acc = MFMA(ring[F % RING], bq[ks & 3], acc);
            if (ks + 4 < 64) bq[ks & 3] = fh[(ks + 4) * 64 + lane];
            RING_NEXT(F, NFC);
            SB();
        }
        STAMP(((u32x4*)ws + (9u<<20)), 10);
#pragma unroll
        for (int r = 0; r < 16; ++r) xv[r] = rbf(xv[r] + 0.5f * rbf(acc[r]));
    }
    ln_stats(xv, stats2, w, lane, mean, rstd);
    {
        uint16_t* op = out + (size_t)(tile * 32 + m) * DM + 32 * w + 4 * h;
#pragma unroll
        for (int g = 0; g < 4; ++g) {
            u32x2 t;
            t[0] = pack2((xv[4 * g] - mean) * rstd, (xv[4 * g + 1] - mean) * rstd);
            t[1] = pack2((xv[4 * g + 2] - mean) * rstd, (xv[4 * g + 3] - mean) * rstd);
            *(u32x2*)(op + 8 * g) = t;
        }
    }
    STAMP(((u32x4*)ws + (9u<<20)), 11); STAMPR(((u32x4*)ws + (9u<<20)), 15);
}

extern "C" void kernel_launch(void* const* d_in, const int* in_sizes, int n_in,
                              void* d_out, int out_size, void* d_ws, size_t ws_size,
                              hipStream_t stream) {
    PackArgs p;
    p.w1a = (const uint16_t*)d_in[1]; p.w1b = (const uint16_t*)d_in[2]; p.wqkv = (const uint16_t*)d_in[3]; p.wo = (const uint16_t*)d_in[4];
    p.wp1 = (const uint16_t*)d_in[5]; p.wp2 = (const uint16_t*)d_in[7]; p.w2a = (const uint16_t*)d_in[8]; p.w2b = (const uint16_t*)d_in[9];
    p.ws = (u32x4*)d_ws;
    u32x4* ws = (u32x4*)d_ws;
    const int nchunk = 8 * (NFA + NFC) * 64;
    k_pack<<<(nchunk + 255) / 256, 256, 0, stream>>>(p);
    k_stageA<<<NTILE, 512, 0, stream>>>((const uint16_t*)d_in[0], ws);
    k_attn<<<256, 512, 0, stream>>>(ws, ws);
    k_stageC<<<NTILE, 512, 0, stream>>>(ws, (const uint16_t*)d_in[6], (uint16_t*)d_out);
}
```

```cpp
#include <hip/hip_runtime.h>
#include <hip/hip_bf16.h>
#include <stdint.h>

typedef short bf16x8 __attribute__((ext_vector_type(8)));
typedef float f32x16 __attribute__((ext_vector_type(16)));
typedef float f32x2 __attribute__((ext_vector_type(2)));
typedef float f32x4 __attribute__((ext_vector_type(4)));
typedef unsigned u32x4 __attribute__((ext_vector_type(4)));
typedef unsigned u32x2 __attribute__((ext_vector_type(2)));
typedef __bf16 bf16v2 __attribute__((ext_vector_type(2)));

#define DI __device__ __forceinline__
#define SB() __builtin_amdgcn_sched_barrier(0)
#define STAMPR(base, i) do {} while (0)
#define STAMP(base, i) do {} while (0)
#define MFMA(a, b, c) __builtin_amdgcn_mfma_f32_32x32x16_bf16(__builtin_bit_cast(bf16x8, (a)), __builtin_bit_cast(bf16x8, (b)), (c), 0, 0, 0)

constexpr int T_SEQ = 4000, DM = 256, NTOK = 8000, NTILE = 250, TPB = 125;
constexpr int NFA = 176;
constexpr int NFC = 192;

constexpr size_t WS_WA = 0;
constexpr size_t WS_WC = WS_WA + (size_t)8 * NFA * 64;
constexpr size_t WS_X1 = WS_WC + (size_t)8 * NFC * 64;
constexpr size_t WS_Q  = WS_X1 + (size_t)NTILE * 16 * 64;
constexpr size_t WS_KV = WS_Q + (size_t)8 * TPB * 4 * 64;
constexpr size_t WS_AT = WS_KV + (size_t)8 * TPB * 8 * 64 + 4096;

DI unsigned pack2(float lo, float hi) {
    f32x2 v = {lo, hi};
    bf16v2 r = __builtin_convertvector(v, bf16v2);
    return __builtin_bit_cast(unsigned, r);
}
DI float bflo(unsigned u) { return __uint_as_float(u << 16); }
DI float bfhi(unsigned u) { return __uint_as_float(u & 0xffff0000u); }
DI u32x4 pack8(const f32x16& a, int s) {
    u32x4 r;
    r[0] = pack2(a[8 * s + 0], a[8 * s + 1]);
    r[1] = pack2(a[8 * s + 2], a[8 * s + 3]);
    r[2] = pack2(a[8 * s + 4], a[8 * s + 5]);
    r[3] = pack2(a[8 * s + 6], a[8 * s + 7]);
    return r;
}
DI void unpack16(f32x16& a, const u32x4& f0, const u32x4& f1) {
#pragma unroll
    for (int i = 0; i < 4; ++i) {
        a[2 * i] = bflo(f0[i]); a[2 * i + 1] = bfhi(f0[i]);
        a[8 + 2 * i] = bflo(f1[i]); a[8 + 2 * i + 1] = bfhi(f1[i]);
    }
}
DI float xor32(float v) { return __shfl_xor(v, 32); }
DI float sigmoidf_(float x) { return __builtin_amdgcn_rcpf(1.0f + __builtin_amdgcn_exp2f(-1.4426950408889634f * x)); }
DI float siluf_(float x) { return x * sigmoidf_(x); }

struct PackArgs {
    const uint16_t* w1a; const uint16_t* w1b; const uint16_t* wqkv; const uint16_t* wo;
    const uint16_t* wp1; const uint16_t* wp2; const uint16_t* w2a; const uint16_t* w2b;
    u32x4* ws;
};

__global__ __launch_bounds__(256) void k_pack(PackArgs p) {
    const int gid = blockIdx.x * 256 + threadIdx.x;
    const int lane = gid & 63, fg = gid >> 6;
    if (fg >= 8 * (NFA + NFC)) return;
    const uint16_t* W; int N, nt, ks;
    if (fg < 8 * NFA) {
        const int w = fg / NFA, f = fg % NFA;
        if (f < 64)       { W = p.w1a; N = 1024; ks = f & 15; nt = 4 * w + (f >> 4); }
        else if (f < 128) { W = p.w1b; N = 256; ks = f - 64; nt = w; }
        else              { const int g = f - 128; W = p.wqkv; N = 768; ks = g & 15; nt = w + 8 * (g >> 4); }
    } else {
        const int fg2 = fg - 8 * NFA, w = fg2 / NFC, f = fg2 % NFC;
        if (f < 16)       { W = p.wo; N = 256; ks = f; nt = w; }
        else if (f < 48)  { const int g = f - 16; W = p.wp1; N = 512; ks = g >> 1; nt = w + 8 * (g & 1); }
        else if (f < 64)  { W = p.wp2; N = 256; ks = f - 48; nt = w; }
        else if (f < 128) { const int g = f - 64; W = p.w2a; N = 1024; ks = g & 15; nt = 4 * w + (g >> 4); }
        else              { W = p.w2b; N = 256; ks = f - 128; nt = w; }
    }
    const int r = lane & 31, h = lane >> 5;
    const uint16_t* src = W + (size_t)(16 * ks + 4 * h) * N + 32 * nt + r;
    unsigned e[8];
#pragma unroll
    for (int j = 0; j < 8; ++j) e[j] = src[(size_t)(8 * (j >> 2) + (j & 3)) * N];
    u32x4 o;
#pragma unroll
    for (int i = 0; i < 4; ++i) o[i] = e[2 * i] | (e[2 * i + 1] << 16);
    p.ws[gid] = o;
}

#define WLOAD(F) ring[(F) & 31] = wp[(F) * 64]
#define REFILL(F, PE, NF) do { if ((F) + 32 < (PE) + 16 && (F) + 32 < (NF)) WLOAD((F) + 32); } while (0)
#define DEFER(PE, NF) do { _Pragma("unroll") for (int i_ = 0; i_ < 16; ++i_) if ((PE) + 16 + i_ < (NF)) WLOAD((PE) + 16 + i_); SB(); } while (0)

DI void ln_partial(const f32x16& v, float* st, int w, int lane) {
    float s = 0.f, q = 0.f;
#pragma unroll
    for (int i = 0; i < 16; ++i) { s += v[i]; q = __builtin_fmaf(v[i], v[i], q); }
    auto r = __builtin_amdgcn_permlane32_swap(__float_as_uint(s), __float_as_uint(q), false, false);
    st[w * 64 + lane] = __uint_as_float(r[0]) + __uint_as_float(r[1]);
}
DI void ln_finish(const float* st, int lane, float& scale, float& shift) {
    const float* p = st + (lane & 31);
    float s = 0.f, q = 0.f;
#pragma unroll
    for (int i = 0; i < 8; ++i) { s += p[i * 64]; q += p[i * 64 + 32]; }
    const float m = s * (1.0f / 256.0f);
    const float var = fmaxf(q * (1.0f / 256.0f) - m * m, 0.f);
    scale = __builtin_amdgcn_rsqf(var + 1e-5f);
    shift = -m * scale;
}
DI void norm_store(const f32x16& v, float scale, float shift, u32x4* dst  , int w, int lane) {
    f32x16 n;
#pragma unroll
    for (int i = 0; i < 16; ++i) n[i] = __builtin_fmaf(v[i], scale, shift);
    dst[(2 * w) * 64 + lane] = pack8(n, 0);
    dst[(2 * w + 1) * 64 + lane] = pack8(n, 1);
}
DI float rbf(float x) { return bflo(pack2(x, 0.f) & 0xffffu); }

#define GEMM16(acc, F0, PE, NF, fxp, SWAP) do { \
        acc = (f32x16){}; u32x4 bq_[4]; \
        _Pragma("unroll") for (int i_ = 0; i_ < 4; ++i_) bq_[i_] = (fxp)[i_ * 64 + lane]; \
        _Pragma("unroll") for (int ks_ = 0; ks_ < 16; ++ks_) { \
            if (SWAP) acc = MFMA(bq_[ks_ & 3], ring[((F0) + ks_) & 31], acc); else acc = MFMA(ring[((F0) + ks_) & 31], bq_[ks_ & 3], acc); \
            if (ks_ + 4 < 16) bq_[ks_ & 3] = (fxp)[(ks_ + 4) * 64 + lane]; \
            REFILL((F0) + ks_, PE, NF); SB(); } } while (0)

#define FFN(acc, F0, NF, DBG, ST) do { \
        _Pragma("unroll") for (int t_ = 0; t_ < 4; ++t_) { \
            f32x16 a_; GEMM16(a_, (F0) + 16 * t_, (F0) + 64, NF, fx, false); \
            if (t_ == 3) { DEFER((F0) + 64, NF); STAMP(DBG, ST); } \
            _Pragma("unroll") for (int r_ = 0; r_ < 16; ++r_) a_[r_] = siluf_(a_[r_]); \
            fh[((4 * w + t_) * 2 + 0) * 64 + lane] = pack8(a_, 0); \
            fh[((4 * w + t_) * 2 + 1) * 64 + lane] = pack8(a_, 1); } \
        __syncthreads(); \
        STAMP(DBG, ST + 1); \
        acc = (f32x16){}; u32x4 bq_[4]; \
        _Pragma("unroll") for (int i_ = 0; i_ < 4; ++i_) bq_[i_] = fh[i_ * 64 + lane]; \
        _Pragma("unroll") for (int ks_ = 0; ks_ < 64; ++ks_) { \
            acc = MFMA(ring[((F0) + 64 + ks_) & 31], bq_[ks_ & 3], acc); \
            if (ks_ + 4 < 64) bq_[ks_ & 3] = fh[(ks_ + 4) * 64 + lane]; \
            REFILL((F0) + 64 + ks_, (F0) + 128, NF); SB(); } \
        DEFER((F0) + 128, NF); STAMP(DBG, ST + 2); } while (0)

#define DBGA (ws + (8u<<20))
__global__ __launch_bounds__(512, 2) void k_stageA(const uint16_t* __restrict__ x, u32x4* __restrict__ ws) {
    __shared__ u32x4 lds[16 * 64 + 64 * 64 + 256];
    u32x4* fx = lds;
    u32x4* fh = lds + 16 * 64;
    float* st0 = (float*)(lds + 16 * 64 + 64 * 64);
    float* st1 = st0 + 512;
    const int tile = blockIdx.x, tid = threadIdx.x, lane = tid & 63, h = lane >> 5, m = lane & 31;
    const int w = __builtin_amdgcn_readfirstlane(tid >> 6);
    STAMPR(DBGA, 14); STAMP(DBGA, 0);
    u32x2 xr[4];
    {
        const uint16_t* xp = x + (size_t)(tile * 32 + m) * DM + 32 * w + 4 * h;
#pragma unroll
        for (int g = 0; g < 4; ++g) xr[g] = *(const u32x2*)(xp + 8 * g);
    }
    const u32x4* wp = ws + WS_WA + (size_t)w * NFA * 64 + lane;
    u32x4 ring[32];
#pragma unroll
    for (int i = 0; i < 32; ++i) WLOAD(i);
    SB();
    f32x16 xv;
#pragma unroll
    for (int g = 0; g < 4; ++g) { xv[4 * g + 0] = bflo(xr[g][0]); xv[4 * g + 1] = bfhi(xr[g][0]); xv[4 * g + 2] = bflo(xr[g][1]); xv[4 * g + 3] = bfhi(xr[g][1]); }
    float sc, sh;
    ln_partial(xv, st0, w, lane);
    __syncthreads();
    ln_finish(st0, lane, sc, sh);
    norm_store(xv, sc, sh, fx, w, lane);
    __syncthreads();
    STAMP(DBGA, 1);
    {
        f32x16 acc;
        FFN(acc, 0, NFA, DBGA, 2);
#pragma unroll
        for (int r = 0; r < 16; ++r) xv[r] = rbf(xv[r] + 0.5f * acc[r]);
    }
    ln_partial(xv, st1, w, lane);
    {
        u32x4* xo = ws + WS_X1 + (size_t)tile * 16 * 64;
        xo[(2 * w) * 64 + lane] = pack8(xv, 0);
        xo[(2 * w + 1) * 64 + lane] = pack8(xv, 1);
    }
    __syncthreads();
    ln_finish(st1, lane, sc, sh);
    norm_store(xv, sc, sh, fx, w, lane);
    __syncthreads();
    STAMP(DBGA, 5);
    {
        const int b = tile / TPB, tt = tile % TPB, bh = b * 4 + (w >> 1), t2 = w & 1;
        u32x4* qo = ws + WS_Q + ((size_t)(bh * TPB + tt) * 4 + 2 * t2) * 64 + lane;
        u32x4* ko = ws + WS_KV + ((size_t)(bh * TPB + tt) * 8 + 2 * t2) * 64 + lane;
        f32x16 a;
        GEMM16(a, 128, NFA, NFA, fx, false);
        const float qs = 0.125f * 1.4426950408889634f;
#pragma unroll
        for (int r = 0; r < 16; ++r) a[r] *= qs;
        qo[0] = pack8(a, 0); qo[64] = pack8(a, 1);
        GEMM16(a, 144, NFA, NFA, fx, false);
        ko[0] = pack8(a, 0); ko[64] = pack8(a, 1);
        GEMM16(a, 160, NFA, NFA, fx, true);
        STAMP(DBGA, 6);
        ko[4 * 64] = pack8(a, 0); ko[5 * 64] = pack8(a, 1);
    }
    STAMP(DBGA, 7); STAMPR(DBGA, 15);
}

DI void glds16(const void* g, unsigned lds_base) {
    unsigned sv;
    asm volatile("s_mov_b32 %0, m0\n\ts_mov_b32 m0, %2\n\ts_nop 0\n\tglobal_load_lds_dwordx4 %1, off\n\ts_mov_b32 m0, %0"
                 : "=&s"(sv) : "v"(g), "s"(lds_base) : "memory");
}
#define WAIT_BAR(N) asm volatile("s_waitcnt vmcnt(" #N ") lgkmcnt(0)\n\ts_barrier" ::: "memory")

constexpr int AT_SLOT = 1024;
constexpr float AT_BIG = 1.0e30f;
#define ABL_NODMA 0
#define ABL_NOBAR 0
#define ABL_NOLDS 0
#define ABL_NOEXP 0
#define ABL_NOADD 0
#define PIN(x) asm volatile("" : "+v"(x))
DI float vadd(float a, float b) { float r; asm("v_add_f32 %0, %1, %2" : "=v"(r) : "v"(a), "v"(b)); return r; }
#define EX2(x) __builtin_amdgcn_exp2f(x)

__global__ __launch_bounds__(512, 2) void k_attn(const u32x4* __restrict__ ws_in, u32x4* __restrict__ ws) {
    __shared__ u32x4 lds[6 * AT_SLOT];
    const int tid = threadIdx.x, lane = tid & 63;
    const int w = __builtin_amdgcn_readfirstlane(tid >> 6);
    STAMP((ws + (10u<<20)), 0);
    const int bh = blockIdx.x & 7, qb = blockIdx.x >> 3, qi = w & 3, g = w >> 2;
    int qt = 4 * qb + qi; const bool qvalid = qt < TPB; if (!qvalid) qt = TPB - 1;
    const u32x4* kv = ws_in + WS_KV + (size_t)bh * TPB * 8 * 64;
    const unsigned lds0 = (unsigned)(uintptr_t)lds;
    const u32x4* src = kv + (w >> 1) * 512 + (w & 1) * 128 + lane;
    const unsigned dstk = lds0 + ((w >> 1) * 256 + (w & 1) * 128) * 16, dstv = dstk + 3 * AT_SLOT * 16;
#define DMA_K(step, slotoff) do { const u32x4* s_ = src + (size_t)(step) * 2048; \
        glds16(s_, (unsigned)__builtin_amdgcn_readfirstlane(dstk + (slotoff))); glds16(s_ + 64, (unsigned)__builtin_amdgcn_readfirstlane(dstk + (slotoff) + 1024)); } while (0)
#define DMA_V(step, slotoff) do { const u32x4* s_ = src + (size_t)(step) * 2048 + 256; \
        glds16(s_, (unsigned)__builtin_amdgcn_readfirstlane(dstv + (slotoff))); glds16(s_ + 64, (unsigned)__builtin_amdgcn_readfirstlane(dstv + (slotoff) + 1024)); } while (0)
    constexpr int SLB = AT_SLOT * 16;
    DMA_K(0, 0); DMA_V(0, 0); DMA_K(1, SLB); DMA_K(2, 2 * SLB); DMA_V(1, SLB);
    u32x4 q[4];
    {
        const u32x4* qp = ws_in + WS_Q + (size_t)(bh * TPB + qt) * 4 * 64 + lane;
#pragma unroll
        for (int f = 0; f < 4; ++f) q[f] = qp[f * 64];
    }
    const char* kbase = (const char*)lds + ((2 * g) * 256 + lane) * 16;
    const char* vbase = kbase + 3 * SLB;
#define KF(off, i) (*(const u32x4*)(kbase + (off) + ((i) >> 2) * 4096 + ((i) & 3) * 1024))
#define VF(off, i) (*(const u32x4*)(vbase + (off) + ((i) >> 2) * 4096 + ((i) & 3) * 1024))
    f32x16 o0 = (f32x16){}, o1 = (f32x16){};
    float l = 0.f, negm;
    f32x16 cm, sA0, sA1, sB0, sB1;
    u32x4 kf[8], vf[8], pk0, pk1, pk2, pk3;
    if (ABL_NOLDS) { _Pragma("unroll") for (int i = 0; i < 8; ++i) vf[i] = q[i & 3]; }
    WAIT_BAR(0);
    {
#pragma unroll
        for (int i = 0; i < 8; ++i) kf[i] = KF(0, i);
        const f32x16 z = (f32x16){};
        sA0 = MFMA(kf[0], q[0], z); sA1 = MFMA(kf[4], q[0], z);
        sA0 = MFMA(kf[1], q[1], sA0); sA1 = MFMA(kf[5], q[1], sA1);
        sA0 = MFMA(kf[2], q[2], sA0); sA1 = MFMA(kf[6], q[2], sA1);
        sA0 = MFMA(kf[3], q[3], sA0); sA1 = MFMA(kf[7], q[3], sA1);
        float mx = sA0[0];
#pragma unroll
        for (int r = 0; r < 16; ++r) { mx = fmaxf(mx, sA0[r]); mx = fmaxf(mx, sA1[r]); }
        mx = fmaxf(mx, xor32(mx));
        negm = -mx;
#pragma unroll
        for (int r = 0; r < 16; ++r) { sA0[r] = EX2(sA0[r] + negm); sA1[r] = EX2(sA1[r] + negm); cm[r] = negm; }
#pragma unroll
        for (int i = 0; i < 8; ++i) kf[i] = KF(SLB, i);
    }
    WAIT_BAR(0);
    DMA_K(3, 0);
    int s_prev = 0, s_cur = SLB, s_next = 2 * SLB;
#define ROT() do { const int t_ = s_prev; s_prev = s_cur; s_cur = s_next; s_next = t_; } while (0)
#define GA(MF, VI, P, i0, PK, e0) do { if (VI >= 0 && !ABL_NOLDS) { vf[VI < 0 ? 0 : VI] = VF(s_prev, VI < 0 ? 0 : VI); SB(); } MF; \
        if (!ABL_NOADD) { su0 = vadd(su0, P[i0]); su1 = vadd(su1, P[i0 + 1]); su0 = vadd(su0, P[i0 + 2]); su1 = vadd(su1, P[i0 + 3]); } \
        PK[e0] = pack2(P[i0], P[i0 + 1]); PK[e0 + 1] = pack2(P[i0 + 2], P[i0 + 3]); PIN(PK); PIN(su0); PIN(su1); SB(); } while (0)
#define GB(MF, KI, VI, C, i0) do { if (!ABL_NOLDS) { kf[KI] = KF(s_next, KI); if (VI >= 0) vf[VI < 0 ? 0 : VI] = VF(s_prev, VI < 0 ? 0 : VI); } SB(); MF; \
        if (!ABL_NOEXP) { C[i0] = EX2(C[i0]); C[i0 + 1] = EX2(C[i0 + 1]); C[i0 + 2] = EX2(C[i0 + 2]); C[i0 + 3] = EX2(C[i0 + 3]); } PIN(C); SB(); } while (0)
#define STEP(C0, C1, P0, P1, t) do { \
        if (!ABL_NODMA) { DMA_K(((t) + 3 < 32 ? (t) + 3 : 31), s_cur); DMA_V(((t) + 1 < 32 ? (t) + 1 : 31), s_next); } \
        float su0 = 0.f, su1 = 0.f; SB(); \
        GA(C0 = MFMA(kf[0], q[0], cm), -1, P0, 0,  pk0, 0); \
        GA(C1 = MFMA(kf[4], q[0], cm), -1, P0, 4,  pk0, 2); \
        GA(C0 = MFMA(kf[1], q[1], C0), -1, P0, 8,  pk1, 0); \
        GA(C1 = MFMA(kf[5], q[1], C1), -1, P0, 12, pk1, 2); \
        GA(C0 = MFMA(kf[2], q[2], C0), -1, P1, 0,  pk2, 0); \
        GA(C1 = MFMA(kf[6], q[2], C1), -1, P1, 4,  pk2, 2); \
        GA(C0 = MFMA(kf[3], q[3], C0), 0, P1, 8,  pk3, 0); \
        GA(C1 = MFMA(kf[7], q[3], C1), 2, P1, 12, pk3, 2); \
        l += su0 + su1; \
        if ((t) == 31) { _Pragma("unroll") for (int r_ = 0; r_ < 16; ++r_) { C1[r_] = -INFINITY; if (g == 1) C0[r_] = -INFINITY; } } \
        SB(); \
        GB(o0 = MFMA(vf[0], pk0, o0), 0, 1, C0, 0); \
        GB(o1 = MFMA(vf[2], pk0, o1), 4, 3, C0, 4); \
        GB(o0 = MFMA(vf[1], pk1, o0), 1, 4, C0, 8); \
        GB(o1 = MFMA(vf[3], pk1, o1), 5, 6, C0, 12); \
        GB(o0 = MFMA(vf[4], pk2, o0), 2, 5, C1, 0); \
        GB(o1 = MFMA(vf[6], pk2, o1), 6, 7, C1, 4); \
        GB(o0 = MFMA(vf[5], pk3, o0), 3, -1, C1, 8); \
        GB(o1 = MFMA(vf[7], pk3, o1), 7, -1, C1, 12); \
        TICK(tk1); if (!ABL_NOBAR) WAIT_BAR(4); TICK(tk2); acc_c += tk1 - tk0; acc_w += tk2 - tk1; tk0 = tk2; ROT(); \
    } while (0)
    int tk0 = 0, tk1 = 0, tk2 = 0, acc_c = 0, acc_w = 0;
#define TICK(x) do {} while (0)
    int t = 1;
    for (; t < 31; t += 2) {
        STEP(sB0, sB1, sA0, sA1, t);
        STEP(sA0, sA1, sB0, sB1, t + 1);
    }
    STEP(sB0, sB1, sA0, sA1, 31);
    {
        float sum = 0.f;
#pragma unroll
        for (int r = 0; r < 16; ++r) sum += sB0[r] + sB1[r];
        pk0 = pack8(sB0, 0); pk1 = pack8(sB0, 1);
        l += sum;
        if (g == 0) {
#pragma unroll
            for (int i = 0; i < 4; ++i) vf[i] = VF(s_prev, i);
            o0 = MFMA(vf[0], pk0, o0); o1 = MFMA(vf[2], pk0, o1);
            o0 = MFMA(vf[1], pk1, o0); o1 = MFMA(vf[3], pk1, o1);
        }
    }
    WAIT_BAR(0);
#undef DMA_K
#undef DMA_V
#undef STEP
#undef GA
#undef GB
#undef ROT
#undef KF
#undef VF
    {
        bool bad = !(l < AT_BIG);
#pragma unroll
        for (int r = 0; r < 16; ++r) bad = bad || !(fabsf(o0[r]) < INFINITY) || !(fabsf(o1[r]) < INFINITY);
        if (__builtin_expect(__any(bad), 0)) {
            o0 = (f32x16){}; o1 = (f32x16){}; l = 0.f; float mrun = -INFINITY;
            for (int kt = 0; kt < TPB; ++kt) {
                if (((kt >> 1) & 1) != g) continue;
                const u32x4* kg = kv + (size_t)kt * 512 + lane;
                f32x16 sc = (f32x16){};
#pragma unroll
                for (int f = 0; f < 4; ++f) sc = MFMA(kg[f * 64], q[f], sc);
                float mx = sc[0];
#pragma unroll
                for (int r = 1; r < 16; ++r) mx = fmaxf(mx, sc[r]);
                mx = fmaxf(mx, xor32(mx));
                const float mn = fmaxf(mrun, mx), f = EX2(mrun - mn);
                mrun = mn; l *= f;
#pragma unroll
                for (int r = 0; r < 16; ++r) { o0[r] *= f; o1[r] *= f; sc[r] = EX2(sc[r] - mn); l += sc[r]; }
                const u32x4 p0 = pack8(sc, 0), p1 = pack8(sc, 1);
                o0 = MFMA(kg[256], p0, o0); o1 = MFMA(kg[256 + 128], p0, o1);
                o0 = MFMA(kg[256 + 64], p1, o0); o1 = MFMA(kg[256 + 192], p1, o1);
            }
            negm = -mrun;
        }
    }
    l += xor32(l);
    float* cf = (float*)lds;
    if (g == 1) {
        float* dst = cf + qi * 34 * 64 + lane;
#pragma unroll
        for (int r = 0; r < 16; ++r) { dst[r * 64] = o0[r]; dst[(16 + r) * 64] = o1[r]; }
        dst[32 * 64] = negm; dst[33 * 64] = l;
    }
    __syncthreads();
    if (g == 0) {
        const float* s = cf + qi * 34 * 64 + lane;
        const float negm1 = s[32 * 64], l1 = s[33 * 64];
        const float nm = fminf(negm, negm1);
        const float f0 = __builtin_amdgcn_exp2f(nm - negm), f1 = __builtin_amdgcn_exp2f(nm - negm1);
        const float inv = __builtin_amdgcn_rcpf(l * f0 + l1 * f1);
        const float c0 = f0 * inv, c1 = f1 * inv;
#pragma unroll
        for (int r = 0; r < 16; ++r) { o0[r] = o0[r] * c0 + s[r * 64] * c1; o1[r] = o1[r] * c0 + s[(16 + r) * 64] * c1; }
        if (qvalid) {
            const int b = bh >> 2, hd = bh & 3;
            u32x4* ao = ws + WS_AT + ((size_t)(b * TPB + qt) * 16 + 4 * hd) * 64 + lane;
            ao[0] = pack8(o0, 0); ao[64] = pack8(o0, 1); ao[128] = pack8(o1, 0); ao[192] = pack8(o1, 1);
        }
    }
    STAMPR((ws + (10u<<20)), 15);
}

#define DBGC ((u32x4*)ws + (9u<<20))
__global__ __launch_bounds__(512, 2) void k_stageC(const u32x4* __restrict__ ws, const uint16_t* __restrict__ dw, uint16_t* __restrict__ out) {
    __shared__ u32x4 lds[32 * 64 + 64 * 64 + 384 + 160];
    u32x4* fx = lds;
    u32x4* fy = lds + 16 * 64;
    u32x4* fh = lds + 32 * 64;
    float* st0 = (float*)(lds + 32 * 64 + 64 * 64);
    float* st1 = st0 + 512;
    float* st2 = st0 + 1024;
    u32x4* dwl = lds + 32 * 64 + 64 * 64 + 384;
    const int tile = blockIdx.x, tid = threadIdx.x, lane = tid & 63, h = lane >> 5, m = lane & 31;
    const int w = __builtin_amdgcn_readfirstlane(tid >> 6);
    STAMPR(DBGC, 14); STAMP(DBGC, 0);

    const int tt = tile % TPB;
    const int hm = m & 3;
    int toff = hm < 2 ? hm - 2 : 30 + hm;
    const int tl = tt * 32 + toff;
    const bool hvalid = (tl >= 0) && (tl < T_SEQ);
    if (!hvalid) toff = 0;
    const int gtok = tile * 32 + toff;
    const size_t hbase = (size_t)(gtok >> 5) * 16 * 64 + h * 32 + (gtok & 31);

    u32x4 in[8];
    {
        const u32x4* at = ws + WS_AT;
        const u32x4* x1 = ws + WS_X1;
        const size_t mb = (size_t)tile * 16 * 64 + lane;
        in[0] = at[mb + (2 * w) * 64]; in[1] = at[mb + (2 * w + 1) * 64];
        in[2] = at[hbase + (2 * w) * 64]; in[3] = at[hbase + (2 * w + 1) * 64];
        in[4] = x1[mb + (2 * w) * 64]; in[5] = x1[mb + (2 * w + 1) * 64];
        in[6] = x1[hbase + (2 * w) * 64]; in[7] = x1[hbase + (2 * w + 1) * 64];
    }
    u32x4 dwr;
    if (tid < 160) dwr = ((const u32x4*)dw)[tid];
    const u32x4* wp = ws + WS_WC + (size_t)w * NFC * 64 + lane;
    u32x4 ring[32];
#pragma unroll
    for (int i = 0; i < 32; ++i) WLOAD(i);
    SB();
    f32x16 xv, yv;
    fx[(2 * w) * 64 + lane] = in[0]; fx[(2 * w + 1) * 64 + lane] = in[1];
    fy[(2 * w) * 64 + lane] = in[2]; fy[(2 * w + 1) * 64 + lane] = in[3];
    if (tid < 160) dwl[tid] = dwr;
    unpack16(xv, in[4], in[5]);
    unpack16(yv, in[6], in[7]);
    __syncthreads();
    STAMP(DBGC, 1);
    {
        f32x16 a = (f32x16){}, b = (f32x16){};
        u32x4 bx[2], by[2];
        bx[0] = fx[lane]; by[0] = fy[lane]; bx[1] = fx[64 + lane]; by[1] = fy[64 + lane];
#pragma unroll
        for (int ks = 0; ks < 16; ++ks) {
            a = MFMA(ring[ks & 31], bx[ks & 1], a);
            b = MFMA(ring[ks & 31], by[ks & 1], b);
            if (ks + 2 < 16) { bx[ks & 1] = fx[(ks + 2) * 64 + lane]; by[ks & 1] = fy[(ks + 2) * 64 + lane]; }
            REFILL(ks, 16, NFC);
            SB();
        }
        DEFER(16, NFC);
        STAMP(DBGC, 2);
#pragma unroll
        for (int r = 0; r < 16; ++r) { xv[r] += a[r]; yv[r] += b[r]; }
    }
    float sc, sh, sc2, sh2;
    ln_partial(xv, st0, w, lane);
    ln_partial(yv, st1, w, lane);
    __syncthreads();
    ln_finish(st0, lane, sc, sh);
    ln_finish(st1, lane, sc2, sh2);
    norm_store(xv, sc, sh, fx, w, lane);
    norm_store(yv, sc2, sh2, fy, w, lane);
    __syncthreads();
    STAMP(DBGC, 3);
    {
        f32x16 a = (f32x16){}, b = (f32x16){}, c = (f32x16){}, d = (f32x16){};
        u32x4 bxn = fx[lane], byn = fy[lane];
#pragma unroll
        for (int ks = 0; ks < 16; ++ks) {
            const int F = 16 + 2 * ks;
            const u32x4 bx = bxn, by = byn;
            if (ks + 1 < 16) { bxn = fx[(ks + 1) * 64 + lane]; byn = fy[(ks + 1) * 64 + lane]; }
            a = MFMA(ring[F & 31], bx, a); c = MFMA(ring[F & 31], by, c); REFILL(F, 48, NFC);
            b = MFMA(ring[(F + 1) & 31], bx, b); d = MFMA(ring[(F + 1) & 31], by, d); REFILL(F + 1, 48, NFC);
            SB();
        }
        STAMP(DBGC, 4);
#pragma unroll
        for (int r = 0; r < 16; ++r) {
            a[r] = a[r] * sigmoidf_(b[r]);
            c[r] = hvalid ? c[r] * sigmoidf_(d[r]) : 0.f;
        }
        f32x4* cs = (f32x4*)fh + w * (4 * 2 * 36);
#pragma unroll
        for (int g = 0; g < 4; ++g) {
            cs[(g * 2 + h) * 36 + m + 2] = (f32x4){a[4 * g], a[4 * g + 1], a[4 * g + 2], a[4 * g + 3]};
            if (m < 4) cs[(g * 2 + h) * 36 + (m < 2 ? m : 32 + m)] = (f32x4){c[4 * g], c[4 * g + 1], c[4 * g + 2], c[4 * g + 3]};
        }
        SB();
        DEFER(48, NFC);
        __syncthreads();
        const uint16_t* dp = (const uint16_t*)dwl + 32 * w + 4 * h;
#pragma unroll
        for (int g = 0; g < 4; ++g) {
            f32x4 acc = (f32x4){0.f, 0.f, 0.f, 0.f};
#pragma unroll
            for (int i = 0; i < 5; ++i) {
                const f32x4 hv = cs[(g * 2 + h) * 36 + m + i];
                const u32x2 t = *(const u32x2*)(dp + i * DM + 8 * g);
                acc[0] += hv[0] * bflo(t[0]); acc[1] += hv[1] * bfhi(t[0]); acc[2] += hv[2] * bflo(t[1]); acc[3] += hv[3] * bfhi(t[1]);
            }
#pragma unroll
            for (int i = 0; i < 4; ++i) a[4 * g + i] = siluf_(acc[i]);
        }
        fx[(2 * w) * 64 + lane] = pack8(a, 0);
        fx[(2 * w + 1) * 64 + lane] = pack8(a, 1);
    }
    __syncthreads();
    STAMP(DBGC, 5);
    {
        f32x16 a;
        GEMM16(a, 48, 64, NFC, fx, false);
        DEFER(64, NFC);
        STAMP(DBGC, 6);
#pragma unroll
        for (int r = 0; r < 16; ++r) xv[r] += a[r];
    }
    ln_partial(xv, st2, w, lane);
    __syncthreads();
    ln_finish(st2, lane, sc, sh);
    norm_store(xv, sc, sh, fx, w, lane);
    __syncthreads();
    STAMP(DBGC, 7);
    {
        f32x16 acc;
        FFN(acc, 64, NFC, DBGC, 8);
#pragma unroll
        for (int r = 0; r < 16; ++r) xv[r] = __builtin_fmaf(0.5f, acc[r], xv[r]);
    }
    ln_partial(xv, st0, w, lane);
    __syncthreads();
    ln_finish(st0, lane, sc, sh);
    {
        uint16_t* op = out + (size_t)(tile * 32 + m) * DM + 32 * w + 4 * h;
#pragma unroll
        for (int g = 0; g < 4; ++g) {
            u32x2 t;
            t[0] = pack2(__builtin_fmaf(xv[4 * g], sc, sh), __builtin_fmaf(xv[4 * g + 1], sc, sh));
            t[1] = pack2(__builtin_fmaf(xv[4 * g + 2], sc, sh), __builtin_fmaf(xv[4 * g + 3], sc, sh));
            *(u32x2*)(op + 8 * g) = t;
        }
    }
    STAMP(DBGC, 11); STAMPR(DBGC, 15);
}

extern "C" void kernel_launch(void* const* d_in, const int* in_sizes, int n_in,
                              void* d_out, int out_size, void* d_ws, size_t ws_size,
                              hipStream_t stream) {
    PackArgs p;
    p.w1a = (const uint16_t*)d_in[1]; p.w1b = (const uint16_t*)d_in[2]; p.wqkv = (const uint16_t*)d_in[3]; p.wo = (const uint16_t*)d_in[4];
    p.wp1 = (const uint16_t*)d_in[5]; p.wp2 = (const uint16_t*)d_in[7]; p.w2a = (const uint16_t*)d_in[8]; p.w2b = (const uint16_t*)d_in[9];
    p.ws = (u32x4*)d_ws;
    u32x4* ws = (u32x4*)d_ws;
    const int nchunk = 8 * (NFA + NFC) * 64;
    k_pack<<<(nchunk + 255) / 256, 256, 0, stream>>>(p);
    k_stageA<<<NTILE, 512, 0, stream>>>((const uint16_t*)d_in[0], ws);
    k_attn<<<256, 512, 0, stream>>>(ws, ws);
    k_stageC<<<NTILE, 512, 0, stream>>>(ws, (const uint16_t*)d_in[6], (uint16_t*)d_out);
}
```

```cpp
#include <hip/hip_runtime.h>
#include <hip/hip_bf16.h>
#include <stdint.h>

typedef short bf16x8 __attribute__((ext_vector_type(8)));
typedef float f32x16 __attribute__((ext_vector_type(16)));
typedef float f32x2 __attribute__((ext_vector_type(2)));
typedef float f32x4 __attribute__((ext_vector_type(4)));
typedef unsigned u32x4 __attribute__((ext_vector_type(4)));
typedef unsigned u32x2 __attribute__((ext_vector_type(2)));
typedef __bf16 bf16v2 __attribute__((ext_vector_type(2)));

#define DI __device__ __forceinline__
#define SB() __builtin_amdgcn_sched_barrier(0)
#define STAMPR(base, i) do {} while (0)
#define STAMP(base, i) do {} while (0)
#define MFMA(a, b, c) __builtin_amdgcn_mfma_f32_32x32x16_bf16(__builtin_bit_cast(bf16x8, (a)), __builtin_bit_cast(bf16x8, (b)), (c), 0, 0, 0)

constexpr int T_SEQ = 4000, DM = 256, NTOK = 8000, NTILE = 250, TPB = 125;
constexpr int NFA = 176;
constexpr int NFC = 192;

constexpr size_t WS_WA = 0;
constexpr size_t WS_WC = WS_WA + (size_t)8 * NFA * 64;
constexpr size_t WS_X1 = WS_WC + (size_t)8 * NFC * 64;
constexpr size_t WS_Q  = WS_X1 + (size_t)NTILE * 16 * 64;
constexpr size_t WS_KV = WS_Q + (size_t)8 * TPB * 4 * 64;
constexpr size_t WS_AT = WS_KV + (size_t)8 * TPB * 8 * 64 + 4096;

DI unsigned pack2(float lo, float hi) {
    f32x2 v = {lo, hi};
    bf16v2 r = __builtin_convertvector(v, bf16v2);
    return __builtin_bit_cast(unsigned, r);
}
DI float bflo(unsigned u) { return __uint_as_float(u << 16); }
DI float bfhi(unsigned u) { return __uint_as_float(u & 0xffff0000u); }
DI u32x4 pack8(const f32x16& a, int s) {
    u32x4 r;
    r[0] = pack2(a[8 * s + 0], a[8 * s + 1]);
    r[1] = pack2(a[8 * s + 2], a[8 * s + 3]);
    r[2] = pack2(a[8 * s + 4], a[8 * s + 5]);
    r[3] = pack2(a[8 * s + 6], a[8 * s + 7]);
    return r;
}
DI void unpack16(f32x16& a, const u32x4& f0, const u32x4& f1) {
#pragma unroll
    for (int i = 0; i < 4; ++i) {
        a[2 * i] = bflo(f0[i]); a[2 * i + 1] = bfhi(f0[i]);
        a[8 + 2 * i] = bflo(f1[i]); a[8 + 2 * i + 1] = bfhi(f1[i]);
    }
}
DI float xor32(float v) { return __shfl_xor(v, 32); }
DI float sigmoidf_(float x) { return __builtin_amdgcn_rcpf(1.0f + __builtin_amdgcn_exp2f(-1.4426950408889634f * x)); }
DI float siluf_(float x) { return x * sigmoidf_(x); }

struct PackArgs {
    const uint16_t* w1a; const uint16_t* w1b; const uint16_t* wqkv; const uint16_t* wo;
    const uint16_t* wp1; const uint16_t* wp2; const uint16_t* w2a; const uint16_t* w2b;
    u32x4* ws;
};

__global__ __launch_bounds__(256) void k_pack(PackArgs p) {
    const int gid = blockIdx.x * 256 + threadIdx.x;
    const int lane = gid & 63, fg = gid >> 6;
    if (fg >= 8 * (NFA + NFC)) return;
    const uint16_t* W; int N, nt, ks;
    if (fg < 8 * NFA) {
        const int w = fg / NFA, f = fg % NFA;
        if (f < 64)       { W = p.w1a; N = 1024; ks = f & 15; nt = 4 * w + (f >> 4); }
        else if (f < 128) { W = p.w1b; N = 256; ks = f - 64; nt = w; }
        else              { const int g = f - 128; W = p.wqkv; N = 768; ks = g & 15; nt = w + 8 * (g >> 4); }
    } else {
        const int fg2 = fg - 8 * NFA, w = fg2 / NFC, f = fg2 % NFC;
        if (f < 16)       { W = p.wo; N = 256; ks = f; nt = w; }
        else if (f < 48)  { const int g = f - 16; W = p.wp1; N = 512; ks = g >> 1; nt = w + 8 * (g & 1); }
        else if (f < 64)  { W = p.wp2; N = 256; ks = f - 48; nt = w; }
        else if (f < 128) { const int g = f - 64; W = p.w2a; N = 1024; ks = g & 15; nt = 4 * w + (g >> 4); }
        else              { W = p.w2b; N = 256; ks = f - 128; nt = w; }
    }
    const int r = lane & 31, h = lane >> 5;
    const uint16_t* src = W + (size_t)(16 * ks + 4 * h) * N + 32 * nt + r;
    unsigned e[8];
#pragma unroll
    for (int j = 0; j < 8; ++j) e[j] = src[(size_t)(8 * (j >> 2) + (j & 3)) * N];
    u32x4 o;
#pragma unroll
    for (int i = 0; i < 4; ++i) o[i] = e[2 * i] | (e[2 * i + 1] << 16);
    p.ws[gid] = o;
}

DI void glds16(const void* g, unsigned lds_base) {
    unsigned sv;
    asm volatile("s_mov_b32 %0, m0\n\ts_mov_b32 m0, %2\n\ts_nop 0\n\tglobal_load_lds_dwordx4 %1, off\n\ts_mov_b32 m0, %0"
                 : "=&s"(sv) : "v"(g), "s"(lds_base) : "memory");
}
template <int NI> DI void l2_warm(const u32x4* base, int total_chunks, int rank, int w, int lane, unsigned lds_scratch) {
#pragma unroll
    for (int i = 0; i < NI; ++i) {
        int c = ((rank * NI + i) * 8 + w) * 64;
        c = c + 64 <= total_chunks ? c : total_chunks - 64;
        glds16(base + c + lane, lds_scratch);
    }
}
#define WLOAD(F) ring[(F) & 31] = wp[(F) * 64]
#define REFILL(F, PE) do { if ((F) + 32 < (NFX)) WLOAD((F) + 32); } while (0)
#define LD8(F) do { SB(); _Pragma("unroll") for (int i_ = 0; i_ < 8; ++i_) WLOAD((F) + i_); SB(); } while (0)
#define RAWBAR() do { SB(); __builtin_amdgcn_s_barrier(); SB(); } while (0)

DI void ln_partial(const f32x16& v, float* st, int w, int lane) {
    float s = 0.f, q = 0.f;
#pragma unroll
    for (int i = 0; i < 16; ++i) { s += v[i]; q = __builtin_fmaf(v[i], v[i], q); }
    auto r = __builtin_amdgcn_permlane32_swap(__float_as_uint(s), __float_as_uint(q), false, false);
    st[w * 64 + lane] = __uint_as_float(r[0]) + __uint_as_float(r[1]);
}
DI void ln_finish(const float* st, int lane, float& scale, float& shift) {
    const float* p = st + (lane & 31);
    float s = 0.f, q = 0.f;
#pragma unroll
    for (int i = 0; i < 8; ++i) { s += p[i * 64]; q += p[i * 64 + 32]; }
    const float m = s * (1.0f / 256.0f);
    const float var = fmaxf(q * (1.0f / 256.0f) - m * m, 0.f);
    scale = __builtin_amdgcn_rsqf(var + 1e-5f);
    shift = -m * scale;
}
DI void norm_store(const f32x16& v, float scale, float shift, u32x4* dst  , int w, int lane) {
    f32x16 n;
#pragma unroll
    for (int i = 0; i < 16; ++i) n[i] = __builtin_fmaf(v[i], scale, shift);
    dst[(2 * w) * 64 + lane] = pack8(n, 0);
    dst[(2 * w + 1) * 64 + lane] = pack8(n, 1);
}
DI float rbf(float x) { return bflo(pack2(x, 0.f) & 0xffffu); }

#define GEMM16(acc, F0, PE, fxp, SWAP) do { \
        acc = (f32x16){}; u32x4 bq_[4]; \
        _Pragma("unroll") for (int i_ = 0; i_ < 4; ++i_) bq_[i_] = (fxp)[i_ * 64 + lane]; \
        _Pragma("unroll") for (int ks_ = 0; ks_ < 16; ++ks_) { \
            if (SWAP) acc = MFMA(bq_[ks_ & 3], ring[((F0) + ks_) & 31], acc); else acc = MFMA(ring[((F0) + ks_) & 31], bq_[ks_ & 3], acc); \
            if (ks_ + 4 < 16) bq_[ks_ & 3] = (fxp)[(ks_ + 4) * 64 + lane]; \
            REFILL((F0) + ks_, PE); SB(); } } while (0)

#define FFN(acc, F0, DBG, ST) do { \
        _Pragma("unroll") for (int t_ = 0; t_ < 4; ++t_) { \
            f32x16 a_; GEMM16(a_, (F0) + 16 * t_, (F0) + 64, fx, false); \
            if (t_ == 3) { STAMP(DBG, ST); } \
            _Pragma("unroll") for (int r_ = 0; r_ < 8; ++r_) a_[r_] = siluf_(a_[r_]); \
            fh[((4 * w + t_) * 2 + 0) * 64 + lane] = pack8(a_, 0); \
            _Pragma("unroll") for (int r_ = 8; r_ < 16; ++r_) a_[r_] = siluf_(a_[r_]); \
            fh[((4 * w + t_) * 2 + 1) * 64 + lane] = pack8(a_, 1); } \
        __syncthreads(); \
        STAMP(DBG, ST + 1); \
        acc = (f32x16){}; u32x4 bq_[4]; \
        _Pragma("unroll") for (int i_ = 0; i_ < 4; ++i_) bq_[i_] = fh[i_ * 64 + lane]; \
        _Pragma("unroll") for (int ks_ = 0; ks_ < 64; ++ks_) { \
            acc = MFMA(ring[((F0) + 64 + ks_) & 31], bq_[ks_ & 3], acc); \
            if (ks_ + 4 < 64) bq_[ks_ & 3] = fh[(ks_ + 4) * 64 + lane]; \
            REFILL((F0) + 64 + ks_, (F0) + 128); SB(); } \
        STAMP(DBG, ST + 2); } while (0)

#define DBGA (ws + (8u<<20))
__global__ __launch_bounds__(512, 2) void k_stageA(const uint16_t* __restrict__ x, u32x4* __restrict__ ws) {
    __shared__ u32x4 lds[16 * 64 + 64 * 64 + 256 + 512];
    u32x4* fx = lds;
    u32x4* fh = lds + 16 * 64;
    float* st0 = (float*)(lds + 16 * 64 + 64 * 64);
    float* st1 = st0 + 512;
    const int tile = blockIdx.x, tid = threadIdx.x, lane = tid & 63, h = lane >> 5, m = lane & 31;
    const int w = __builtin_amdgcn_readfirstlane(tid >> 6);
    STAMPR(DBGA, 14); STAMP(DBGA, 0);
    u32x2 xr[4];
    {
        const uint16_t* xp = x + (size_t)(tile * 32 + m) * DM + 32 * w + 4 * h;
#pragma unroll
        for (int g = 0; g < 4; ++g) xr[g] = *(const u32x2*)(xp + 8 * g);
    }
    RAWBAR();
    constexpr int NFX = NFA;
    const u32x4* wp = ws + WS_WA + (size_t)w * NFA * 64 + lane;
    u32x4 ring[32];
    LD8(0);
    f32x16 xv;
#pragma unroll
    for (int g = 0; g < 4; ++g) { xv[4 * g + 0] = bflo(xr[g][0]); xv[4 * g + 1] = bfhi(xr[g][0]); xv[4 * g + 2] = bflo(xr[g][1]); xv[4 * g + 3] = bfhi(xr[g][1]); }
    float sc, sh;
    ln_partial(xv, st0, w, lane);
    LD8(8);
    __syncthreads();
    LD8(16);
    ln_finish(st0, lane, sc, sh);
    norm_store(xv, sc, sh, fx, w, lane);
    LD8(24);
    __syncthreads();
    STAMP(DBGA, 1);
    {
        f32x16 acc;
        FFN(acc, 0, DBGA, 2);
#pragma unroll
        for (int r = 0; r < 16; ++r) xv[r] = rbf(__builtin_fmaf(0.5f, rbf(acc[r]), xv[r]));
    }
    ln_partial(xv, st1, w, lane);
    {
        u32x4* xo = ws + WS_X1 + (size_t)tile * 16 * 64;
        xo[(2 * w) * 64 + lane] = pack8(xv, 0);
        xo[(2 * w + 1) * 64 + lane] = pack8(xv, 1);
    }
    __syncthreads();
    ln_finish(st1, lane, sc, sh);
    norm_store(xv, sc, sh, fx, w, lane);
    __syncthreads();
    STAMP(DBGA, 5);
    {
        const int b = tile / TPB, tt = tile % TPB, bh = b * 4 + (w >> 1), t2 = w & 1;
        u32x4* qo = ws + WS_Q + ((size_t)(bh * TPB + tt) * 4 + 2 * t2) * 64 + lane;
        u32x4* ko = ws + WS_KV + ((size_t)(bh * TPB + tt) * 8 + 2 * t2) * 64 + lane;
        f32x16 a;
        GEMM16(a, 128, NFA, fx, false);
        const float qs = 0.125f * 1.4426950408889634f;
#pragma unroll
        for (int r = 0; r < 16; ++r) a[r] *= qs;
        qo[0] = pack8(a, 0); qo[64] = pack8(a, 1);
        GEMM16(a, 144, NFA, fx, false);
        ko[0] = pack8(a, 0); ko[64] = pack8(a, 1);
        GEMM16(a, 160, NFA, fx, true);
        STAMP(DBGA, 6);
        ko[4 * 64] = pack8(a, 0); ko[5 * 64] = pack8(a, 1);
    }
    STAMP(DBGA, 7); STAMPR(DBGA, 15);
}

#define WAIT_BAR(N) asm volatile("s_waitcnt vmcnt(" #N ") lgkmcnt(0)\n\ts_barrier" ::: "memory")

constexpr int AT_SLOT = 1024;
constexpr float AT_BIG = 1.0e30f;
#define ABL_NODMA 0
#define ABL_NOBAR 0
#define ABL_NOLDS 0
#define ABL_NOEXP 0
#define ABL_NOADD 0
#define PIN(x) asm volatile("" : "+v"(x))
DI float vadd(float a, float b) { float r; asm("v_add_f32 %0, %1, %2" : "=v"(r) : "v"(a), "v"(b)); return r; }
#define EX2(x) __builtin_amdgcn_exp2f(x)

__global__ __launch_bounds__(512, 2) void k_attn(const u32x4* __restrict__ ws_in, u32x4* __restrict__ ws) {
    __shared__ u32x4 lds[4 * 2048];
    const int tid = threadIdx.x, lane = tid & 63;
    const int w = __builtin_amdgcn_readfirstlane(tid >> 6);
    STAMP((ws + (10u<<20)), 0);
    const int bh = blockIdx.x & 7, qb = blockIdx.x >> 3, qi = w & 3, g = w >> 2;
    int qt = 4 * qb + qi; const bool qvalid = qt < TPB; if (!qvalid) qt = TPB - 1;
    const u32x4* kv = ws_in + WS_KV + (size_t)bh * TPB * 8 * 64;
    const unsigned lds0 = (unsigned)(uintptr_t)lds;
    const u32x4* src = kv + (w >> 1) * 512 + (w & 1) * 128 + lane;
    const u32x4* src31 = kv + 124 * 512 + (w & 1) * 128 + lane;
    const unsigned dstk = lds0 + (w >> 1) * 4096 + (w & 1) * 2048;
#define DMA2(sp, dst) do { glds16((sp), (unsigned)__builtin_amdgcn_readfirstlane(dst)); glds16((sp) + 64, (unsigned)__builtin_amdgcn_readfirstlane((dst) + 1024)); } while (0)
#define DMA_K(step) do { const int st_ = (step); const u32x4* s_ = st_ < 31 ? src + (size_t)st_ * 2048 : src31; DMA2(s_, dstk + (st_ & 3) * 32768); } while (0)
#define DMA_V(step) do { const int st_ = (step); const u32x4* s_ = st_ < 31 ? src + (size_t)st_ * 2048 : src31; DMA2(s_ + 256, dstk + (st_ & 3) * 32768 + 16384); } while (0)
    DMA_K(0); DMA_V(0); DMA_K(1);
    u32x4 q[4];
    {
        const u32x4* qp = ws_in + WS_Q + (size_t)(bh * TPB + qt) * 4 * 64 + lane;
#pragma unroll
        for (int f = 0; f < 4; ++f) q[f] = qp[f * 64];
    }
    DMA_K(2); DMA_V(1);
    const char* lbase = (const char*)lds + (2 * g) * 4096 + lane * 16;
#define KF(off, i) (*(const u32x4*)(lbase + (off) + (i) * 1024))
#define VF(off, i) (*(const u32x4*)(lbase + (off) + 16384 + (i) * 1024))
    f32x16 o0 = (f32x16){}, o1 = (f32x16){};
    float negm, su0 = 0.f, su1 = 0.f;
    f32x16 cm, sA, sB;
    u32x4 kf[4], vf[4], pA0, pA1, pB0, pB1;
    WAIT_BAR(4);
    {
#pragma unroll
        for (int i = 0; i < 4; ++i) kf[i] = KF(0, i);
        const f32x16 z = (f32x16){};
        sA = MFMA(kf[0], q[0], z); sA = MFMA(kf[1], q[1], sA); sA = MFMA(kf[2], q[2], sA); sA = MFMA(kf[3], q[3], sA);
#pragma unroll
        for (int i = 0; i < 4; ++i) kf[i] = KF(4096, i);
        float mx = sA[0];
#pragma unroll
        for (int r = 1; r < 16; ++r) mx = fmaxf(mx, sA[r]);
        mx = fmaxf(mx, xor32(mx));
        negm = -mx;
#pragma unroll
        for (int r = 0; r < 16; ++r) { sA[r] += negm; cm[r] = negm; sB[r] = 0.f; }
        pB0 = (u32x4){0u, 0u, 0u, 0u}; pB1 = pB0;
    }
#define FIN(S, i, PK, e) do { su0 = vadd(su0, S[i]); su1 = vadd(su1, S[(i) + 1]); PK[e] = pack2(S[i], S[(i) + 1]); PIN(PK); PIN(su0); PIN(su1); } while (0)
#define EXP2(S, i) do { S[i] = EX2(S[i]); S[(i) + 1] = EX2(S[(i) + 1]); PIN(S); } while (0)
#define PERIOD(SC, SN, PP0, PP1, PC0, PC1, koff, voff) do { \
        vf[0] = VF(voff, 0); FIN(SN, 14, PP1, 3); SB(); SN = MFMA(kf[0], q[0], cm); EXP2(SC, 0); SB(); \
        vf[2] = VF(voff, 2); SB(); SN = MFMA(kf[1], q[1], SN); EXP2(SC, 2); FIN(SC, 0, PC0, 0); SB(); \
        vf[1] = VF(voff, 1); SB(); SN = MFMA(kf[2], q[2], SN); EXP2(SC, 4); FIN(SC, 2, PC0, 1); SB(); \
        vf[3] = VF(voff, 3); SB(); SN = MFMA(kf[3], q[3], SN); EXP2(SC, 6); FIN(SC, 4, PC0, 2); SB(); \
        kf[0] = KF(koff, 0); SB(); o0 = MFMA(vf[0], PP0, o0); EXP2(SC, 8); FIN(SC, 6, PC0, 3); SB(); \
        kf[1] = KF(koff, 1); SB(); o1 = MFMA(vf[2], PP0, o1); EXP2(SC, 10); FIN(SC, 8, PC1, 0); SB(); \
        kf[2] = KF(koff, 2); SB(); o0 = MFMA(vf[1], PP1, o0); EXP2(SC, 12); FIN(SC, 10, PC1, 1); SB(); \
        kf[3] = KF(koff, 3); SB(); o1 = MFMA(vf[3], PP1, o1); EXP2(SC, 14); FIN(SC, 12, PC1, 2); SB(); \
    } while (0)
    int tk0 = 0, tk1 = 0, tk2 = 0, acc_c = 0, acc_w = 0;
#define TICK(x) do {} while (0)
    int vprev = 0;
    for (int i = 0; i < 31; ++i) {
        const int scur = (i & 3) * 32768, snext = ((i + 1) & 3) * 32768;
        if (!ABL_NODMA) { DMA_K(i + 3 < 32 ? i + 3 : 31); DMA_V(i + 2 < 32 ? i + 2 : 31); }
        SB();
        PERIOD(sA, sB, pB0, pB1, pA0, pA1, snext, vprev);
        PERIOD(sB, sA, pA0, pA1, pB0, pB1, snext + 4096, scur);
        vprev = scur + 4096;
        TICK(tk1); if (!ABL_NOBAR) WAIT_BAR(4); TICK(tk2); acc_c += tk1 - tk0; acc_w += tk2 - tk1; tk0 = tk2;
    }
    {
        if (g == 1) {
#pragma unroll
            for (int r = 0; r < 16; ++r) sA[r] = -INFINITY;
        }
        PERIOD(sA, sB, pB0, pB1, pA0, pA1, 0, vprev);
#pragma unroll
        for (int r = 0; r < 16; ++r) sB[r] = -INFINITY;
        PERIOD(sB, sA, pA0, pA1, pB0, pB1, 0, 3 * 32768);
    }
    float l = su0 + su1;
    WAIT_BAR(0);
#undef DMA_K
#undef DMA_V
#undef DMA2
#undef PERIOD
#undef FIN
#undef EXP2
#undef KF
#undef VF
    {
        bool bad = !(l < AT_BIG);
#pragma unroll
        for (int r = 0; r < 16; ++r) bad = bad || !(fabsf(o0[r]) < INFINITY) || !(fabsf(o1[r]) < INFINITY);
        if (__builtin_expect(__any(bad), 0)) {
            o0 = (f32x16){}; o1 = (f32x16){}; l = 0.f; float mrun = -INFINITY;
            for (int kt = 0; kt < TPB; ++kt) {
                if (((kt >> 1) & 1) != g) continue;
                const u32x4* kg = kv + (size_t)kt * 512 + lane;
                f32x16 sc = (f32x16){};
#pragma unroll
                for (int f = 0; f < 4; ++f) sc = MFMA(kg[f * 64], q[f], sc);
                float mx = sc[0];
#pragma unroll
                for (int r = 1; r < 16; ++r) mx = fmaxf(mx, sc[r]);
                mx = fmaxf(mx, xor32(mx));
                const float mn = fmaxf(mrun, mx), f = EX2(mrun - mn);
                mrun = mn; l *= f;
#pragma unroll
                for (int r = 0; r < 16; ++r) { o0[r] *= f; o1[r] *= f; sc[r] = EX2(sc[r] - mn); l += sc[r]; }
                const u32x4 p0 = pack8(sc, 0), p1 = pack8(sc, 1);
                o0 = MFMA(kg[256], p0, o0); o1 = MFMA(kg[256 + 128], p0, o1);
                o0 = MFMA(kg[256 + 64], p1, o0); o1 = MFMA(kg[256 + 192], p1, o1);
            }
            negm = -mrun;
        }
    }
    l += xor32(l);
    float* cf = (float*)lds;
    if (g == 1) {
        float* dst = cf + qi * 34 * 64 + lane;
#pragma unroll
        for (int r = 0; r < 16; ++r) { dst[r * 64] = o0[r]; dst[(16 + r) * 64] = o1[r]; }
        dst[32 * 64] = negm; dst[33 * 64] = l;
    }
    __syncthreads();
    if (g == 0) {
        const float* s = cf + qi * 34 * 64 + lane;
        const float negm1 = s[32 * 64], l1 = s[33 * 64];
        const float nm = fminf(negm, negm1);
        const float f0 = __builtin_amdgcn_exp2f(nm - negm), f1 = __builtin_amdgcn_exp2f(nm - negm1);
        const float inv = __builtin_amdgcn_rcpf(l * f0 + l1 * f1);
        const float c0 = f0 * inv, c1 = f1 * inv;
#pragma unroll
        for (int r = 0; r < 16; ++r) { o0[r] = o0[r] * c0 + s[r * 64] * c1; o1[r] = o1[r] * c0 + s[(16 + r) * 64] * c1; }
        if (qvalid) {
            const int b = bh >> 2, hd = bh & 3;
            u32x4* ao = ws + WS_AT + ((size_t)(b * TPB + qt) * 16 + 4 * hd) * 64 + lane;
            ao[0] = pack8(o0, 0); ao[64] = pack8(o0, 1); ao[128] = pack8(o1, 0); ao[192] = pack8(o1, 1);
        }
    }
    STAMPR((ws + (10u<<20)), 15);
}

#define DBGC ((u32x4*)ws + (9u<<20))
__global__ __launch_bounds__(512, 2) void k_stageC(const u32x4* __restrict__ ws, const uint16_t* __restrict__ dw, uint16_t* __restrict__ out) {
    __shared__ u32x4 lds[32 * 64 + 64 * 64 + 384 + 160 + 512];
    u32x4* fx = lds;
    u32x4* fy = lds + 16 * 64;
    u32x4* fh = lds + 32 * 64;
    float* st0 = (float*)(lds + 32 * 64 + 64 * 64);
    float* st1 = st0 + 512;
    float* st2 = st0 + 1024;
    u32x4* dwl = lds + 32 * 64 + 64 * 64 + 384;
    const int tile = blockIdx.x, tid = threadIdx.x, lane = tid & 63, h = lane >> 5, m = lane & 31;
    const int w = __builtin_amdgcn_readfirstlane(tid >> 6);
    STAMPR(DBGC, 14); STAMP(DBGC, 0);

    const int tt = tile % TPB;
    const int hm = m & 3;
    int toff = hm < 2 ? hm - 2 : 30 + hm;
    const int tl = tt * 32 + toff;
    const bool hvalid = (tl >= 0) && (tl < T_SEQ);
    if (!hvalid) toff = 0;
    const int gtok = tile * 32 + toff;
    const size_t hbase = (size_t)(gtok >> 5) * 16 * 64 + h * 32 + (gtok & 31);

    u32x4 in[8];
    {
        const u32x4* at = ws + WS_AT;
        const u32x4* x1 = ws + WS_X1;
        const size_t mb = (size_t)tile * 16 * 64 + lane;
        in[0] = at[mb + (2 * w) * 64]; in[1] = at[mb + (2 * w + 1) * 64];
        in[2] = at[hbase + (2 * w) * 64]; in[3] = at[hbase + (2 * w + 1) * 64];
        in[4] = x1[mb + (2 * w) * 64]; in[5] = x1[mb + (2 * w + 1) * 64];
        in[6] = x1[hbase + (2 * w) * 64]; in[7] = x1[hbase + (2 * w + 1) * 64];
    }
    u32x4 dwr;
    if (tid < 160) dwr = ((const u32x4*)dw)[tid];
    RAWBAR();
    constexpr int NFX = NFC;
    const u32x4* wp = ws + WS_WC + (size_t)w * NFC * 64 + lane;
    u32x4 ring[32];
    LD8(0); LD8(8);
    f32x16 xv, yv;
    fx[(2 * w) * 64 + lane] = in[0]; fx[(2 * w + 1) * 64 + lane] = in[1];
    fy[(2 * w) * 64 + lane] = in[2]; fy[(2 * w + 1) * 64 + lane] = in[3];
    if (tid < 160) dwl[tid] = dwr;
    unpack16(xv, in[4], in[5]);
    unpack16(yv, in[6], in[7]);
    LD8(16);
    __syncthreads();
    LD8(24);
    STAMP(DBGC, 1);
    {
        f32x16 a = (f32x16){}, b = (f32x16){};
        u32x4 bx[2], by[2];
        bx[0] = fx[lane]; by[0] = fy[lane]; bx[1] = fx[64 + lane]; by[1] = fy[64 + lane];
#pragma unroll
        for (int ks = 0; ks < 16; ++ks) {
            a = MFMA(ring[ks & 31], bx[ks & 1], a);
            b = MFMA(ring[ks & 31], by[ks & 1], b);
            if (ks + 2 < 16) { bx[ks & 1] = fx[(ks + 2) * 64 + lane]; by[ks & 1] = fy[(ks + 2) * 64 + lane]; }
            REFILL(ks, 0);
            SB();
        }
        STAMP(DBGC, 2);
#pragma unroll
        for (int r = 0; r < 16; ++r) { xv[r] = rbf(xv[r] + rbf(a[r])); yv[r] = rbf(yv[r] + rbf(b[r])); }
    }
    float sc, sh, sc2, sh2;
    ln_partial(xv, st0, w, lane);
    ln_partial(yv, st1, w, lane);
    __syncthreads();
    ln_finish(st0, lane, sc, sh);
    ln_finish(st1, lane, sc2, sh2);
    norm_store(xv, sc, sh, fx, w, lane);
    norm_store(yv, sc2, sh2, fy, w, lane);
    __syncthreads();
    STAMP(DBGC, 3);
    {
        f32x16 a = (f32x16){}, b = (f32x16){}, c = (f32x16){}, d = (f32x16){};
        u32x4 bxn = fx[lane], byn = fy[lane];
#pragma unroll
        for (int ks = 0; ks < 16; ++ks) {
            const int F = 16 + 2 * ks;
            const u32x4 bx = bxn, by = byn;
            if (ks + 1 < 16) { bxn = fx[(ks + 1) * 64 + lane]; byn = fy[(ks + 1) * 64 + lane]; }
            a = MFMA(ring[F & 31], bx, a); c = MFMA(ring[F & 31], by, c); REFILL(F, 0);
            b = MFMA(ring[(F + 1) & 31], bx, b); d = MFMA(ring[(F + 1) & 31], by, d); REFILL(F + 1, 0);
            SB();
        }
        STAMP(DBGC, 4);
#pragma unroll
        for (int r = 0; r < 16; ++r) {
            a[r] = a[r] * sigmoidf_(b[r]);
            c[r] = hvalid ? c[r] * sigmoidf_(d[r]) : 0.f;
        }
        f32x4* cs = (f32x4*)fh + w * (4 * 2 * 36);
#pragma unroll
        for (int g = 0; g < 4; ++g) {
            cs[(g * 2 + h) * 36 + m + 2] = (f32x4){a[4 * g], a[4 * g + 1], a[4 * g + 2], a[4 * g + 3]};
            if (m < 4) cs[(g * 2 + h) * 36 + (m < 2 ? m : 32 + m)] = (f32x4){c[4 * g], c[4 * g + 1], c[4 * g + 2], c[4 * g + 3]};
        }
        __syncthreads();
        const uint16_t* dp = (const uint16_t*)dwl + 32 * w + 4 * h;
#pragma unroll
        for (int g = 0; g < 4; ++g) {
            f32x4 acc = (f32x4){0.f, 0.f, 0.f, 0.f};
#pragma unroll
            for (int i = 0; i < 5; ++i) {
                const f32x4 hv = cs[(g * 2 + h) * 36 + m + i];
                const u32x2 t = *(const u32x2*)(dp + i * DM + 8 * g);
                acc[0] += hv[0] * bflo(t[0]); acc[1] += hv[1] * bfhi(t[0]); acc[2] += hv[2] * bflo(t[1]); acc[3] += hv[3] * bfhi(t[1]);
            }
#pragma unroll
            for (int i = 0; i < 4; ++i) a[4 * g + i] = siluf_(acc[i]);
        }
        fx[(2 * w) * 64 + lane] = pack8(a, 0);
        fx[(2 * w + 1) * 64 + lane] = pack8(a, 1);
    }
    __syncthreads();
    STAMP(DBGC, 5);
    {
        f32x16 a;
        GEMM16(a, 48, 64, fx, false);
        STAMP(DBGC, 6);
#pragma unroll
        for (int r = 0; r < 16; ++r) xv[r] = rbf(xv[r] + rbf(a[r]));
    }
    ln_partial(xv, st2, w, lane);
    __syncthreads();
    ln_finish(st2, lane, sc, sh);
    norm_store(xv, sc, sh, fx, w, lane);
    __syncthreads();
    STAMP(DBGC, 7);
    {
        f32x16 acc;
        FFN(acc, 64, DBGC, 8);
#pragma unroll
        for (int r = 0; r < 16; ++r) xv[r] = rbf(__builtin_fmaf(0.5f, rbf(acc[r]), xv[r]));
    }
    ln_partial(xv, st0, w, lane);
    __syncthreads();
    ln_finish(st0, lane, sc, sh);
    {
        uint16_t* op = out + (size_t)(tile * 32 + m) * DM + 32 * w + 4 * h;
#pragma unroll
        for (int g = 0; g < 4; ++g) {
            u32x2 t;
            t[0] = pack2(__builtin_fmaf(xv[4 * g], sc, sh), __builtin_fmaf(xv[4 * g + 1], sc, sh));
            t[1] = pack2(__builtin_fmaf(xv[4 * g + 2], sc, sh), __builtin_fmaf(xv[4 * g + 3], sc, sh));
            *(u32x2*)(op + 8 * g) = t;
        }
    }
    STAMP(DBGC, 11); STAMPR(DBGC, 15);
}

extern "C" void kernel_launch(void* const* d_in, const int* in_sizes, int n_in,
                              void* d_out, int out_size, void* d_ws, size_t ws_size,
                              hipStream_t stream) {
    PackArgs p;
    p.w1a = (const uint16_t*)d_in[1]; p.w1b = (const uint16_t*)d_in[2]; p.wqkv = (const uint16_t*)d_in[3]; p.wo = (const uint16_t*)d_in[4];
    p.wp1 = (const uint16_t*)d_in[5]; p.wp2 = (const uint16_t*)d_in[7]; p.w2a = (const uint16_t*)d_in[8]; p.w2b = (const uint16_t*)d_in[9];
    p.ws = (u32x4*)d_ws;
    u32x4* ws = (u32x4*)d_ws;
    const int nchunk = 8 * (NFA + NFC) * 64;
    k_pack<<<(nchunk + 255) / 256, 256, 0, stream>>>(p);
    k_stageA<<<NTILE, 512, 0, stream>>>((const uint16_t*)d_in[0], ws);
    k_attn<<<256, 512, 0, stream>>>(ws, ws);
    k_stageC<<<NTILE, 512, 0, stream>>>(ws, (const uint16_t*)d_in[6], (uint16_t*)d_out);
}
```

```cpp
#include <hip/hip_runtime.h>
#include <hip/hip_bf16.h>
#include <stdint.h>

typedef short bf16x8 __attribute__((ext_vector_type(8)));
typedef float f32x16 __attribute__((ext_vector_type(16)));
typedef float f32x2 __attribute__((ext_vector_type(2)));
typedef float f32x4 __attribute__((ext_vector_type(4)));
typedef unsigned u32x4 __attribute__((ext_vector_type(4)));
typedef unsigned u32x2 __attribute__((ext_vector_type(2)));
typedef __bf16 bf16v2 __attribute__((ext_vector_type(2)));

#define DI __device__ __forceinline__
#define SB() __builtin_amdgcn_sched_barrier(0)
#define STAMPR(base, i) do {} while (0)
#define STAMP(base, i) do {} while (0)
#define MFMA(a, b, c) __builtin_amdgcn_mfma_f32_32x32x16_bf16(__builtin_bit_cast(bf16x8, (a)), __builtin_bit_cast(bf16x8, (b)), (c), 0, 0, 0)

constexpr int T_SEQ = 4000, DM = 256, NTOK = 8000, NTILE = 250, TPB = 125;
constexpr int NFA = 176;
constexpr int NFC = 192;

constexpr size_t WS_WA = 0;
constexpr size_t WS_WC = WS_WA + (size_t)8 * NFA * 64;
constexpr size_t WS_X1 = WS_WC + (size_t)8 * NFC * 64;
constexpr size_t WS_Q  = WS_X1 + (size_t)NTILE * 16 * 64;
constexpr size_t WS_KV = WS_Q + (size_t)8 * TPB * 4 * 64;
constexpr size_t WS_AT = WS_KV + (size_t)8 * TPB * 8 * 64 + 4096;

DI unsigned pack2(float lo, float hi) {
    f32x2 v = {lo, hi};
    bf16v2 r = __builtin_convertvector(v, bf16v2);
    return __builtin_bit_cast(unsigned, r);
}
DI float bflo(unsigned u) { return __uint_as_float(u << 16); }
DI float bfhi(unsigned u) { return __uint_as_float(u & 0xffff0000u); }
DI u32x4 pack8(const f32x16& a, int s) {
    u32x4 r;
    r[0] = pack2(a[8 * s + 0], a[8 * s + 1]);
    r[1] = pack2(a[8 * s + 2], a[8 * s + 3]);
    r[2] = pack2(a[8 * s + 4], a[8 * s + 5]);
    r[3] = pack2(a[8 * s + 6], a[8 * s + 7]);
    return r;
}
DI void unpack16(f32x16& a, const u32x4& f0, const u32x4& f1) {
#pragma unroll
    for (int i = 0; i < 4; ++i) {
        a[2 * i] = bflo(f0[i]); a[2 * i + 1] = bfhi(f0[i]);
        a[8 + 2 * i] = bflo(f1[i]); a[8 + 2 * i + 1] = bfhi(f1[i]);
    }
}
DI float xor32(float v) { return __shfl_xor(v, 32); }
DI float sigmoidf_(float x) { return __builtin_amdgcn_rcpf(1.0f + __builtin_amdgcn_exp2f(-1.4426950408889634f * x)); }
DI float siluf_(float x) { return x * sigmoidf_(x); }

struct PackArgs {
    const uint16_t* w1a; const uint16_t* w1b; const uint16_t* wqkv; const uint16_t* wo;
    const uint16_t* wp1; const uint16_t* wp2; const uint16_t* w2a; const uint16_t* w2b;
    u32x4* ws;
};

__global__ __launch_bounds__(256) void k_pack(PackArgs p) {
    const int gid = blockIdx.x * 256 + threadIdx.x;
    const int lane = gid & 63, fg = gid >> 6;
    if (fg >= 8 * (NFA + NFC)) return;
    const uint16_t* W; int N, nt, ks;
    if (fg < 8 * NFA) {
        const int w = fg / NFA, f = fg % NFA;
        if (f < 64)       { W = p.w1a; N = 1024; ks = f & 15; nt = 4 * w + (f >> 4); }
        else if (f < 128) { W = p.w1b; N = 256; ks = f - 64; nt = w; }
        else              { const int g = f - 128; W = p.wqkv; N = 768; ks = g & 15; nt = w + 8 * (g >> 4); }
    } else {
        const int fg2 = fg - 8 * NFA, w = fg2 / NFC, f = fg2 % NFC;
        if (f < 16)       { W = p.wo; N = 256; ks = f; nt = w; }
        else if (f < 48)  { const int g = f - 16; W = p.wp1; N = 512; ks = g >> 1; nt = w + 8 * (g & 1); }
        else if (f < 64)  { W = p.wp2; N = 256; ks = f - 48; nt = w; }
        else if (f < 128) { const int g = f - 64; W = p.w2a; N = 1024; ks = g & 15; nt = 4 * w + (g >> 4); }
        else              { W = p.w2b; N = 256; ks = f - 128; nt = w; }
    }
    const int r = lane & 31, h = lane >> 5;
    const uint16_t* src = W + (size_t)(16 * ks + 4 * h) * N + 32 * nt + r;
    unsigned e[8];
#pragma unroll
    for (int j = 0; j < 8; ++j) e[j] = src[(size_t)(8 * (j >> 2) + (j & 3)) * N];
    u32x4 o;
#pragma unroll
    for (int i = 0; i < 4; ++i) o[i] = e[2 * i] | (e[2 * i + 1] << 16);
    p.ws[gid] = o;
}

DI void glds16(const void* g, unsigned lds_base) {
    unsigned sv;
    asm volatile("s_mov_b32 %0, m0\n\ts_mov_b32 m0, %2\n\ts_nop 0\n\tglobal_load_lds_dwordx4 %1, off\n\ts_mov_b32 m0, %0"
                 : "=&s"(sv) : "v"(g), "s"(lds_base) : "memory");
}
template <int NI> DI void l2_warm(const u32x4* base, int total_chunks, int rank, int w, int lane, unsigned lds_scratch) {
#pragma unroll
    for (int i = 0; i < NI; ++i) {
        int c = ((rank * NI + i) * 8 + w) * 64;
        c = c + 64 <= total_chunks ? c : total_chunks - 64;
        glds16(base + c + lane, lds_scratch);
    }
}
#define WLOAD(F) ring[(F) & 31] = wp[(F) * 64]
#define REFILL(F, PE) do { if ((F) + 32 < (NFX)) WLOAD((F) + 32); } while (0)
#define LD8(F) do { SB(); _Pragma("unroll") for (int i_ = 0; i_ < 8; ++i_) WLOAD((F) + i_); SB(); } while (0)
#define RAWBAR() do { SB(); __builtin_amdgcn_s_barrier(); SB(); } while (0)

DI void ln_partial(const f32x16& v, float* st, int w, int lane) {
    float s = 0.f, q = 0.f;
#pragma unroll
    for (int i = 0; i < 16; ++i) { s += v[i]; q = __builtin_fmaf(v[i], v[i], q); }
    auto r = __builtin_amdgcn_permlane32_swap(__float_as_uint(s), __float_as_uint(q), false, false);
    st[w * 64 + lane] = __uint_as_float(r[0]) + __uint_as_float(r[1]);
}
DI void ln_finish(const float* st, int lane, float& scale, float& shift) {
    const float* p = st + (lane & 31);
    float s = 0.f, q = 0.f;
#pragma unroll
    for (int i = 0; i < 8; ++i) { s += p[i * 64]; q += p[i * 64 + 32]; }
    const float m = s * (1.0f / 256.0f);
    const float var = fmaxf(q * (1.0f / 256.0f) - m * m, 0.f);
    scale = __builtin_amdgcn_rsqf(var + 1e-5f);
    shift = -m * scale;
}
DI void norm_store(const f32x16& v, float scale, float shift, u32x4* dst  , int w, int lane) {
    f32x16 n;
#pragma unroll
    for (int i = 0; i < 16; ++i) n[i] = __builtin_fmaf(v[i], scale, shift);
    dst[(2 * w) * 64 + lane] = pack8(n, 0);
    dst[(2 * w + 1) * 64 + lane] = pack8(n, 1);
}
DI float rbf(float x) { return bflo(pack2(x, 0.f) & 0xffffu); }

#define GEMM16(acc, F0, PE, fxp, SWAP) do { \
        acc = (f32x16){}; u32x4 bq_[4]; \
        _Pragma("unroll") for (int i_ = 0; i_ < 4; ++i_) bq_[i_] = (fxp)[i_ * 64 + lane]; \
        _Pragma("unroll") for (int ks_ = 0; ks_ < 16; ++ks_) { \
            if (SWAP) acc = MFMA(bq_[ks_ & 3], ring[((F0) + ks_) & 31], acc); else acc = MFMA(ring[((F0) + ks_) & 31], bq_[ks_ & 3], acc); \
            if (ks_ + 4 < 16) bq_[ks_ & 3] = (fxp)[(ks_ + 4) * 64 + lane]; \
            REFILL((F0) + ks_, PE); SB(); } } while (0)

#define FFN(acc, F0, DBG, ST) do { \
        _Pragma("unroll") for (int t_ = 0; t_ < 4; ++t_) { \
            f32x16 a_; GEMM16(a_, (F0) + 16 * t_, (F0) + 64, fx, false); \
            if (t_ == 3) { STAMP(DBG, ST); } \
            _Pragma("unroll") for (int r_ = 0; r_ < 8; ++r_) a_[r_] = siluf_(a_[r_]); \
            fh[((4 * w + t_) * 2 + 0) * 64 + lane] = pack8(a_, 0); \
            _Pragma("unroll") for (int r_ = 8; r_ < 16; ++r_) a_[r_] = siluf_(a_[r_]); \
            fh[((4 * w + t_) * 2 + 1) * 64 + lane] = pack8(a_, 1); } \
        __syncthreads(); \
        STAMP(DBG, ST + 1); \
        acc = (f32x16){}; u32x4 bq_[4]; \
        _Pragma("unroll") for (int i_ = 0; i_ < 4; ++i_) bq_[i_] = fh[i_ * 64 + lane]; \
        _Pragma("unroll") for (int ks_ = 0; ks_ < 64; ++ks_) { \
            acc = MFMA(ring[((F0) + 64 + ks_) & 31], bq_[ks_ & 3], acc); \
            if (ks_ + 4 < 64) bq_[ks_ & 3] = fh[(ks_ + 4) * 64 + lane]; \
            REFILL((F0) + 64 + ks_, (F0) + 128); SB(); } \
        STAMP(DBG, ST + 2); } while (0)

#define DBGA (ws + (8u<<20))
__global__ __launch_bounds__(512, 2) void k_stageA(const uint16_t* __restrict__ x, u32x4* __restrict__ ws) {
    __shared__ u32x4 lds[16 * 64 + 64 * 64 + 256 + 512];
    u32x4* fx = lds;
    u32x4* fh = lds + 16 * 64;
    float* st0 = (float*)(lds + 16 * 64 + 64 * 64);
    float* st1 = st0 + 512;
    const int tile = blockIdx.x, tid = threadIdx.x, lane = tid & 63, h = lane >> 5, m = lane & 31;
    const int w = __builtin_amdgcn_readfirstlane(tid >> 6);
    STAMPR(DBGA, 14); STAMP(DBGA, 0);
    u32x2 xr[4];
    {
        const uint16_t* xp = x + (size_t)(tile * 32 + m) * DM + 32 * w + 4 * h;
#pragma unroll
        for (int g = 0; g < 4; ++g) xr[g] = *(const u32x2*)(xp + 8 * g);
    }
    RAWBAR();
    constexpr int NFX = NFA;
    const u32x4* wp = ws + WS_WA + (size_t)w * NFA * 64 + lane;
    u32x4 ring[32];
    LD8(0);
    f32x16 xv;
#pragma unroll
    for (int g = 0; g < 4; ++g) { xv[4 * g + 0] = bflo(xr[g][0]); xv[4 * g + 1] = bfhi(xr[g][0]); xv[4 * g + 2] = bflo(xr[g][1]); xv[4 * g + 3] = bfhi(xr[g][1]); }
    float sc, sh;
    ln_partial(xv, st0, w, lane);
    LD8(8);
    __syncthreads();
    LD8(16);
    ln_finish(st0, lane, sc, sh);
    norm_store(xv, sc, sh, fx, w, lane);
    LD8(24);
    __syncthreads();
    STAMP(DBGA, 1);
    {
        f32x16 acc;
        FFN(acc, 0, DBGA, 2);
#pragma unroll
        for (int r = 0; r < 16; ++r) xv[r] = rbf(__builtin_fmaf(0.5f, rbf(acc[r]), xv[r]));
    }
    ln_partial(xv, st1, w, lane);
    {
        u32x4* xo = ws + WS_X1 + (size_t)tile * 16 * 64;
        xo[(2 * w) * 64 + lane] = pack8(xv, 0);
        xo[(2 * w + 1) * 64 + lane] = pack8(xv, 1);
    }
    __syncthreads();
    ln_finish(st1, lane, sc, sh);
    norm_store(xv, sc, sh, fx, w, lane);
    __syncthreads();
    STAMP(DBGA, 5);
    {
        const int b = tile / TPB, tt = tile % TPB, bh = b * 4 + (w >> 1), t2 = w & 1;
        u32x4* qo = ws + WS_Q + ((size_t)(bh * TPB + tt) * 4 + 2 * t2) * 64 + lane;
        u32x4* ko = ws + WS_KV + ((size_t)(bh * TPB + tt) * 8 + 2 * t2) * 64 + lane;
        f32x16 a;
        GEMM16(a, 128, NFA, fx, false);
        const float qs = 0.125f * 1.4426950408889634f;
#pragma unroll
        for (int r = 0; r < 16; ++r) a[r] *= qs;
        qo[0] = pack8(a, 0); qo[64] = pack8(a, 1);
        GEMM16(a, 144, NFA, fx, false);
        ko[0] = pack8(a, 0); ko[64] = pack8(a, 1);
        GEMM16(a, 160, NFA, fx, true);
        STAMP(DBGA, 6);
        ko[4 * 64] = pack8(a, 0); ko[5 * 64] = pack8(a, 1);
    }
    STAMP(DBGA, 7); STAMPR(DBGA, 15);
}

#define WAIT_BAR(N) asm volatile("s_waitcnt vmcnt(" #N ") lgkmcnt(0)\n\ts_barrier" ::: "memory")

constexpr int AT_SLOT = 1024;
constexpr float AT_BIG = 1.0e30f;
#define ABL_NODMA 0
#define ABL_NOBAR 0
#define ABL_NOLDS 0
#define ABL_NOEXP 0
#define ABL_NOADD 0
#define PIN(x) asm volatile("" : "+v"(x))
DI float vadd(float a, float b) { float r; asm("v_add_f32 %0, %1, %2" : "=v"(r) : "v"(a), "v"(b)); return r; }
#define EX2(x) __builtin_amdgcn_exp2f(x)

__global__ __launch_bounds__(512, 2) void k_attn(const u32x4* __restrict__ ws_in, u32x4* __restrict__ ws) {
    __shared__ u32x4 lds[4 * 2048];
    const int tid = threadIdx.x, lane = tid & 63;
    const int w = __builtin_amdgcn_readfirstlane(tid >> 6);
    STAMP((ws + (10u<<20)), 0);
    const int bh = blockIdx.x & 7, qb = blockIdx.x >> 3, qi = w & 3, g = w >> 2;
    int qt = 4 * qb + qi; const bool qvalid = qt < TPB; if (!qvalid) qt = TPB - 1;
    const u32x4* kv = ws_in + WS_KV + (size_t)bh * TPB * 8 * 64;
    const unsigned lds0 = (unsigned)(uintptr_t)lds;
    const u32x4* src = kv + (w >> 1) * 512 + (w & 1) * 128 + lane;
    const u32x4* src31 = kv + 124 * 512 + (w & 1) * 128 + lane;
    const unsigned dstk = lds0 + (w >> 1) * 4096 + (w & 1) * 2048;
#define DMA2(sp, dst) do { glds16((sp), (unsigned)__builtin_amdgcn_readfirstlane(dst)); glds16((sp) + 64, (unsigned)__builtin_amdgcn_readfirstlane((dst) + 1024)); } while (0)
#define DMA_K(step) do { const int st_ = (step); const u32x4* s_ = st_ < 31 ? src + (size_t)st_ * 2048 : src31; DMA2(s_, dstk + (st_ & 3) * 32768); } while (0)
#define DMA_V(step) do { const int st_ = (step); const u32x4* s_ = st_ < 31 ? src + (size_t)st_ * 2048 : src31; DMA2(s_ + 256, dstk + (st_ & 3) * 32768 + 16384); } while (0)
    DMA_K(0); DMA_V(0); DMA_K(1);
    u32x4 q[4];
    {
        const u32x4* qp = ws_in + WS_Q + (size_t)(bh * TPB + qt) * 4 * 64 + lane;
#pragma unroll
        for (int f = 0; f < 4; ++f) q[f] = qp[f * 64];
    }
    DMA_K(2); DMA_V(1);
    const char* lbase = (const char*)lds + (2 * g) * 4096 + lane * 16;
#define KF(off, i) (*(const u32x4*)(lbase + (off) + (i) * 1024))
#define VF(off, i) (*(const u32x4*)(lbase + (off) + 16384 + (i) * 1024))
    f32x16 o0 = (f32x16){}, o1 = (f32x16){};
    float negm;
    f32x4 lsum = (f32x4){0.f, 0.f, 0.f, 0.f};
    u32x4 onesA;
    { const unsigned o_ = ((lane & 15) == ((lane >> 4) & 1)) ? 0x3f803f80u : 0u; onesA = (u32x4){o_, o_, o_, o_}; }
    f32x16 cm, sA, sB;
    u32x4 kf[4], vf[4], pA0, pA1, pB0, pB1;
    WAIT_BAR(4);
    {
#pragma unroll
        for (int i = 0; i < 4; ++i) kf[i] = KF(0, i);
        const f32x16 z = (f32x16){};
        sA = MFMA(kf[0], q[0], z); sA = MFMA(kf[1], q[1], sA); sA = MFMA(kf[2], q[2], sA); sA = MFMA(kf[3], q[3], sA);
#pragma unroll
        for (int i = 0; i < 4; ++i) kf[i] = KF(4096, i);
        float mx = sA[0];
#pragma unroll
        for (int r = 1; r < 16; ++r) mx = fmaxf(mx, sA[r]);
        mx = fmaxf(mx, xor32(mx));
        negm = -mx;
#pragma unroll
        for (int r = 0; r < 16; ++r) { sA[r] += negm; cm[r] = negm; sB[r] = 0.f; }
        pB0 = (u32x4){0u, 0u, 0u, 0u}; pB1 = pB0;
    }
#define FIN(S, i, PK, e) do { PK[e] = pack2(S[i], S[(i) + 1]); PIN(PK); } while (0)
#define EXP2(S, i) do { S[i] = EX2(S[i]); S[(i) + 1] = EX2(S[(i) + 1]); PIN(S); } while (0)
#define LSUM(PK) do { lsum = __builtin_amdgcn_mfma_f32_16x16x32_bf16(__builtin_bit_cast(bf16x8, onesA), __builtin_bit_cast(bf16x8, PK), lsum, 0, 0, 0); PIN(lsum); } while (0)
#define PERIOD(SC, SN, PP0, PP1, PC0, PC1, koff, voff, D0, D1) do { \
        vf[0] = VF(voff, 0); FIN(SN, 14, PP1, 3); SB(); SN = MFMA(kf[0], q[0], cm); PIN(SN); EXP2(SC, 0); SB(); \
        vf[2] = VF(voff, 2); SB(); SN = MFMA(kf[1], q[1], SN); PIN(SN); EXP2(SC, 2); FIN(SC, 0, PC0, 0); SB(); \
        vf[1] = VF(voff, 1); SB(); SN = MFMA(kf[2], q[2], SN); PIN(SN); EXP2(SC, 4); FIN(SC, 2, PC0, 1); SB(); D0; SB(); \
        vf[3] = VF(voff, 3); SB(); SN = MFMA(kf[3], q[3], SN); PIN(SN); EXP2(SC, 6); FIN(SC, 4, PC0, 2); SB(); \
        kf[0] = KF(koff, 0); SB(); o0 = MFMA(vf[0], PP0, o0); PIN(o0); EXP2(SC, 8); FIN(SC, 6, PC0, 3); SB(); LSUM(PP0); SB(); \
        kf[1] = KF(koff, 1); SB(); o1 = MFMA(vf[2], PP0, o1); PIN(o1); EXP2(SC, 10); FIN(SC, 8, PC1, 0); SB(); \
        kf[2] = KF(koff, 2); SB(); o0 = MFMA(vf[1], PP1, o0); PIN(o0); EXP2(SC, 12); FIN(SC, 10, PC1, 1); SB(); LSUM(PP1); SB(); D1; SB(); \
        kf[3] = KF(koff, 3); SB(); o1 = MFMA(vf[3], PP1, o1); PIN(o1); EXP2(SC, 14); FIN(SC, 12, PC1, 2); SB(); \
    } while (0)
    int tk0 = 0, tk1 = 0, tk2 = 0, acc_c = 0, acc_w = 0;
#define TICK(x) do {} while (0)
    int vprev = 0;
    for (int i = 0; i < 31; ++i) {
        const int scur = (i & 3) * 32768, snext = ((i + 1) & 3) * 32768;
        const int sk = i + 3 < 32 ? i + 3 : 31, sv = i + 2 < 32 ? i + 2 : 31;
        const u32x4* pk_ = sk < 31 ? src + (size_t)sk * 2048 : src31;
        const u32x4* pv_ = (sv < 31 ? src + (size_t)sv * 2048 : src31) + 256;
        const unsigned dk_ = dstk + (sk & 3) * 32768, dv_ = dstk + (sv & 3) * 32768 + 16384;
#define G1(p, d) do { if (!ABL_NODMA) glds16((p), (unsigned)__builtin_amdgcn_readfirstlane(d)); } while (0)
        SB();
        PERIOD(sA, sB, pB0, pB1, pA0, pA1, snext, vprev, G1(pk_, dk_), G1(pk_ + 64, dk_ + 1024));
        PERIOD(sB, sA, pA0, pA1, pB0, pB1, snext + 4096, scur, G1(pv_, dv_), G1(pv_ + 64, dv_ + 1024));
        vprev = scur + 4096;
        TICK(tk1); if (!ABL_NOBAR) WAIT_BAR(4); TICK(tk2); acc_c += tk1 - tk0; acc_w += tk2 - tk1; tk0 = tk2;
    }
    {
        if (g == 1) {
#pragma unroll
            for (int r = 0; r < 16; ++r) sA[r] = -INFINITY;
        }
        PERIOD(sA, sB, pB0, pB1, pA0, pA1, 0, vprev, (void)0, (void)0);
#pragma unroll
        for (int r = 0; r < 16; ++r) sB[r] = -INFINITY;
        PERIOD(sB, sA, pA0, pA1, pB0, pB1, 0, 3 * 32768, (void)0, (void)0);
    }
    float l;
    { const float la = __shfl(lsum[0], lane & 15), lb = __shfl(lsum[1], lane & 15); l = 0.5f * ((lane & 16) ? lb : la); }
    WAIT_BAR(0);
#undef DMA_K
#undef DMA_V
#undef DMA2
#undef PERIOD
#undef FIN
#undef EXP2
#undef LSUM
#undef G1
#undef KF
#undef VF
    {
        bool bad = !(l < AT_BIG);
#pragma unroll
        for (int r = 0; r < 16; ++r) bad = bad || !(fabsf(o0[r]) < INFINITY) || !(fabsf(o1[r]) < INFINITY);
        if (__builtin_expect(__any(bad), 0)) {
            o0 = (f32x16){}; o1 = (f32x16){}; l = 0.f; float mrun = -INFINITY;
            for (int kt = 0; kt < TPB; ++kt) {
                if (((kt >> 1) & 1) != g) continue;
                const u32x4* kg = kv + (size_t)kt * 512 + lane;
                f32x16 sc = (f32x16){};
#pragma unroll
                for (int f = 0; f < 4; ++f) sc = MFMA(kg[f * 64], q[f], sc);
                float mx = sc[0];
#pragma unroll
                for (int r = 1; r < 16; ++r) mx = fmaxf(mx, sc[r]);
                mx = fmaxf(mx, xor32(mx));
                const float mn = fmaxf(mrun, mx), f = EX2(mrun - mn);
                mrun = mn; l *= f;
#pragma unroll
                for (int r = 0; r < 16; ++r) { o0[r] *= f; o1[r] *= f; sc[r] = EX2(sc[r] - mn); l += sc[r]; }
                const u32x4 p0 = pack8(sc, 0), p1 = pack8(sc, 1);
                o0 = MFMA(kg[256], p0, o0); o1 = MFMA(kg[256 + 128], p0, o1);
                o0 = MFMA(kg[256 + 64], p1, o0); o1 = MFMA(kg[256 + 192], p1, o1);
            }
            negm = -mrun;
        }
    }
    l += xor32(l);
    float* cf = (float*)lds;
    if (g == 1) {
        float* dst = cf + qi * 34 * 64 + lane;
#pragma unroll
        for (int r = 0; r < 16; ++r) { dst[r * 64] = o0[r]; dst[(16 + r) * 64] = o1[r]; }
        dst[32 * 64] = negm; dst[33 * 64] = l;
    }
    __syncthreads();
    if (g == 0) {
        const float* s = cf + qi * 34 * 64 + lane;
        const float negm1 = s[32 * 64], l1 = s[33 * 64];
        const float nm = fminf(negm, negm1);
        const float f0 = __builtin_amdgcn_exp2f(nm - negm), f1 = __builtin_amdgcn_exp2f(nm - negm1);
        const float inv = __builtin_amdgcn_rcpf(l * f0 + l1 * f1);
        const float c0 = f0 * inv, c1 = f1 * inv;
#pragma unroll
        for (int r = 0; r < 16; ++r) { o0[r] = o0[r] * c0 + s[r * 64] * c1; o1[r] = o1[r] * c0 + s[(16 + r) * 64] * c1; }
        if (qvalid) {
            const int b = bh >> 2, hd = bh & 3;
            u32x4* ao = ws + WS_AT + ((size_t)(b * TPB + qt) * 16 + 4 * hd) * 64 + lane;
            ao[0] = pack8(o0, 0); ao[64] = pack8(o0, 1); ao[128] = pack8(o1, 0); ao[192] = pack8(o1, 1);
        }
    }
    STAMPR((ws + (10u<<20)), 15);
}

#define DBGC ((u32x4*)ws + (9u<<20))
__global__ __launch_bounds__(512, 2) void k_stageC(const u32x4* __restrict__ ws, const uint16_t* __restrict__ dw, uint16_t* __restrict__ out) {
    __shared__ u32x4 lds[32 * 64 + 64 * 64 + 384 + 160 + 512];
    u32x4* fx = lds;
    u32x4* fy = lds + 16 * 64;
    u32x4* fh = lds + 32 * 64;
    float* st0 = (float*)(lds + 32 * 64 + 64 * 64);
    float* st1 = st0 + 512;
    float* st2 = st0 + 1024;
    u32x4* dwl = lds + 32 * 64 + 64 * 64 + 384;
    const int tile = blockIdx.x, tid = threadIdx.x, lane = tid & 63, h = lane >> 5, m = lane & 31;
    const int w = __builtin_amdgcn_readfirstlane(tid >> 6);
    STAMPR(DBGC, 14); STAMP(DBGC, 0);

    const int tt = tile % TPB;
    const int hm = m & 3;
    int toff = hm < 2 ? hm - 2 : 30 + hm;
    const int tl = tt * 32 + toff;
    const bool hvalid = (tl >= 0) && (tl < T_SEQ);
    if (!hvalid) toff = 0;
    const int gtok = tile * 32 + toff;
    const size_t hbase = (size_t)(gtok >> 5) * 16 * 64 + h * 32 + (gtok & 31);

    u32x4 in[8];
    {
        const u32x4* at = ws + WS_AT;
        const u32x4* x1 = ws + WS_X1;
        const size_t mb = (size_t)tile * 16 * 64 + lane;
        in[0] = at[mb + (2 * w) * 64]; in[1] = at[mb + (2 * w + 1) * 64];
        in[2] = at[hbase + (2 * w) * 64]; in[3] = at[hbase + (2 * w + 1) * 64];
        in[4] = x1[mb + (2 * w) * 64]; in[5] = x1[mb + (2 * w + 1) * 64];
        in[6] = x1[hbase + (2 * w) * 64]; in[7] = x1[hbase + (2 * w + 1) * 64];
    }
    u32x4 dwr;
    if (tid < 160) dwr = ((const u32x4*)dw)[tid];
    RAWBAR();
    constexpr int NFX = NFC;
    const u32x4* wp = ws + WS_WC + (size_t)w * NFC * 64 + lane;
    u32x4 ring[32];
    LD8(0); LD8(8);
    f32x16 xv, yv;
    fx[(2 * w) * 64 + lane] = in[0]; fx[(2 * w + 1) * 64 + lane] = in[1];
    fy[(2 * w) * 64 + lane] = in[2]; fy[(2 * w + 1) * 64 + lane] = in[3];
    if (tid < 160) dwl[tid] = dwr;
    unpack16(xv, in[4], in[5]);
    unpack16(yv, in[6], in[7]);
    LD8(16);
    __syncthreads();
    LD8(24);
    STAMP(DBGC, 1);
    {
        f32x16 a = (f32x16){}, b = (f32x16){};
        u32x4 bx[2], by[2];
        bx[0] = fx[lane]; by[0] = fy[lane]; bx[1] = fx[64 + lane]; by[1] = fy[64 + lane];
#pragma unroll
        for (int ks = 0; ks < 16; ++ks) {
            a = MFMA(ring[ks & 31], bx[ks & 1], a);
            b = MFMA(ring[ks & 31], by[ks & 1], b);
            if (ks + 2 < 16) { bx[ks & 1] = fx[(ks + 2) * 64 + lane]; by[ks & 1] = fy[(ks + 2) * 64 + lane]; }
            REFILL(ks, 0);
            SB();
        }
        STAMP(DBGC, 2);
#pragma unroll
        for (int r = 0; r < 16; ++r) { xv[r] = rbf(xv[r] + rbf(a[r])); yv[r] = rbf(yv[r] + rbf(b[r])); }
    }
    float sc, sh, sc2, sh2;
    ln_partial(xv, st0, w, lane);
    ln_partial(yv, st1, w, lane);
    __syncthreads();
    ln_finish(st0, lane, sc, sh);
    ln_finish(st1, lane, sc2, sh2);
    norm_store(xv, sc, sh, fx, w, lane);
    norm_store(yv, sc2, sh2, fy, w, lane);
    __syncthreads();
    STAMP(DBGC, 3);
    {
        f32x16 a = (f32x16){}, b = (f32x16){}, c = (f32x16){}, d = (f32x16){};
        u32x4 bxn = fx[lane], byn = fy[lane];
#pragma unroll
        for (int ks = 0; ks < 16; ++ks) {
            const int F = 16 + 2 * ks;
            const u32x4 bx = bxn, by = byn;
            if (ks + 1 < 16) { bxn = fx[(ks + 1) * 64 + lane]; byn = fy[(ks + 1) * 64 + lane]; }
            a = MFMA(ring[F & 31], bx, a); c = MFMA(ring[F & 31], by, c); REFILL(F, 0);
            b = MFMA(ring[(F + 1) & 31], bx, b); d = MFMA(ring[(F + 1) & 31], by, d); REFILL(F + 1, 0);
            SB();
        }
        STAMP(DBGC, 4);
#pragma unroll
        for (int r = 0; r < 16; ++r) {
            a[r] = a[r] * sigmoidf_(b[r]);
            c[r] = hvalid ? c[r] * sigmoidf_(d[r]) : 0.f;
        }
        f32x4* cs = (f32x4*)fh + w * (4 * 2 * 36);
#pragma unroll
        for (int g = 0; g < 4; ++g) {
            cs[(g * 2 + h) * 36 + m + 2] = (f32x4){a[4 * g], a[4 * g + 1], a[4 * g + 2], a[4 * g + 3]};
            if (m < 4) cs[(g * 2 + h) * 36 + (m < 2 ? m : 32 + m)] = (f32x4){c[4 * g], c[4 * g + 1], c[4 * g + 2], c[4 * g + 3]};
        }
        __syncthreads();
        const uint16_t* dp = (const uint16_t*)dwl + 32 * w + 4 * h;
#pragma unroll
        for (int g = 0; g < 4; ++g) {
            f32x4 acc = (f32x4){0.f, 0.f, 0.f, 0.f};
#pragma unroll
            for (int i = 0; i < 5; ++i) {
                const f32x4 hv = cs[(g * 2 + h) * 36 + m + i];
                const u32x2 t = *(const u32x2*)(dp + i * DM + 8 * g);
                acc[0] += hv[0] * bflo(t[0]); acc[1] += hv[1] * bfhi(t[0]); acc[2] += hv[2] * bflo(t[1]); acc[3] += hv[3] * bfhi(t[1]);
            }
#pragma unroll
            for (int i = 0; i < 4; ++i) a[4 * g + i] = siluf_(acc[i]);
        }
        fx[(2 * w) * 64 + lane] = pack8(a, 0);
        fx[(2 * w + 1) * 64 + lane] = pack8(a, 1);
    }
    __syncthreads();
    STAMP(DBGC, 5);
    {
        f32x16 a;
        GEMM16(a, 48, 64, fx, false);
        STAMP(DBGC, 6);
#pragma unroll
        for (int r = 0; r < 16; ++r) xv[r] = rbf(xv[r] + rbf(a[r]));
    }
    ln_partial(xv, st2, w, lane);
    __syncthreads();
    ln_finish(st2, lane, sc, sh);
    norm_store(xv, sc, sh, fx, w, lane);
    __syncthreads();
    STAMP(DBGC, 7);
    {
        f32x16 acc;
        FFN(acc, 64, DBGC, 8);
#pragma unroll
        for (int r = 0; r < 16; ++r) xv[r] = rbf(__builtin_fmaf(0.5f, rbf(acc[r]), xv[r]));
    }
    ln_partial(xv, st0, w, lane);
    __syncthreads();
    ln_finish(st0, lane, sc, sh);
    {
        uint16_t* op = out + (size_t)(tile * 32 + m) * DM + 32 * w + 4 * h;
#pragma unroll
        for (int g = 0; g < 4; ++g) {
            u32x2 t;
            t[0] = pack2(__builtin_fmaf(xv[4 * g], sc, sh), __builtin_fmaf(xv[4 * g + 1], sc, sh));
            t[1] = pack2(__builtin_fmaf(xv[4 * g + 2], sc, sh), __builtin_fmaf(xv[4 * g + 3], sc, sh));
            *(u32x2*)(op + 8 * g) = t;
        }
    }
    STAMP(DBGC, 11); STAMPR(DBGC, 15);
}

extern "C" void kernel_launch(void* const* d_in, const int* in_sizes, int n_in,
                              void* d_out, int out_size, void* d_ws, size_t ws_size,
                              hipStream_t stream) {
    PackArgs p;
    p.w1a = (const uint16_t*)d_in[1]; p.w1b = (const uint16_t*)d_in[2]; p.wqkv = (const uint16_t*)d_in[3]; p.wo = (const uint16_t*)d_in[4];
    p.wp1 = (const uint16_t*)d_in[5]; p.wp2 = (const uint16_t*)d_in[7]; p.w2a = (const uint16_t*)d_in[8]; p.w2b = (const uint16_t*)d_in[9];
    p.ws = (u32x4*)d_ws;
    u32x4* ws = (u32x4*)d_ws;
    const int nchunk = 8 * (NFA + NFC) * 64;
    k_pack<<<(nchunk + 255) / 256, 256, 0, stream>>>(p);
    k_stageA<<<NTILE, 512, 0, stream>>>((const uint16_t*)d_in[0], ws);
    k_attn<<<256, 512, 0, stream>>>(ws, ws);
    k_stageC<<<NTILE, 512, 0, stream>>>(ws, (const uint16_t*)d_in[6], (uint16_t*)d_out);
}
```

```cpp
#include <hip/hip_runtime.h>
#include <hip/hip_bf16.h>
#include <stdint.h>

typedef short bf16x8 __attribute__((ext_vector_type(8)));
typedef float f32x16 __attribute__((ext_vector_type(16)));
typedef float f32x2 __attribute__((ext_vector_type(2)));
typedef float f32x4 __attribute__((ext_vector_type(4)));
typedef unsigned u32x4 __attribute__((ext_vector_type(4)));
typedef unsigned u32x2 __attribute__((ext_vector_type(2)));
typedef __bf16 bf16v2 __attribute__((ext_vector_type(2)));

#define DI __device__ __forceinline__
#define SB() __builtin_amdgcn_sched_barrier(0)
#define STAMPR(base, i) do {} while (0)
#define STAMP(base, i) do {} while (0)
#define MFMA(a, b, c) __builtin_amdgcn_mfma_f32_32x32x16_bf16(__builtin_bit_cast(bf16x8, (a)), __builtin_bit_cast(bf16x8, (b)), (c), 0, 0, 0)

constexpr int T_SEQ = 4000, DM = 256, NTOK = 8000, NTILE = 250, TPB = 125;
constexpr int NFA = 176;
constexpr int NFC = 192;

constexpr size_t WS_WA = 0;
constexpr size_t WS_WC = WS_WA + (size_t)8 * NFA * 64;
constexpr size_t WS_X1 = WS_WC + (size_t)8 * NFC * 64;
constexpr size_t WS_Q  = WS_X1 + (size_t)NTILE * 16 * 64;
constexpr size_t WS_KV = WS_Q + (size_t)8 * TPB * 4 * 64;
constexpr size_t WS_AT = WS_KV + (size_t)8 * TPB * 8 * 64 + 4096;

DI unsigned pack2(float lo, float hi) {
    f32x2 v = {lo, hi};
    bf16v2 r = __builtin_convertvector(v, bf16v2);
    return __builtin_bit_cast(unsigned, r);
}
DI float bflo(unsigned u) { return __uint_as_float(u << 16); }
DI float bfhi(unsigned u) { return __uint_as_float(u & 0xffff0000u); }
DI u32x4 pack8(const f32x16& a, int s) {
    u32x4 r;
    r[0] = pack2(a[8 * s + 0], a[8 * s + 1]);
    r[1] = pack2(a[8 * s + 2], a[8 * s + 3]);
    r[2] = pack2(a[8 * s + 4], a[8 * s + 5]);
    r[3] = pack2(a[8 * s + 6], a[8 * s + 7]);
    return r;
}
DI void unpack16(f32x16& a, const u32x4& f0, const u32x4& f1) {
#pragma unroll
    for (int i = 0; i < 4; ++i) {
        a[2 * i] = bflo(f0[i]); a[2 * i + 1] = bfhi(f0[i]);
        a[8 + 2 * i] = bflo(f1[i]); a[8 + 2 * i + 1] = bfhi(f1[i]);
    }
}
DI float xor32(float v) { return __shfl_xor(v, 32); }
DI float sigmoidf_(float x) { return __builtin_amdgcn_rcpf(1.0f + __builtin_amdgcn_exp2f(-1.4426950408889634f * x)); }
DI float siluf_(float x) { return x * sigmoidf_(x); }

struct WPtrs {
    const uint16_t* w1a; const uint16_t* w1b; const uint16_t* wqkv; const uint16_t* wo;
    const uint16_t* wp1; const uint16_t* wp2; const uint16_t* w2a; const uint16_t* w2b;
};

DI u32x4 pack_chunk(const WPtrs& p, int gid) {
    const int lane = gid & 63, fg = gid >> 6;
    const uint16_t* W; int N, nt, ks;
    const int w = fg / NFC, f = fg % NFC;
    if (f < 16)       { W = p.wo; N = 256; ks = f; nt = w; }
    else if (f < 48)  { const int g = f - 16; W = p.wp1; N = 512; ks = g >> 1; nt = w + 8 * (g & 1); }
    else if (f < 64)  { W = p.wp2; N = 256; ks = f - 48; nt = w; }
    else if (f < 128) { const int g = f - 64; W = p.w2a; N = 1024; ks = g & 15; nt = 4 * w + (g >> 4); }
    else              { W = p.w2b; N = 256; ks = f - 128; nt = w; }
    const int r = lane & 31, h = lane >> 5;
    const uint16_t* src = W + (size_t)(16 * ks + 4 * h) * N + 32 * nt + r;
    unsigned e[8];
#pragma unroll
    for (int j = 0; j < 8; ++j) e[j] = src[(size_t)(8 * (j >> 2) + (j & 3)) * N];
    u32x4 o;
#pragma unroll
    for (int i = 0; i < 4; ++i) o[i] = e[2 * i] | (e[2 * i + 1] << 16);
    return o;
}

DI void glds16(const void* g, unsigned lds_base) {
    unsigned sv;
    asm volatile("s_mov_b32 %0, m0\n\ts_mov_b32 m0, %2\n\ts_nop 0\n\tglobal_load_lds_dwordx4 %1, off\n\ts_mov_b32 m0, %0"
                 : "=&s"(sv) : "v"(g), "s"(lds_base) : "memory");
}
template <int NI> DI void l2_warm(const u32x4* base, int total_chunks, int rank, int w, int lane, unsigned lds_scratch) {
#pragma unroll
    for (int i = 0; i < NI; ++i) {
        int c = ((rank * NI + i) * 8 + w) * 64;
        c = c + 64 <= total_chunks ? c : total_chunks - 64;
        glds16(base + c + lane, lds_scratch);
    }
}
#define WLOAD(F) ring[(F) & 31] = wp[(F) * 64]
#define REFILL(F, PE) do { if ((F) < 16) WLOAD((F) + 16); if ((F) + 32 < (NFX)) WLOAD((F) + 32); } while (0)
#define LD8(F) do { SB(); _Pragma("unroll") for (int i_ = 0; i_ < 8; ++i_) WLOAD((F) + i_); SB(); } while (0)
#define RAWBAR() do { SB(); __builtin_amdgcn_s_barrier(); SB(); } while (0)

DI void ln_partial(const f32x16& v, float* st, int w, int lane) {
    float s = 0.f, q = 0.f;
#pragma unroll
    for (int i = 0; i < 16; ++i) { s += v[i]; q = __builtin_fmaf(v[i], v[i], q); }
    auto r = __builtin_amdgcn_permlane32_swap(__float_as_uint(s), __float_as_uint(q), false, false);
    st[w * 64 + lane] = __uint_as_float(r[0]) + __uint_as_float(r[1]);
}
DI void ln_finish(const float* st, int lane, float& scale, float& shift) {
    const float* p = st + (lane & 31);
    float s = 0.f, q = 0.f;
#pragma unroll
    for (int i = 0; i < 8; ++i) { s += p[i * 64]; q += p[i * 64 + 32]; }
    const float m = s * (1.0f / 256.0f);
    const float var = fmaxf(q * (1.0f / 256.0f) - m * m, 0.f);
    scale = __builtin_amdgcn_rsqf(var + 1e-5f);
    shift = -m * scale;
}
DI void norm_store(const f32x16& v, float scale, float shift, u32x4* dst  , int w, int lane) {
    f32x16 n;
#pragma unroll
    for (int i = 0; i < 16; ++i) n[i] = __builtin_fmaf(v[i], scale, shift);
    dst[(2 * w) * 64 + lane] = pack8(n, 0);
    dst[(2 * w + 1) * 64 + lane] = pack8(n, 1);
}
DI float rbf(float x) { return bflo(pack2(x, 0.f) & 0xffffu); }

#define GEMM16(acc, F0, PE, fxp, SWAP) do { \
        acc = (f32x16){}; u32x4 bq_[4]; \
        _Pragma("unroll") for (int i_ = 0; i_ < 4; ++i_) bq_[i_] = (fxp)[i_ * 64 + lane]; \
        _Pragma("unroll") for (int ks_ = 0; ks_ < 16; ++ks_) { \
            if (SWAP) acc = MFMA(bq_[ks_ & 3], ring[((F0) + ks_) & 31], acc); else acc = MFMA(ring[((F0) + ks_) & 31], bq_[ks_ & 3], acc); \
            if (ks_ + 4 < 16) bq_[ks_ & 3] = (fxp)[(ks_ + 4) * 64 + lane]; \
            REFILL((F0) + ks_, PE); SB(); } } while (0)

#define FFN(acc, F0, DBG, ST) do { \
        _Pragma("unroll") for (int t_ = 0; t_ < 4; ++t_) { \
            f32x16 a_; GEMM16(a_, (F0) + 16 * t_, (F0) + 64, fx, false); \
            if (t_ == 3) { STAMP(DBG, ST); } \
            _Pragma("unroll") for (int r_ = 0; r_ < 8; ++r_) a_[r_] = siluf_(a_[r_]); \
            fh[((4 * w + t_) * 2 + 0) * 64 + lane] = pack8(a_, 0); \
            _Pragma("unroll") for (int r_ = 8; r_ < 16; ++r_) a_[r_] = siluf_(a_[r_]); \
            fh[((4 * w + t_) * 2 + 1) * 64 + lane] = pack8(a_, 1); } \
        __syncthreads(); \
        STAMP(DBG, ST + 1); \
        acc = (f32x16){}; u32x4 bq_[4]; \
        _Pragma("unroll") for (int i_ = 0; i_ < 4; ++i_) bq_[i_] = fh[i_ * 64 + lane]; \
        _Pragma("unroll") for (int ks_ = 0; ks_ < 64; ++ks_) { \
            acc = MFMA(ring[((F0) + 64 + ks_) & 31], bq_[ks_ & 3], acc); \
            if (ks_ + 4 < 64) bq_[ks_ & 3] = fh[(ks_ + 4) * 64 + lane]; \
            REFILL((F0) + 64 + ks_, (F0) + 128); SB(); } \
        STAMP(DBG, ST + 2); } while (0)

#define DBGA (ws + (8u<<20))
typedef short s16x4 __attribute__((ext_vector_type(4)));
typedef __attribute__((address_space(3))) const char* lds_cptr;
DI u32x2 trread(lds_cptr p) { return __builtin_bit_cast(u32x2, __builtin_amdgcn_ds_read_tr16_b64_v4i16((__attribute__((address_space(3))) s16x4*)p)); }
DI void glds16s(const void* sbase, unsigned voff, unsigned lds_base) {
    unsigned sv;
    asm volatile("s_mov_b32 %0, m0\n\ts_mov_b32 m0, %3\n\ts_nop 0\n\tglobal_load_lds_dwordx4 %1, %2\n\ts_mov_b32 m0, %0"
                 : "=&s"(sv) : "v"(voff), "s"(sbase), "s"(lds_base) : "memory");
}
constexpr int A_LDS_FX = 0, A_LDS_FH = 16384, A_LDS_ST = 81920, A_LDS_RING = 86016, A_LDS_BYTES = 86016 + 65536;
constexpr int SD = 8;

__global__ __launch_bounds__(512, 2) void k_stageA(const uint16_t* __restrict__ x, WPtrs wt, u32x4* __restrict__ ws) {
    __shared__ __attribute__((aligned(16))) char smem[A_LDS_BYTES];
    const int tile = blockIdx.x, tid = threadIdx.x, lane = tid & 63, h = lane >> 5, m = lane & 31;
    if (tile >= NTILE) {
        for (int c = (tile - NTILE) * 512 + tid; c < 8 * NFC * 64; c += 6 * 512) ws[WS_WC + c] = pack_chunk(wt, c);
        return;
    }
    u32x4* fx = (u32x4*)(smem + A_LDS_FX);
    u32x4* fh = (u32x4*)(smem + A_LDS_FH);
    float* st0 = (float*)(smem + A_LDS_ST);
    float* st1 = st0 + 512;
    const int w = __builtin_amdgcn_readfirstlane(tid >> 6);
    STAMPR(DBGA, 14); STAMP(DBGA, 0);
    u32x2 xr[4];
    {
        const uint16_t* xp = x + (size_t)(tile * 32 + m) * DM + 32 * w + 4 * h;
#pragma unroll
        for (int g = 0; g < 4; ++g) xr[g] = *(const u32x2*)(xp + 8 * g);
    }
    RAWBAR();
    const unsigned ringb = (unsigned)(uintptr_t)smem + A_LDS_RING + w * 8192;
    const lds_cptr ringp = (lds_cptr)smem + A_LDS_RING + w * 8192;
    const unsigned vo128 = (lane >> 3) * 2048 + (((lane & 7) ^ ((lane >> 4 & 1) << 2)) * 16);
    const unsigned vo512 = (lane >> 2) * 512 + (lane & 3) * 16, vo1536 = (lane >> 2) * 1536 + (lane & 3) * 16;
    const int G = lane >> 4, q4 = (lane >> 2) & 3, p4 = lane & 3;
    const int va64 = (4 * (G >> 1) + q4) * 64 + (G & 1) * 32 + p4 * 8;
    const int va128_0 = (4 * (G >> 1) + q4) * 128 + ((q4 >> 1) * 64) + (G & 1) * 32 + p4 * 8, va128_1 = va128_0 ^ 64;
    const char* b1a = (const char*)wt.w1a + w * 256;
    const char* b1b = (const char*)wt.w1b + w * 64;
    const char* bqk = (const char*)wt.wqkv + w * 64;
#define ISSUE(F) do { if ((F) < NFA) { \
        if ((F) < 64) glds16s(b1a + (16 * (((F) >> 1) & 15) + 8 * ((F) & 1)) * 2048 + ((F) >> 5) * 128, vo128, ringb + ((F) & 7) * 1024); \
        else if ((F) < 128) glds16s(b1b + ((F) - 64) * 16 * 512, vo512, ringb + ((F) & 7) * 1024); \
        else glds16s(bqk + (((F) - 128) & 15) * 16 * 1536 + (((F) - 128) >> 4) * 512, vo1536, ringb + ((F) & 7) * 1024); } } while (0)
#define TRREAD(F) do { if ((F) < NFA) { u32x2 lo_, hi_; \
        if ((F) < 64) { const int va_ = ((F) & 1) ? va128_1 : va128_0; lo_ = trread(ringp + va_ + (((F) & 6) * 1024)); hi_ = trread(ringp + va_ + ((((F) & 6) + 1) * 1024)); } \
        else { lo_ = trread(ringp + va64 + ((F) & 7) * 1024); hi_ = trread(ringp + va64 + ((F) & 7) * 1024 + 512); } \
        ar[(F) & 15] = (u32x4){lo_[0], lo_[1], hi_[0], hi_[1]}; } } while (0)
#define WAITV(n) asm volatile("s_waitcnt vmcnt(%0)" :: "n"(n) : "memory")
#define PRE(F) do { if ((F) + SD < NFA) { WAITV(((F) + SD + 6 <= NFA ? 4 : NFA - (F) - SD - 2)); TRREAD((F) + SD); TRREAD((F) + SD + 1); } SB(); } while (0)
#define POST(F) do { SB(); if ((F) + SD + 6 < NFA) { asm volatile("s_waitcnt lgkmcnt(4)" ::: "memory"); ISSUE((F) + SD + 6); ISSUE((F) + SD + 7); } SB(); } while (0)
    u32x4 ar[16];
#pragma unroll
    for (int i = 0; i < 8; ++i) ISSUE(i);
    SB();
    f32x16 xv;
#pragma unroll
    for (int g = 0; g < 4; ++g) { xv[4 * g + 0] = bflo(xr[g][0]); xv[4 * g + 1] = bfhi(xr[g][0]); xv[4 * g + 2] = bflo(xr[g][1]); xv[4 * g + 3] = bfhi(xr[g][1]); }
    float sc, sh;
    ln_partial(xv, st0, w, lane);
#pragma unroll
    for (int i = 0; i < 8; i += 2) {
        WAITV(6); TRREAD(i); TRREAD(i + 1); SB();
        if (i < 6) { asm volatile("s_waitcnt lgkmcnt(0)" ::: "memory"); ISSUE(i + 8); ISSUE(i + 9); SB(); }
        if (i == 2) __syncthreads();
    }
    ln_finish(st0, lane, sc, sh);
    norm_store(xv, sc, sh, fx, w, lane);
    __syncthreads();
    STAMP(DBGA, 1);
#pragma unroll
    for (int pp = 0; pp < 2; ++pp) {
        f32x16 a0 = (f32x16){}, a1 = (f32x16){};
        u32x4 bq[2];
        bq[0] = fx[lane]; bq[1] = fx[64 + lane];
#pragma unroll
        for (int ks = 0; ks < 16; ++ks) {
            const int F = pp * 32 + 2 * ks;
            const u32x4 b = bq[ks & 1];
            if (ks + 2 < 16) bq[ks & 1] = fx[(ks + 2) * 64 + lane];
            PRE(F);
            a0 = MFMA(ar[F & 15], b, a0); a1 = MFMA(ar[(F + 1) & 15], b, a1);
            POST(F);
        }
        if (pp == 1) STAMP(DBGA, 2);
#pragma unroll
        for (int r = 0; r < 16; ++r) { a0[r] = siluf_(a0[r]); a1[r] = siluf_(a1[r]); }
        fh[((4 * w + 2 * pp) * 2 + 0) * 64 + lane] = pack8(a0, 0);
        fh[((4 * w + 2 * pp) * 2 + 1) * 64 + lane] = pack8(a0, 1);
        fh[((4 * w + 2 * pp) * 2 + 2) * 64 + lane] = pack8(a1, 0);
        fh[((4 * w + 2 * pp) * 2 + 3) * 64 + lane] = pack8(a1, 1);
    }
    __syncthreads();
    STAMP(DBGA, 3);
    {
        f32x16 acc = (f32x16){};
        u32x4 bq[4];
#pragma unroll
        for (int i = 0; i < 4; ++i) bq[i] = fh[i * 64 + lane];
#pragma unroll
        for (int ks = 0; ks < 64; ks += 2) {
            const int F = 64 + ks;
            const u32x4 b0 = bq[ks & 3], b1 = bq[(ks + 1) & 3];
            if (ks + 4 < 64) { bq[ks & 3] = fh[(ks + 4) * 64 + lane]; bq[(ks + 1) & 3] = fh[(ks + 5) * 64 + lane]; }
            PRE(F);
            acc = MFMA(ar[F & 15], b0, acc); acc = MFMA(ar[(F + 1) & 15], b1, acc);
            POST(F);
        }
        STAMP(DBGA, 4);
#pragma unroll
        for (int r = 0; r < 16; ++r) xv[r] = rbf(__builtin_fmaf(0.5f, rbf(acc[r]), xv[r]));
    }
    ln_partial(xv, st1, w, lane);
    {
        u32x4* xo = ws + WS_X1 + (size_t)tile * 16 * 64;
        xo[(2 * w) * 64 + lane] = pack8(xv, 0);
        xo[(2 * w + 1) * 64 + lane] = pack8(xv, 1);
    }
    __syncthreads();
    ln_finish(st1, lane, sc, sh);
    norm_store(xv, sc, sh, fx, w, lane);
    __syncthreads();
    STAMP(DBGA, 5);
    {
        const int b = tile / TPB, tt = tile % TPB, bh = b * 4 + (w >> 1), t2 = w & 1;
        u32x4* qo = ws + WS_Q + ((size_t)(bh * TPB + tt) * 4 + 2 * t2) * 64 + lane;
        u32x4* ko = ws + WS_KV + ((size_t)(bh * TPB + tt) * 8 + 2 * t2) * 64 + lane;
#pragma unroll
        for (int t = 0; t < 3; ++t) {
            f32x16 a = (f32x16){};
            u32x4 bq[4];
#pragma unroll
            for (int i = 0; i < 4; ++i) bq[i] = fx[i * 64 + lane];
#pragma unroll
            for (int ks = 0; ks < 16; ks += 2) {
                const int F = 128 + 16 * t + ks;
                const u32x4 b0 = bq[ks & 3], b1 = bq[(ks + 1) & 3];
                if (ks + 4 < 16) { bq[ks & 3] = fx[(ks + 4) * 64 + lane]; bq[(ks + 1) & 3] = fx[(ks + 5) * 64 + lane]; }
                PRE(F);
                if (t < 2) { a = MFMA(ar[F & 15], b0, a); a = MFMA(ar[(F + 1) & 15], b1, a); }
                else       { a = MFMA(b0, ar[F & 15], a); a = MFMA(b1, ar[(F + 1) & 15], a); }
                POST(F);
            }
            if (t == 0) {
                const float qs = 0.125f * 1.4426950408889634f;
#pragma unroll
                for (int r = 0; r < 16; ++r) a[r] *= qs;
                qo[0] = pack8(a, 0); qo[64] = pack8(a, 1);
            } else if (t == 1) { ko[0] = pack8(a, 0); ko[64] = pack8(a, 1); }
            else { STAMP(DBGA, 6); ko[4 * 64] = pack8(a, 0); ko[5 * 64] = pack8(a, 1); }
        }
    }
    STAMP(DBGA, 7); STAMPR(DBGA, 15);
#undef ISSUE
#undef TRREAD
#undef PRE
#undef POST
}

#define WAIT_BAR(N) asm volatile("s_waitcnt vmcnt(" #N ") lgkmcnt(0)\n\ts_barrier" ::: "memory")

constexpr int AT_SLOT = 1024;
constexpr float AT_BIG = 1.0e30f;
#define ABL_NODMA 0
#define ABL_NOBAR 0
#define ABL_NOLDS 0
#define ABL_NOEXP 0
#define ABL_NOADD 0
#define PIN(x) asm volatile("" : "+v"(x))
DI float vadd(float a, float b) { float r; asm("v_add_f32 %0, %1, %2" : "=v"(r) : "v"(a), "v"(b)); return r; }
#define EX2(x) __builtin_amdgcn_exp2f(x)

__global__ __launch_bounds__(512, 2) void k_attn(const u32x4* __restrict__ ws_in, u32x4* __restrict__ ws) {
    __shared__ u32x4 lds[4 * 2048];
    const int tid = threadIdx.x, lane = tid & 63;
    const int w = __builtin_amdgcn_readfirstlane(tid >> 6);
    STAMP((ws + (10u<<20)), 0);
    const int bh = blockIdx.x & 7, qb = blockIdx.x >> 3, qi = w & 3, g = w >> 2;
    int qt = 4 * qb + qi; const bool qvalid = qt < TPB; if (!qvalid) qt = TPB - 1;
    const u32x4* kv = ws_in + WS_KV + (size_t)bh * TPB * 8 * 64;
    const unsigned lds0 = (unsigned)(uintptr_t)lds;
    const u32x4* src = kv + (w >> 1) * 512 + (w & 1) * 128 + lane;
    const u32x4* src31 = kv + 124 * 512 + (w & 1) * 128 + lane;
    const unsigned dstk = lds0 + (w >> 1) * 4096 + (w & 1) * 2048;
    const u32x4* srcw = kv + qi * 512 + lane;
    const u32x4* srcw31 = kv + 124 * 512 + lane;
    const unsigned dstw = lds0 + qi * 4096;
#define DMA2(sp, dst) do { glds16((sp), (unsigned)__builtin_amdgcn_readfirstlane(dst)); glds16((sp) + 64, (unsigned)__builtin_amdgcn_readfirstlane((dst) + 1024)); } while (0)
#define DMA_K(step) do { const int st_ = (step); const u32x4* s_ = st_ < 31 ? src + (size_t)st_ * 2048 : src31; DMA2(s_, dstk + (st_ & 3) * 32768); } while (0)
#define DMA_V(step) do { const int st_ = (step); const u32x4* s_ = st_ < 31 ? src + (size_t)st_ * 2048 : src31; DMA2(s_ + 256, dstk + (st_ & 3) * 32768 + 16384); } while (0)
    DMA_K(0); DMA_V(0); DMA_K(1);
    u32x4 q[4];
    {
        const u32x4* qp = ws_in + WS_Q + (size_t)(bh * TPB + qt) * 4 * 64 + lane;
#pragma unroll
        for (int f = 0; f < 4; ++f) q[f] = qp[f * 64];
    }
    DMA_K(2); DMA_V(1);
    const char* lbase = (const char*)lds + (2 * g) * 4096 + lane * 16;
#define KF(off, i) (*(const u32x4*)(lbase + (off) + (i) * 1024))
#define VF(off, i) (*(const u32x4*)(lbase + (off) + 16384 + (i) * 1024))
    f32x16 o0 = (f32x16){}, o1 = (f32x16){};
    float negm;
    f32x4 lsum = (f32x4){0.f, 0.f, 0.f, 0.f};
    u32x4 onesA;
    { const unsigned o_ = ((lane & 15) == ((lane >> 4) & 1)) ? 0x3f803f80u : 0u; onesA = (u32x4){o_, o_, o_, o_}; }
    f32x16 cm, sA, sB;
    u32x4 kf[4], vf[4], pA0, pA1, pB0, pB1;
    WAIT_BAR(4);
    {
#pragma unroll
        for (int i = 0; i < 4; ++i) kf[i] = KF(0, i);
        const f32x16 z = (f32x16){};
        sA = MFMA(kf[0], q[0], z); sA = MFMA(kf[1], q[1], sA); sA = MFMA(kf[2], q[2], sA); sA = MFMA(kf[3], q[3], sA);
#pragma unroll
        for (int i = 0; i < 4; ++i) kf[i] = KF(4096, i);
        float mx = sA[0];
#pragma unroll
        for (int r = 1; r < 16; ++r) mx = fmaxf(mx, sA[r]);
        mx = fmaxf(mx, xor32(mx));
        negm = -mx;
#pragma unroll
        for (int r = 0; r < 16; ++r) { sA[r] += negm; cm[r] = negm; sB[r] = 0.f; }
        pB0 = (u32x4){0u, 0u, 0u, 0u}; pB1 = pB0;
    }
#define FIN(S, i, PK, e) do { PK[e] = pack2(S[i], S[(i) + 1]); PIN(PK); } while (0)
#define EXP2(S, i) do { S[i] = EX2(S[i]); S[(i) + 1] = EX2(S[(i) + 1]); PIN(S); } while (0)
#define LSUM(PK) do { lsum = __builtin_amdgcn_mfma_f32_16x16x32_bf16(__builtin_bit_cast(bf16x8, onesA), __builtin_bit_cast(bf16x8, PK), lsum, 0, 0, 0); PIN(lsum); } while (0)
#define PERIOD(SC, SN, PP0, PP1, PC0, PC1, koff, voff, D0, D1) do { \
        vf[0] = VF(voff, 0); FIN(SN, 14, PP1, 3); SB(); SN = MFMA(kf[0], q[0], cm); PIN(SN); EXP2(SC, 0); SB(); \
        vf[2] = VF(voff, 2); SB(); SN = MFMA(kf[1], q[1], SN); PIN(SN); EXP2(SC, 2); FIN(SC, 0, PC0, 0); SB(); \
        vf[1] = VF(voff, 1); SB(); SN = MFMA(kf[2], q[2], SN); PIN(SN); EXP2(SC, 4); FIN(SC, 2, PC0, 1); SB(); D0; SB(); \
        vf[3] = VF(voff, 3); SB(); SN = MFMA(kf[3], q[3], SN); PIN(SN); EXP2(SC, 6); FIN(SC, 4, PC0, 2); SB(); \
        kf[0] = KF(koff, 0); SB(); o0 = MFMA(vf[0], PP0, o0); PIN(o0); EXP2(SC, 8); FIN(SC, 6, PC0, 3); SB(); LSUM(PP0); SB(); \
        kf[1] = KF(koff, 1); SB(); o1 = MFMA(vf[2], PP0, o1); PIN(o1); EXP2(SC, 10); FIN(SC, 8, PC1, 0); SB(); \
        kf[2] = KF(koff, 2); SB(); o0 = MFMA(vf[1], PP1, o0); PIN(o0); EXP2(SC, 12); FIN(SC, 10, PC1, 1); SB(); LSUM(PP1); SB(); D1; SB(); \
        kf[3] = KF(koff, 3); SB(); o1 = MFMA(vf[3], PP1, o1); PIN(o1); EXP2(SC, 14); FIN(SC, 12, PC1, 2); SB(); \
    } while (0)
    int tk0 = 0, tk1 = 0, tk2 = 0, acc_c = 0, acc_w = 0;
#define TICK(x) do {} while (0)
    int vprev = 0;
    for (int i = 0; i < 31; ++i) {
        const int scur = (i & 3) * 32768, snext = ((i + 1) & 3) * 32768;
        const int sk = i + 3 < 32 ? i + 3 : 31, sv = i + 2 < 32 ? i + 2 : 31;
        const u32x4* pk_ = sk < 31 ? srcw + (size_t)sk * 2048 : srcw31;
        const u32x4* pv_ = (sv < 31 ? srcw + (size_t)sv * 2048 : srcw31) + 256;
        const unsigned dk_ = dstw + (sk & 3) * 32768, dv_ = dstw + (sv & 3) * 32768 + 16384;
#define G2(p, d) do { if (!ABL_NODMA && g == 0) { glds16((p), (unsigned)__builtin_amdgcn_readfirstlane(d)); glds16((p) + 64, (unsigned)__builtin_amdgcn_readfirstlane((d) + 1024)); } } while (0)
        SB();
        PERIOD(sA, sB, pB0, pB1, pA0, pA1, snext, vprev, G2(pk_, dk_), G2(pk_ + 128, dk_ + 2048));
        PERIOD(sB, sA, pA0, pA1, pB0, pB1, snext + 4096, scur, G2(pv_, dv_), G2(pv_ + 128, dv_ + 2048));
        vprev = scur + 4096;
        TICK(tk1); if (!ABL_NOBAR) { if (g) WAIT_BAR(0); else WAIT_BAR(8); } TICK(tk2); acc_c += tk1 - tk0; acc_w += tk2 - tk1; tk0 = tk2;
    }
    {
        if (g == 1) {
#pragma unroll
            for (int r = 0; r < 16; ++r) sA[r] = -INFINITY;
        }
        PERIOD(sA, sB, pB0, pB1, pA0, pA1, 0, vprev, (void)0, (void)0);
#pragma unroll
        for (int r = 0; r < 16; ++r) sB[r] = -INFINITY;
        PERIOD(sB, sA, pA0, pA1, pB0, pB1, 0, 3 * 32768, (void)0, (void)0);
    }
    float l;
    { const float la = __shfl(lsum[0], lane & 15), lb = __shfl(lsum[1], lane & 15); l = 0.5f * ((lane & 16) ? lb : la); }
    WAIT_BAR(0);
#undef DMA_K
#undef DMA_V
#undef DMA2
#undef PERIOD
#undef FIN
#undef EXP2
#undef LSUM
#undef G2
#undef KF
#undef VF
    {
        bool bad = !(l < AT_BIG);
#pragma unroll
        for (int r = 0; r < 16; ++r) bad = bad || !(fabsf(o0[r]) < INFINITY) || !(fabsf(o1[r]) < INFINITY);
        if (__builtin_expect(__any(bad), 0)) {
            o0 = (f32x16){}; o1 = (f32x16){}; l = 0.f; float mrun = -INFINITY;
            for (int kt = 0; kt < TPB; ++kt) {
                if (((kt >> 1) & 1) != g) continue;
                const u32x4* kg = kv + (size_t)kt * 512 + lane;
                f32x16 sc = (f32x16){};
#pragma unroll
                for (int f = 0; f < 4; ++f) sc = MFMA(kg[f * 64], q[f], sc);
                float mx = sc[0];
#pragma unroll
                for (int r = 1; r < 16; ++r) mx = fmaxf(mx, sc[r]);
                mx = fmaxf(mx, xor32(mx));
                const float mn = fmaxf(mrun, mx), f = EX2(mrun - mn);
                mrun = mn; l *= f;
#pragma unroll
                for (int r = 0; r < 16; ++r) { o0[r] *= f; o1[r] *= f; sc[r] = EX2(sc[r] - mn); l += sc[r]; }
                const u32x4 p0 = pack8(sc, 0), p1 = pack8(sc, 1);
                o0 = MFMA(kg[256], p0, o0); o1 = MFMA(kg[256 + 128], p0, o1);
                o0 = MFMA(kg[256 + 64], p1, o0); o1 = MFMA(kg[256 + 192], p1, o1);
            }
            negm = -mrun;
        }
    }
    l += xor32(l);
    float* cf = (float*)lds;
    if (g == 1) {
        float* dst = cf + qi * 34 * 64 + lane;
#pragma unroll
        for (int r = 0; r < 16; ++r) { dst[r * 64] = o0[r]; dst[(16 + r) * 64] = o1[r]; }
        dst[32 * 64] = negm; dst[33 * 64] = l;
    }
    __syncthreads();
    if (g == 0) {
        const float* s = cf + qi * 34 * 64 + lane;
        const float negm1 = s[32 * 64], l1 = s[33 * 64];
        const float nm = fminf(negm, negm1);
        const float f0 = __builtin_amdgcn_exp2f(nm - negm), f1 = __builtin_amdgcn_exp2f(nm - negm1);
        const float inv = __builtin_amdgcn_rcpf(l * f0 + l1 * f1);
        const float c0 = f0 * inv, c1 = f1 * inv;
#pragma unroll
        for (int r = 0; r < 16; ++r) { o0[r] = o0[r] * c0 + s[r * 64] * c1; o1[r] = o1[r] * c0 + s[(16 + r) * 64] * c1; }
        if (qvalid) {
            const int b = bh >> 2, hd = bh & 3;
            u32x4* ao = ws + WS_AT + ((size_t)(b * TPB + qt) * 16 + 4 * hd) * 64 + lane;
            ao[0] = pack8(o0, 0); ao[64] = pack8(o0, 1); ao[128] = pack8(o1, 0); ao[192] = pack8(o1, 1);
        }
    }
    STAMPR((ws + (10u<<20)), 15);
}

#define DBGC ((u32x4*)ws + (9u<<20))
__global__ __launch_bounds__(512, 2) void k_stageC(const u32x4* __restrict__ ws, const uint16_t* __restrict__ dw, uint16_t* __restrict__ out) {
    __shared__ u32x4 lds[32 * 64 + 64 * 64 + 384 + 160 + 512];
    u32x4* fx = lds;
    u32x4* fy = lds + 16 * 64;
    u32x4* fh = lds + 32 * 64;
    float* st0 = (float*)(lds + 32 * 64 + 64 * 64);
    float* st1 = st0 + 512;
    float* st2 = st0 + 1024;
    u32x4* dwl = lds + 32 * 64 + 64 * 64 + 384;
    const int tile = blockIdx.x, tid = threadIdx.x, lane = tid & 63, h = lane >> 5, m = lane & 31;
    const int w = __builtin_amdgcn_readfirstlane(tid >> 6);
    STAMPR(DBGC, 14); STAMP(DBGC, 0);

    const int tt = tile % TPB;
    const int hm = m & 3;
    int toff = hm < 2 ? hm - 2 : 30 + hm;
    const int tl = tt * 32 + toff;
    const bool hvalid = (tl >= 0) && (tl < T_SEQ);
    if (!hvalid) toff = 0;
    const int gtok = tile * 32 + toff;
    const size_t hbase = (size_t)(gtok >> 5) * 16 * 64 + h * 32 + (gtok & 31);

    u32x4 in[8];
    {
        const u32x4* at = ws + WS_AT;
        const u32x4* x1 = ws + WS_X1;
        const size_t mb = (size_t)tile * 16 * 64 + lane;
        in[0] = at[mb + (2 * w) * 64]; in[1] = at[mb + (2 * w + 1) * 64];
        in[2] = at[hbase + (2 * w) * 64]; in[3] = at[hbase + (2 * w + 1) * 64];
        in[4] = x1[mb + (2 * w) * 64]; in[5] = x1[mb + (2 * w + 1) * 64];
        in[6] = x1[hbase + (2 * w) * 64]; in[7] = x1[hbase + (2 * w + 1) * 64];
    }
    u32x4 dwr;
    if (tid < 160) dwr = ((const u32x4*)dw)[tid];
    RAWBAR();
    constexpr int NFX = NFC;
    const u32x4* wp = ws + WS_WC + (size_t)w * NFC * 64 + lane;
    u32x4 ring[32];
    LD8(0); LD8(8);
    f32x16 xv, yv;
    fx[(2 * w) * 64 + lane] = in[0]; fx[(2 * w + 1) * 64 + lane] = in[1];
    fy[(2 * w) * 64 + lane] = in[2]; fy[(2 * w + 1) * 64 + lane] = in[3];
    if (tid < 160) dwl[tid] = dwr;
    unpack16(xv, in[4], in[5]);
    unpack16(yv, in[6], in[7]);
    __syncthreads();
    STAMP(DBGC, 1);
    {
        f32x16 a = (f32x16){}, b = (f32x16){};
        u32x4 bx[2], by[2];
        bx[0] = fx[lane]; by[0] = fy[lane]; bx[1] = fx[64 + lane]; by[1] = fy[64 + lane];
#pragma unroll
        for (int ks = 0; ks < 16; ++ks) {
            a = MFMA(ring[ks & 31], bx[ks & 1], a);
            b = MFMA(ring[ks & 31], by[ks & 1], b);
            if (ks + 2 < 16) { bx[ks & 1] = fx[(ks + 2) * 64 + lane]; by[ks & 1] = fy[(ks + 2) * 64 + lane]; }
            REFILL(ks, 0);
            SB();
        }
        STAMP(DBGC, 2);
#pragma unroll
        for (int r = 0; r < 16; ++r) { xv[r] = rbf(xv[r] + rbf(a[r])); yv[r] = rbf(yv[r] + rbf(b[r])); }
    }
    float sc, sh, sc2, sh2;
    ln_partial(xv, st0, w, lane);
    ln_partial(yv, st1, w, lane);
    __syncthreads();
    ln_finish(st0, lane, sc, sh);
    ln_finish(st1, lane, sc2, sh2);
    norm_store(xv, sc, sh, fx, w, lane);
    norm_store(yv, sc2, sh2, fy, w, lane);
    __syncthreads();
    STAMP(DBGC, 3);
    {
        f32x16 a = (f32x16){}, b = (f32x16){}, c = (f32x16){}, d = (f32x16){};
        u32x4 bxn = fx[lane], byn = fy[lane];
#pragma unroll
        for (int ks = 0; ks < 16; ++ks) {
            const int F = 16 + 2 * ks;
            const u32x4 bx = bxn, by = byn;
            if (ks + 1 < 16) { bxn = fx[(ks + 1) * 64 + lane]; byn = fy[(ks + 1) * 64 + lane]; }
            a = MFMA(ring[F & 31], bx, a); c = MFMA(ring[F & 31], by, c); REFILL(F, 0);
            b = MFMA(ring[(F + 1) & 31], bx, b); d = MFMA(ring[(F + 1) & 31], by, d); REFILL(F + 1, 0);
            SB();
        }
        STAMP(DBGC, 4);
#pragma unroll
        for (int r = 0; r < 16; ++r) {
            a[r] = a[r] * sigmoidf_(b[r]);
            c[r] = hvalid ? c[r] * sigmoidf_(d[r]) : 0.f;
        }
        f32x4* cs = (f32x4*)fh + w * (4 * 2 * 36);
#pragma unroll
        for (int g = 0; g < 4; ++g) {
            cs[(g * 2 + h) * 36 + m + 2] = (f32x4){a[4 * g], a[4 * g + 1], a[4 * g + 2], a[4 * g + 3]};
            if (m < 4) cs[(g * 2 + h) * 36 + (m < 2 ? m : 32 + m)] = (f32x4){c[4 * g], c[4 * g + 1], c[4 * g + 2], c[4 * g + 3]};
        }
        __syncthreads();
        const uint16_t* dp = (const uint16_t*)dwl + 32 * w + 4 * h;
#pragma unroll
        for (int g = 0; g < 4; ++g) {
            f32x4 acc = (f32x4){0.f, 0.f, 0.f, 0.f};
#pragma unroll
            for (int i = 0; i < 5; ++i) {
                const f32x4 hv = cs[(g * 2 + h) * 36 + m + i];
                const u32x2 t = *(const u32x2*)(dp + i * DM + 8 * g);
                acc[0] += hv[0] * bflo(t[0]); acc[1] += hv[1] * bfhi(t[0]); acc[2] += hv[2] * bflo(t[1]); acc[3] += hv[3] * bfhi(t[1]);
            }
#pragma unroll
            for (int i = 0; i < 4; ++i) a[4 * g + i] = siluf_(acc[i]);
        }
        fx[(2 * w) * 64 + lane] = pack8(a, 0);
        fx[(2 * w + 1) * 64 + lane] = pack8(a, 1);
    }
    __syncthreads();
    STAMP(DBGC, 5);
    {
        f32x16 a;
        GEMM16(a, 48, 64, fx, false);
        STAMP(DBGC, 6);
#pragma unroll
        for (int r = 0; r < 16; ++r) xv[r] = rbf(xv[r] + rbf(a[r]));
    }
    ln_partial(xv, st2, w, lane);
    __syncthreads();
    ln_finish(st2, lane, sc, sh);
    norm_store(xv, sc, sh, fx, w, lane);
    __syncthreads();
    STAMP(DBGC, 7);
    {
        f32x16 acc;
        FFN(acc, 64, DBGC, 8);
#pragma unroll
        for (int r = 0; r < 16; ++r) xv[r] = rbf(__builtin_fmaf(0.5f, rbf(acc[r]), xv[r]));
    }
    ln_partial(xv, st0, w, lane);
    __syncthreads();
    ln_finish(st0, lane, sc, sh);
    {
        uint16_t* op = out + (size_t)(tile * 32 + m) * DM + 32 * w + 4 * h;
#pragma unroll
        for (int g = 0; g < 4; ++g) {
            u32x2 t;
            t[0] = pack2(__builtin_fmaf(xv[4 * g], sc, sh), __builtin_fmaf(xv[4 * g + 1], sc, sh));
            t[1] = pack2(__builtin_fmaf(xv[4 * g + 2], sc, sh), __builtin_fmaf(xv[4 * g + 3], sc, sh));
            *(u32x2*)(op + 8 * g) = t;
        }
    }
    STAMP(DBGC, 11); STAMPR(DBGC, 15);
}

extern "C" void kernel_launch(void* const* d_in, const int* in_sizes, int n_in,
                              void* d_out, int out_size, void* d_ws, size_t ws_size,
                              hipStream_t stream) {
    WPtrs p;
    p.w1a = (const uint16_t*)d_in[1]; p.w1b = (const uint16_t*)d_in[2]; p.wqkv = (const uint16_t*)d_in[3]; p.wo = (const uint16_t*)d_in[4];
    p.wp1 = (const uint16_t*)d_in[5]; p.wp2 = (const uint16_t*)d_in[7]; p.w2a = (const uint16_t*)d_in[8]; p.w2b = (const uint16_t*)d_in[9];
    u32x4* ws = (u32x4*)d_ws;
    k_stageA<<<NTILE + 6, 512, 0, stream>>>((const uint16_t*)d_in[0], p, ws);
    k_attn<<<256, 512, 0, stream>>>(ws, ws);
    k_stageC<<<NTILE, 512, 0, stream>>>(ws, (const uint16_t*)d_in[6], (uint16_t*)d_out);
}
```

```cpp
#include <hip/hip_runtime.h>
#include <hip/hip_bf16.h>
#include <stdint.h>

typedef short bf16x8 __attribute__((ext_vector_type(8)));
typedef float f32x16 __attribute__((ext_vector_type(16)));
typedef float f32x2 __attribute__((ext_vector_type(2)));
typedef float f32x4 __attribute__((ext_vector_type(4)));
typedef unsigned u32x4 __attribute__((ext_vector_type(4)));
typedef unsigned u32x2 __attribute__((ext_vector_type(2)));
typedef __bf16 bf16v2 __attribute__((ext_vector_type(2)));

#define DI __device__ __forceinline__
#define SB() __builtin_amdgcn_sched_barrier(0)
#define STAMPR(base, i) do {} while (0)
#define STAMP(base, i) do {} while (0)
#define MFMA(a, b, c) __builtin_amdgcn_mfma_f32_32x32x16_bf16(__builtin_bit_cast(bf16x8, (a)), __builtin_bit_cast(bf16x8, (b)), (c), 0, 0, 0)

constexpr int T_SEQ = 4000, DM = 256, NTOK = 8000, NTILE = 250, TPB = 125;
constexpr int NFA = 176;
constexpr int NFC = 192;

constexpr size_t WS_WA = 0;
constexpr size_t WS_WC = WS_WA + (size_t)8 * NFA * 64;
constexpr size_t WS_X1 = WS_WC + (size_t)8 * NFC * 64;
constexpr size_t WS_Q  = WS_X1 + (size_t)NTILE * 16 * 64;
constexpr size_t WS_KV = WS_Q + (size_t)8 * TPB * 4 * 64;
constexpr size_t WS_AT = WS_KV + (size_t)8 * TPB * 8 * 64 + 4096;

DI unsigned pack2(float lo, float hi) {
    f32x2 v = {lo, hi};
    bf16v2 r = __builtin_convertvector(v, bf16v2);
    return __builtin_bit_cast(unsigned, r);
}
DI float bflo(unsigned u) { return __uint_as_float(u << 16); }
DI float bfhi(unsigned u) { return __uint_as_float(u & 0xffff0000u); }
DI u32x4 pack8(const f32x16& a, int s) {
    u32x4 r;
    r[0] = pack2(a[8 * s + 0], a[8 * s + 1]);
    r[1] = pack2(a[8 * s + 2], a[8 * s + 3]);
    r[2] = pack2(a[8 * s + 4], a[8 * s + 5]);
    r[3] = pack2(a[8 * s + 6], a[8 * s + 7]);
    return r;
}
DI void unpack16(f32x16& a, const u32x4& f0, const u32x4& f1) {
#pragma unroll
    for (int i = 0; i < 4; ++i) {
        a[2 * i] = bflo(f0[i]); a[2 * i + 1] = bfhi(f0[i]);
        a[8 + 2 * i] = bflo(f1[i]); a[8 + 2 * i + 1] = bfhi(f1[i]);
    }
}
DI float xor32(float v) { return __shfl_xor(v, 32); }
DI float sigmoidf_(float x) { return __builtin_amdgcn_rcpf(1.0f + __builtin_amdgcn_exp2f(-1.4426950408889634f * x)); }
DI float siluf_(float x) { return x * sigmoidf_(x); }

struct WPtrs {
    const uint16_t* w1a; const uint16_t* w1b; const uint16_t* wqkv; const uint16_t* wo;
    const uint16_t* wp1; const uint16_t* wp2; const uint16_t* w2a; const uint16_t* w2b;
};

DI u32x4 pack_chunk(const WPtrs& p, int gid) {
    const int lane = gid & 63, fg = gid >> 6;
    const uint16_t* W; int N, nt, ks;
    const int w = fg / NFC, f = fg % NFC;
    if (f < 16)       { W = p.wo; N = 256; ks = f; nt = w; }
    else if (f < 48)  { const int g = f - 16; W = p.wp1; N = 512; ks = g >> 1; nt = w + 8 * (g & 1); }
    else if (f < 64)  { W = p.wp2; N = 256; ks = f - 48; nt = w; }
    else if (f < 128) { const int g = f - 64; W = p.w2a; N = 1024; ks = g & 15; nt = 4 * w + (g >> 4); }
    else              { W = p.w2b; N = 256; ks = f - 128; nt = w; }
    const int r = lane & 31, h = lane >> 5;
    const uint16_t* src = W + (size_t)(16 * ks + 4 * h) * N + 32 * nt + r;
    unsigned e[8];
#pragma unroll
    for (int j = 0; j < 8; ++j) e[j] = src[(size_t)(8 * (j >> 2) + (j & 3)) * N];
    u32x4 o;
#pragma unroll
    for (int i = 0; i < 4; ++i) o[i] = e[2 * i] | (e[2 * i + 1] << 16);
    return o;
}

DI void glds16(const void* g, unsigned lds_base) {
    unsigned sv;
    asm volatile("s_mov_b32 %0, m0\n\ts_mov_b32 m0, %2\n\ts_nop 0\n\tglobal_load_lds_dwordx4 %1, off\n\ts_mov_b32 m0, %0"
                 : "=&s"(sv) : "v"(g), "s"(lds_base) : "memory");
}
template <int NI> DI void l2_warm(const u32x4* base, int total_chunks, int rank, int w, int lane, unsigned lds_scratch) {
#pragma unroll
    for (int i = 0; i < NI; ++i) {
        int c = ((rank * NI + i) * 8 + w) * 64;
        c = c + 64 <= total_chunks ? c : total_chunks - 64;
        glds16(base + c + lane, lds_scratch);
    }
}
#define WLOAD(F) ring[(F) & 31] = wp[(F) * 64]
#define REFILL(F, PE) do { if ((F) < 16) WLOAD((F) + 16); if ((F) + 32 < (NFX)) WLOAD((F) + 32); } while (0)
#define LD8(F) do { SB(); _Pragma("unroll") for (int i_ = 0; i_ < 8; ++i_) WLOAD((F) + i_); SB(); } while (0)
#define RAWBAR() do { SB(); __builtin_amdgcn_s_barrier(); SB(); } while (0)

DI void ln_partial(const f32x16& v, float* st, int w, int lane) {
    float s = 0.f, q = 0.f;
#pragma unroll
    for (int i = 0; i < 16; ++i) { s += v[i]; q = __builtin_fmaf(v[i], v[i], q); }
    auto r = __builtin_amdgcn_permlane32_swap(__float_as_uint(s), __float_as_uint(q), false, false);
    st[w * 64 + lane] = __uint_as_float(r[0]) + __uint_as_float(r[1]);
}
DI void ln_finish(const float* st, int lane, float& scale, float& shift) {
    const float* p = st + (lane & 31);
    float s = 0.f, q = 0.f;
#pragma unroll
    for (int i = 0; i < 8; ++i) { s += p[i * 64]; q += p[i * 64 + 32]; }
    const float m = s * (1.0f / 256.0f);
    const float var = fmaxf(q * (1.0f / 256.0f) - m * m, 0.f);
    scale = __builtin_amdgcn_rsqf(var + 1e-5f);
    shift = -m * scale;
}
DI void norm_store(const f32x16& v, float scale, float shift, u32x4* dst  , int w, int lane) {
    f32x16 n;
#pragma unroll
    for (int i = 0; i < 16; ++i) n[i] = __builtin_fmaf(v[i], scale, shift);
    dst[(2 * w) * 64 + lane] = pack8(n, 0);
    dst[(2 * w + 1) * 64 + lane] = pack8(n, 1);
}
DI f32x16 sel16(int c, const f32x16& a, const f32x16& b) {
    f32x16 r;
#pragma unroll
    for (int i = 0; i < 16; ++i) r[i] = c ? a[i] : b[i];
    return r;
}
DI float rbf(float x) { return bflo(pack2(x, 0.f) & 0xffffu); }

#define GEMM16(acc, F0, PE, fxp, SWAP) do { \
        acc = (f32x16){}; u32x4 bq_[4]; \
        _Pragma("unroll") for (int i_ = 0; i_ < 4; ++i_) bq_[i_] = (fxp)[i_ * 64 + lane]; \
        _Pragma("unroll") for (int ks_ = 0; ks_ < 16; ++ks_) { \
            if (SWAP) acc = MFMA(bq_[ks_ & 3], ring[((F0) + ks_) & 31], acc); else acc = MFMA(ring[((F0) + ks_) & 31], bq_[ks_ & 3], acc); \
            if (ks_ + 4 < 16) bq_[ks_ & 3] = (fxp)[(ks_ + 4) * 64 + lane]; \
            REFILL((F0) + ks_, PE); SB(); } } while (0)

#define FFN(acc, F0, DBG, ST) do { \
        _Pragma("unroll") for (int t_ = 0; t_ < 4; ++t_) { \
            f32x16 a_; GEMM16(a_, (F0) + 16 * t_, (F0) + 64, fx, false); \
            if (t_ == 3) { STAMP(DBG, ST); } \
            _Pragma("unroll") for (int r_ = 0; r_ < 8; ++r_) a_[r_] = siluf_(a_[r_]); \
            fh[((4 * w + t_) * 2 + 0) * 64 + lane] = pack8(a_, 0); \
            _Pragma("unroll") for (int r_ = 8; r_ < 16; ++r_) a_[r_] = siluf_(a_[r_]); \
            fh[((4 * w + t_) * 2 + 1) * 64 + lane] = pack8(a_, 1); } \
        __syncthreads(); \
        STAMP(DBG, ST + 1); \
        acc = (f32x16){}; u32x4 bq_[4]; \
        _Pragma("unroll") for (int i_ = 0; i_ < 4; ++i_) bq_[i_] = fh[i_ * 64 + lane]; \
        _Pragma("unroll") for (int ks_ = 0; ks_ < 64; ++ks_) { \
            acc = MFMA(ring[((F0) + 64 + ks_) & 31], bq_[ks_ & 3], acc); \
            if (ks_ + 4 < 64) bq_[ks_ & 3] = fh[(ks_ + 4) * 64 + lane]; \
            REFILL((F0) + 64 + ks_, (F0) + 128); SB(); } \
        STAMP(DBG, ST + 2); } while (0)

#define DBGA (ws + (8u<<20))
typedef short s16x4 __attribute__((ext_vector_type(4)));
typedef __attribute__((address_space(3))) const char* lds_cptr;
DI u32x2 trread(lds_cptr p) { return __builtin_bit_cast(u32x2, __builtin_amdgcn_ds_read_tr16_b64_v4i16((__attribute__((address_space(3))) s16x4*)p)); }
DI void glds16s(const void* sbase, unsigned voff, unsigned lds_base) {
    unsigned sv;
    asm volatile("s_mov_b32 %0, m0\n\ts_mov_b32 m0, %3\n\ts_nop 0\n\tglobal_load_lds_dwordx4 %1, %2\n\ts_mov_b32 m0, %0"
                 : "=&s"(sv) : "v"(voff), "s"(sbase), "s"(lds_base) : "memory");
}
constexpr int A_LDS_FX = 0, A_LDS_FH = 16384, A_LDS_ST = 81920, A_LDS_RING = 86016, A_LDS_BYTES = 86016 + 65536;
constexpr int SD = 8;

__global__ __launch_bounds__(512, 2) void k_stageA(const uint16_t* __restrict__ x, WPtrs wt, u32x4* __restrict__ ws) {
    __shared__ __attribute__((aligned(16))) char smem[A_LDS_BYTES];
    const int tile = blockIdx.x, tid = threadIdx.x, lane = tid & 63, h = lane >> 5, m = lane & 31;
    if (tile >= NTILE) {
        for (int c = (tile - NTILE) * 512 + tid; c < 8 * NFC * 64; c += 6 * 512) ws[WS_WC + c] = pack_chunk(wt, c);
        return;
    }
    u32x4* fx = (u32x4*)(smem + A_LDS_FX);
    u32x4* fh = (u32x4*)(smem + A_LDS_FH);
    float* st0 = (float*)(smem + A_LDS_ST);
    float* st1 = st0 + 512;
    const int w = __builtin_amdgcn_readfirstlane(tid >> 6);
    STAMPR(DBGA, 14); STAMP(DBGA, 0);
    u32x2 xr[4];
    {
        const uint16_t* xp = x + (size_t)(tile * 32 + m) * DM + 32 * w + 4 * h;
#pragma unroll
        for (int g = 0; g < 4; ++g) xr[g] = *(const u32x2*)(xp + 8 * g);
    }
    RAWBAR();
    const unsigned ringb = (unsigned)(uintptr_t)smem + A_LDS_RING + w * 8192;
    const lds_cptr ringp = (lds_cptr)smem + A_LDS_RING + w * 8192;
    const unsigned vsw = (((lane & 7) ^ ((lane >> 4 & 1) << 2)) * 16);
    const unsigned vo2048 = (lane >> 3) * 2048 + vsw, vo512 = (lane >> 3) * 512 + vsw, vo1536 = (lane >> 3) * 1536 + vsw;
    const int G = lane >> 4, q4 = (lane >> 2) & 3, p4 = lane & 3;
    const int va128_0 = (4 * (G >> 1) + q4) * 128 + ((q4 >> 1) * 64) + (G & 1) * 32 + p4 * 8, va128_1 = va128_0 ^ 64;
    const int pi = w >> 1, kh = w & 1;
    const char* b1a = (const char*)wt.w1a + w * 256;
    const char* b1b = (const char*)wt.w1b + pi * 128 + kh * (512 * 512);
    const char* bqk = (const char*)wt.wqkv + pi * 128 + kh * (128 * 1536);
#define ISSUE(F) do { if ((F) < NFA) { \
        if ((F) < 64) glds16s(b1a + (8 * ((F) & 31)) * 2048 + ((F) >> 5) * 128, vo2048, ringb + ((F) & 7) * 1024); \
        else if ((F) < 128) glds16s(b1b + ((F) - 64) * 8 * 512, vo512, ringb + ((F) & 7) * 1024); \
        else glds16s(bqk + (((F) - 128) & 15) * 8 * 1536 + (((F) - 128) >> 4) * 512, vo1536, ringb + ((F) & 7) * 1024); } } while (0)
#define TRREAD(F) do { if ((F) < NFA) { const int va_ = ((F) & 1) ? va128_1 : va128_0; \
        const u32x2 lo_ = trread(ringp + va_ + (((F) & 6) * 1024)), hi_ = trread(ringp + va_ + ((((F) & 6) + 1) * 1024)); \
        ar[(F) & 15] = (u32x4){lo_[0], lo_[1], hi_[0], hi_[1]}; } } while (0)
#define WAITV(n) asm volatile("s_waitcnt vmcnt(%0)" :: "n"(n) : "memory")
#define PRE(F) do { if ((F) + SD < NFA) { WAITV(((F) + SD + 8 <= NFA ? 6 : NFA - (F) - SD - 2)); TRREAD((F) + SD); TRREAD((F) + SD + 1); } SB(); } while (0)
#define POST(F) do { SB(); if ((F) + SD + 8 < NFA) { asm volatile("s_waitcnt lgkmcnt(0)" ::: "memory"); ISSUE((F) + SD + 8); ISSUE((F) + SD + 9); } SB(); } while (0)
    u32x4 ar[16];
#pragma unroll
    for (int i = 0; i < 8; ++i) ISSUE(i);
    SB();
    f32x16 xv;
#pragma unroll
    for (int g = 0; g < 4; ++g) { xv[4 * g + 0] = bflo(xr[g][0]); xv[4 * g + 1] = bfhi(xr[g][0]); xv[4 * g + 2] = bflo(xr[g][1]); xv[4 * g + 3] = bfhi(xr[g][1]); }
    float sc, sh;
    ln_partial(xv, st0, w, lane);
#pragma unroll
    for (int i = 0; i < 8; i += 2) {
        WAITV(6); TRREAD(i); TRREAD(i + 1); SB();
        asm volatile("s_waitcnt lgkmcnt(0)" ::: "memory"); ISSUE(i + 8); ISSUE(i + 9); SB();
        if (i == 2) __syncthreads();
    }
    ln_finish(st0, lane, sc, sh);
    norm_store(xv, sc, sh, fx, w, lane);
    __syncthreads();
    STAMP(DBGA, 1);
#pragma unroll
    for (int pp = 0; pp < 2; ++pp) {
        f32x16 a0 = (f32x16){}, a1 = (f32x16){};
        u32x4 bq[2];
        bq[0] = fx[lane]; bq[1] = fx[64 + lane];
#pragma unroll
        for (int ks = 0; ks < 16; ++ks) {
            const int F = pp * 32 + 2 * ks;
            const u32x4 b = bq[ks & 1];
            if (ks + 2 < 16) bq[ks & 1] = fx[(ks + 2) * 64 + lane];
            PRE(F);
            a0 = MFMA(ar[F & 15], b, a0); a1 = MFMA(ar[(F + 1) & 15], b, a1);
            POST(F);
        }
        if (pp == 1) STAMP(DBGA, 2);
#pragma unroll
        for (int r = 0; r < 16; ++r) { a0[r] = siluf_(a0[r]); a1[r] = siluf_(a1[r]); }
        fh[((4 * w + 2 * pp) * 2 + 0) * 64 + lane] = pack8(a0, 0);
        fh[((4 * w + 2 * pp) * 2 + 1) * 64 + lane] = pack8(a0, 1);
        fh[((4 * w + 2 * pp) * 2 + 2) * 64 + lane] = pack8(a1, 0);
        fh[((4 * w + 2 * pp) * 2 + 3) * 64 + lane] = pack8(a1, 1);
    }
    __syncthreads();
    STAMP(DBGA, 3);
    {
        f32x16 a0 = (f32x16){}, a1 = (f32x16){};
        const u32x4* fhk = fh + kh * 32 * 64 + lane;
        u32x4 bq[2];
        bq[0] = fhk[0]; bq[1] = fhk[64];
#pragma unroll
        for (int ks = 0; ks < 32; ++ks) {
            const int F = 64 + 2 * ks;
            const u32x4 b = bq[ks & 1];
            if (ks + 2 < 32) bq[ks & 1] = fhk[(ks + 2) * 64];
            PRE(F);
            a0 = MFMA(ar[F & 15], b, a0); a1 = MFMA(ar[(F + 1) & 15], b, a1);
            POST(F);
        }
        STAMP(DBGA, 4);
        __syncthreads();
        f32x4* xch = (f32x4*)fh;
        const f32x16 snd = sel16(kh, a0, a1);
#pragma unroll
        for (int g = 0; g < 4; ++g) xch[(w * 4 + g) * 64 + lane] = (f32x4){snd[4 * g], snd[4 * g + 1], snd[4 * g + 2], snd[4 * g + 3]};
        __syncthreads();
        const f32x16 own = sel16(kh, a1, a0);
#pragma unroll
        for (int g = 0; g < 4; ++g) {
            const f32x4 o = xch[((w ^ 1) * 4 + g) * 64 + lane];
#pragma unroll
            for (int i = 0; i < 4; ++i) xv[4 * g + i] = rbf(__builtin_fmaf(0.5f, rbf(own[4 * g + i] + o[i]), xv[4 * g + i]));
        }
    }
    ln_partial(xv, st1, w, lane);
    {
        u32x4* xo = ws + WS_X1 + (size_t)tile * 16 * 64;
        xo[(2 * w) * 64 + lane] = pack8(xv, 0);
        xo[(2 * w + 1) * 64 + lane] = pack8(xv, 1);
    }
    __syncthreads();
    ln_finish(st1, lane, sc, sh);
    norm_store(xv, sc, sh, fx, w, lane);
    __syncthreads();
    STAMP(DBGA, 5);
    {
        const int b = tile / TPB, tt = tile % TPB, bh = b * 4 + (w >> 1), t2 = w & 1;
        u32x4* qo = ws + WS_Q + ((size_t)(bh * TPB + tt) * 4 + 2 * t2) * 64 + lane;
        u32x4* ko = ws + WS_KV + ((size_t)(bh * TPB + tt) * 8 + 2 * t2) * 64 + lane;
        f32x16 acc[6];
        const u32x4* fxk = fx + kh * 8 * 64 + lane;
#pragma unroll
        for (int t = 0; t < 3; ++t) {
            acc[2 * t] = (f32x16){}; acc[2 * t + 1] = (f32x16){};
            u32x4 bq[2];
            bq[0] = fxk[0]; bq[1] = fxk[64];
#pragma unroll
            for (int ks = 0; ks < 8; ++ks) {
                const int F = 128 + 16 * t + 2 * ks;
                const u32x4 b = bq[ks & 1];
                if (ks + 2 < 8) bq[ks & 1] = fxk[(ks + 2) * 64];
                PRE(F);
                if (t < 2) { acc[2 * t] = MFMA(ar[F & 15], b, acc[2 * t]); acc[2 * t + 1] = MFMA(ar[(F + 1) & 15], b, acc[2 * t + 1]); }
                else       { acc[2 * t] = MFMA(b, ar[F & 15], acc[2 * t]); acc[2 * t + 1] = MFMA(b, ar[(F + 1) & 15], acc[2 * t + 1]); }
                POST(F);
            }
        }
        STAMP(DBGA, 6);
        f32x4* xch = (f32x4*)fh;
        f32x4* xv_ = (f32x4*)(smem + A_LDS_RING);
#pragma unroll
        for (int t = 0; t < 3; ++t) {
            const f32x16 snd = sel16(kh, acc[2 * t], acc[2 * t + 1]);
            f32x4* d = t < 2 ? xch + (t * 32 + w * 4) * 64 + lane : xv_ + w * 512 + lane;
#pragma unroll
            for (int g = 0; g < 4; ++g) d[g * 64] = (f32x4){snd[4 * g], snd[4 * g + 1], snd[4 * g + 2], snd[4 * g + 3]};
        }
        __syncthreads();
#pragma unroll
        for (int t = 0; t < 3; ++t) {
            f32x16 a = sel16(kh, acc[2 * t + 1], acc[2 * t]);
            const f32x4* d = t < 2 ? xch + (t * 32 + (w ^ 1) * 4) * 64 + lane : xv_ + (w ^ 1) * 512 + lane;
            const float qs = t == 0 ? 0.125f * 1.4426950408889634f : 1.0f;
#pragma unroll
            for (int g = 0; g < 4; ++g) {
                const f32x4 o = d[g * 64];
#pragma unroll
                for (int i = 0; i < 4; ++i) a[4 * g + i] = (a[4 * g + i] + o[i]) * qs;
            }
            u32x4* dst = t == 0 ? qo : (t == 1 ? ko : ko + 4 * 64);
            dst[0] = pack8(a, 0); dst[64] = pack8(a, 1);
        }
    }
    STAMP(DBGA, 7); STAMPR(DBGA, 15);
#undef ISSUE
#undef TRREAD
#undef PRE
#undef POST
}

#define WAIT_BAR(N) asm volatile("s_waitcnt vmcnt(" #N ") lgkmcnt(0)\n\ts_barrier" ::: "memory")

constexpr int AT_SLOT = 1024;
constexpr float AT_BIG = 1.0e30f;
#define ABL_NODMA 0
#define ABL_NOBAR 0
#define ABL_NOLDS 0
#define ABL_NOEXP 0
#define ABL_NOADD 0
#define PIN(x) asm volatile("" : "+v"(x))
DI float vadd(float a, float b) { float r; asm("v_add_f32 %0, %1, %2" : "=v"(r) : "v"(a), "v"(b)); return r; }
#define EX2(x) __builtin_amdgcn_exp2f(x)

__global__ __launch_bounds__(512, 2) void k_attn(const u32x4* __restrict__ ws_in, u32x4* __restrict__ ws) {
    __shared__ u32x4 lds[4 * 2048];
    const int tid = threadIdx.x, lane = tid & 63;
    const int w = __builtin_amdgcn_readfirstlane(tid >> 6);
    STAMP((ws + (10u<<20)), 0);
    const int bh = blockIdx.x & 7, qb = blockIdx.x >> 3, qi = w & 3, g = w >> 2;
    int qt = 4 * qb + qi; const bool qvalid = qt < TPB; if (!qvalid) qt = TPB - 1;
    const u32x4* kv = ws_in + WS_KV + (size_t)bh * TPB * 8 * 64;
    const unsigned lds0 = (unsigned)(uintptr_t)lds;
    const u32x4* src = kv + (w >> 1) * 512 + (w & 1) * 128 + lane;
    const u32x4* src31 = kv + 124 * 512 + (w & 1) * 128 + lane;
    const unsigned dstk = lds0 + (w >> 1) * 4096 + (w & 1) * 2048;
    const u32x4* srcw = kv + qi * 512 + lane;
    const u32x4* srcw31 = kv + 124 * 512 + lane;
    const unsigned dstw = lds0 + qi * 4096;
#define DMA2(sp, dst) do { glds16((sp), (unsigned)__builtin_amdgcn_readfirstlane(dst)); glds16((sp) + 64, (unsigned)__builtin_amdgcn_readfirstlane((dst) + 1024)); } while (0)
#define DMA_K(step) do { const int st_ = (step); const u32x4* s_ = st_ < 31 ? src + (size_t)st_ * 2048 : src31; DMA2(s_, dstk + (st_ & 3) * 32768); } while (0)
#define DMA_V(step) do { const int st_ = (step); const u32x4* s_ = st_ < 31 ? src + (size_t)st_ * 2048 : src31; DMA2(s_ + 256, dstk + (st_ & 3) * 32768 + 16384); } while (0)
    DMA_K(0); DMA_V(0); DMA_K(1);
    u32x4 q[4];
    {
        const u32x4* qp = ws_in + WS_Q + (size_t)(bh * TPB + qt) * 4 * 64 + lane;
#pragma unroll
        for (int f = 0; f < 4; ++f) q[f] = qp[f * 64];
    }
    DMA_K(2); DMA_V(1);
    const char* lbase = (const char*)lds + (2 * g) * 4096 + lane * 16;
#define KF(off, i) (*(const u32x4*)(lbase + (off) + (i) * 1024))
#define VF(off, i) (*(const u32x4*)(lbase + (off) + 16384 + (i) * 1024))
    f32x16 o0 = (f32x16){}, o1 = (f32x16){};
    float negm;
    f32x4 lsum = (f32x4){0.f, 0.f, 0.f, 0.f};
    u32x4 onesA;
    { const unsigned o_ = ((lane & 15) == ((lane >> 4) & 1)) ? 0x3f803f80u : 0u; onesA = (u32x4){o_, o_, o_, o_}; }
    f32x16 cm, sA, sB;
    u32x4 kf[4], vf[4], pA0, pA1, pB0, pB1;
    WAIT_BAR(4);
    {
#pragma unroll
        for (int i = 0; i < 4; ++i) kf[i] = KF(0, i);
        const f32x16 z = (f32x16){};
        sA = MFMA(kf[0], q[0], z); sA = MFMA(kf[1], q[1], sA); sA = MFMA(kf[2], q[2], sA); sA = MFMA(kf[3], q[3], sA);
#pragma unroll
        for (int i = 0; i < 4; ++i) kf[i] = KF(4096, i);
        float mx = sA[0];
#pragma unroll
        for (int r = 1; r < 16; ++r) mx = fmaxf(mx, sA[r]);
        mx = fmaxf(mx, xor32(mx));
        negm = -mx;
#pragma unroll
        for (int r = 0; r < 16; ++r) { sA[r] += negm; cm[r] = negm; sB[r] = 0.f; }
        pB0 = (u32x4){0u, 0u, 0u, 0u}; pB1 = pB0;
    }
#define FIN(S, i, PK, e) do { PK[e] = pack2(S[i], S[(i) + 1]); PIN(PK); } while (0)
#define EXP2(S, i) do { S[i] = EX2(S[i]); S[(i) + 1] = EX2(S[(i) + 1]); PIN(S); } while (0)
#define LSUM(PK) do { lsum = __builtin_amdgcn_mfma_f32_16x16x32_bf16(__builtin_bit_cast(bf16x8, onesA), __builtin_bit_cast(bf16x8, PK), lsum, 0, 0, 0); PIN(lsum); } while (0)
#define PERIOD(SC, SN, PP0, PP1, PC0, PC1, koff, voff, D0, D1) do { \
        vf[0] = VF(voff, 0); FIN(SN, 14, PP1, 3); SB(); SN = MFMA(kf[0], q[0], cm); PIN(SN); EXP2(SC, 0); SB(); \
        vf[2] = VF(voff, 2); SB(); SN = MFMA(kf[1], q[1], SN); PIN(SN); EXP2(SC, 2); FIN(SC, 0, PC0, 0); SB(); \
        vf[1] = VF(voff, 1); SB(); SN = MFMA(kf[2], q[2], SN); PIN(SN); EXP2(SC, 4); FIN(SC, 2, PC0, 1); SB(); D0; SB(); \
        vf[3] = VF(voff, 3); SB(); SN = MFMA(kf[3], q[3], SN); PIN(SN); EXP2(SC, 6); FIN(SC, 4, PC0, 2); SB(); \
        kf[0] = KF(koff, 0); SB(); o0 = MFMA(vf[0], PP0, o0); PIN(o0); EXP2(SC, 8); FIN(SC, 6, PC0, 3); SB(); LSUM(PP0); SB(); \
        kf[1] = KF(koff, 1); SB(); o1 = MFMA(vf[2], PP0, o1); PIN(o1); EXP2(SC, 10); FIN(SC, 8, PC1, 0); SB(); \
        kf[2] = KF(koff, 2); SB(); o0 = MFMA(vf[1], PP1, o0); PIN(o0); EXP2(SC, 12); FIN(SC, 10, PC1, 1); SB(); LSUM(PP1); SB(); D1; SB(); \
        kf[3] = KF(koff, 3); SB(); o1 = MFMA(vf[3], PP1, o1); PIN(o1); EXP2(SC, 14); FIN(SC, 12, PC1, 2); SB(); \
    } while (0)
    int tk0 = 0, tk1 = 0, tk2 = 0, acc_c = 0, acc_w = 0;
#define TICK(x) do {} while (0)
    int vprev = 0;
    for (int i = 0; i < 31; ++i) {
        const int scur = (i & 3) * 32768, snext = ((i + 1) & 3) * 32768;
        const int sk = i + 3 < 32 ? i + 3 : 31, sv = i + 2 < 32 ? i + 2 : 31;
        const u32x4* pk_ = sk < 31 ? srcw + (size_t)sk * 2048 : srcw31;
        const u32x4* pv_ = (sv < 31 ? srcw + (size_t)sv * 2048 : srcw31) + 256;
        const unsigned dk_ = dstw + (sk & 3) * 32768, dv_ = dstw + (sv & 3) * 32768 + 16384;
#define G2(p, d) do { if (!ABL_NODMA && g == 0) { glds16((p), (unsigned)__builtin_amdgcn_readfirstlane(d)); glds16((p) + 64, (unsigned)__builtin_amdgcn_readfirstlane((d) + 1024)); } } while (0)
        SB();
        PERIOD(sA, sB, pB0, pB1, pA0, pA1, snext, vprev, G2(pk_, dk_), G2(pk_ + 128, dk_ + 2048));
        PERIOD(sB, sA, pA0, pA1, pB0, pB1, snext + 4096, scur, G2(pv_, dv_), G2(pv_ + 128, dv_ + 2048));
        vprev = scur + 4096;
        TICK(tk1); if (!ABL_NOBAR) { if (g) WAIT_BAR(0); else WAIT_BAR(8); } TICK(tk2); acc_c += tk1 - tk0; acc_w += tk2 - tk1; tk0 = tk2;
    }
    {
        if (g == 1) {
#pragma unroll
            for (int r = 0; r < 16; ++r) sA[r] = -INFINITY;
        }
        PERIOD(sA, sB, pB0, pB1, pA0, pA1, 0, vprev, (void)0, (void)0);
#pragma unroll
        for (int r = 0; r < 16; ++r) sB[r] = -INFINITY;
        PERIOD(sB, sA, pA0, pA1, pB0, pB1, 0, 3 * 32768, (void)0, (void)0);
    }
    float l;
    { const float la = __shfl(lsum[0], lane & 15), lb = __shfl(lsum[1], lane & 15); l = 0.5f * ((lane & 16) ? lb : la); }
    WAIT_BAR(0);
#undef DMA_K
#undef DMA_V
#undef DMA2
#undef PERIOD
#undef FIN
#undef EXP2
#undef LSUM
#undef G2
#undef KF
#undef VF
    {
        bool bad = !(l < AT_BIG);
#pragma unroll
        for (int r = 0; r < 16; ++r) bad = bad || !(fabsf(o0[r]) < INFINITY) || !(fabsf(o1[r]) < INFINITY);
        if (__builtin_expect(__any(bad), 0)) {
            o0 = (f32x16){}; o1 = (f32x16){}; l = 0.f; float mrun = -INFINITY;
            for (int kt = 0; kt < TPB; ++kt) {
                if (((kt >> 1) & 1) != g) continue;
                const u32x4* kg = kv + (size_t)kt * 512 + lane;
                f32x16 sc = (f32x16){};
#pragma unroll
                for (int f = 0; f < 4; ++f) sc = MFMA(kg[f * 64], q[f], sc);
                float mx = sc[0];
#pragma unroll
                for (int r = 1; r < 16; ++r) mx = fmaxf(mx, sc[r]);
                mx = fmaxf(mx, xor32(mx));
                const float mn = fmaxf(mrun, mx), f = EX2(mrun - mn);
                mrun = mn; l *= f;
#pragma unroll
                for (int r = 0; r < 16; ++r) { o0[r] *= f; o1[r] *= f; sc[r] = EX2(sc[r] - mn); l += sc[r]; }
                const u32x4 p0 = pack8(sc, 0), p1 = pack8(sc, 1);
                o0 = MFMA(kg[256], p0, o0); o1 = MFMA(kg[256 + 128], p0, o1);
                o0 = MFMA(kg[256 + 64], p1, o0); o1 = MFMA(kg[256 + 192], p1, o1);
            }
            negm = -mrun;
        }
    }
    l += xor32(l);
    float* cf = (float*)lds;
    if (g == 1) {
        float* dst = cf + qi * 34 * 64 + lane;
#pragma unroll
        for (int r = 0; r < 16; ++r) { dst[r * 64] = o0[r]; dst[(16 + r) * 64] = o1[r]; }
        dst[32 * 64] = negm; dst[33 * 64] = l;
    }
    __syncthreads();
    if (g == 0) {
        const float* s = cf + qi * 34 * 64 + lane;
        const float negm1 = s[32 * 64], l1 = s[33 * 64];
        const float nm = fminf(negm, negm1);
        const float f0 = __builtin_amdgcn_exp2f(nm - negm), f1 = __builtin_amdgcn_exp2f(nm - negm1);
        const float inv = __builtin_amdgcn_rcpf(l * f0 + l1 * f1);
        const float c0 = f0 * inv, c1 = f1 * inv;
#pragma unroll
        for (int r = 0; r < 16; ++r) { o0[r] = o0[r] * c0 + s[r * 64] * c1; o1[r] = o1[r] * c0 + s[(16 + r) * 64] * c1; }
        if (qvalid) {
            const int b = bh >> 2, hd = bh & 3;
            u32x4* ao = ws + WS_AT + ((size_t)(b * TPB + qt) * 16 + 4 * hd) * 64 + lane;
            ao[0] = pack8(o0, 0); ao[64] = pack8(o0, 1); ao[128] = pack8(o1, 0); ao[192] = pack8(o1, 1);
        }
    }
    STAMPR((ws + (10u<<20)), 15);
}

#define DBGC ((u32x4*)ws + (9u<<20))
__global__ __launch_bounds__(512, 2) void k_stageC(const u32x4* __restrict__ ws, const uint16_t* __restrict__ dw, uint16_t* __restrict__ out) {
    __shared__ u32x4 lds[32 * 64 + 64 * 64 + 384 + 160 + 512];
    u32x4* fx = lds;
    u32x4* fy = lds + 16 * 64;
    u32x4* fh = lds + 32 * 64;
    float* st0 = (float*)(lds + 32 * 64 + 64 * 64);
    float* st1 = st0 + 512;
    float* st2 = st0 + 1024;
    u32x4* dwl = lds + 32 * 64 + 64 * 64 + 384;
    const int tile = blockIdx.x, tid = threadIdx.x, lane = tid & 63, h = lane >> 5, m = lane & 31;
    const int w = __builtin_amdgcn_readfirstlane(tid >> 6);
    STAMPR(DBGC, 14); STAMP(DBGC, 0);

    const int tt = tile % TPB;
    const int hm = m & 3;
    int toff = hm < 2 ? hm - 2 : 30 + hm;
    const int tl = tt * 32 + toff;
    const bool hvalid = (tl >= 0) && (tl < T_SEQ);
    if (!hvalid) toff = 0;
    const int gtok = tile * 32 + toff;
    const size_t hbase = (size_t)(gtok >> 5) * 16 * 64 + h * 32 + (gtok & 31);

    u32x4 in[8];
    {
        const u32x4* at = ws + WS_AT;
        const u32x4* x1 = ws + WS_X1;
        const size_t mb = (size_t)tile * 16 * 64 + lane;
        in[0] = at[mb + (2 * w) * 64]; in[1] = at[mb + (2 * w + 1) * 64];
        in[2] = at[hbase + (2 * w) * 64]; in[3] = at[hbase + (2 * w + 1) * 64];
        in[4] = x1[mb + (2 * w) * 64]; in[5] = x1[mb + (2 * w + 1) * 64];
        in[6] = x1[hbase + (2 * w) * 64]; in[7] = x1[hbase + (2 * w + 1) * 64];
    }
    u32x4 dwr;
    if (tid < 160) dwr = ((const u32x4*)dw)[tid];
    RAWBAR();
    constexpr int NFX = NFC;
    const u32x4* wp = ws + WS_WC + (size_t)w * NFC * 64 + lane;
    u32x4 ring[32];
    LD8(0); LD8(8);
    f32x16 xv, yv;
    fx[(2 * w) * 64 + lane] = in[0]; fx[(2 * w + 1) * 64 + lane] = in[1];
    fy[(2 * w) * 64 + lane] = in[2]; fy[(2 * w + 1) * 64 + lane] = in[3];
    if (tid < 160) dwl[tid] = dwr;
    unpack16(xv, in[4], in[5]);
    unpack16(yv, in[6], in[7]);
    __syncthreads();
    STAMP(DBGC, 1);
    {
        f32x16 a = (f32x16){}, b = (f32x16){};
        u32x4 bx[2], by[2];
        bx[0] = fx[lane]; by[0] = fy[lane]; bx[1] = fx[64 + lane]; by[1] = fy[64 + lane];
#pragma unroll
        for (int ks = 0; ks < 16; ++ks) {
            a = MFMA(ring[ks & 31], bx[ks & 1], a);
            b = MFMA(ring[ks & 31], by[ks & 1], b);
            if (ks + 2 < 16) { bx[ks & 1] = fx[(ks + 2) * 64 + lane]; by[ks & 1] = fy[(ks + 2) * 64 + lane]; }
            REFILL(ks, 0);
            SB();
        }
        STAMP(DBGC, 2);
#pragma unroll
        for (int r = 0; r < 16; ++r) { xv[r] = rbf(xv[r] + rbf(a[r])); yv[r] = rbf(yv[r] + rbf(b[r])); }
    }
    float sc, sh, sc2, sh2;
    ln_partial(xv, st0, w, lane);
    ln_partial(yv, st1, w, lane);
    __syncthreads();
    ln_finish(st0, lane, sc, sh);
    ln_finish(st1, lane, sc2, sh2);
    norm_store(xv, sc, sh, fx, w, lane);
    norm_store(yv, sc2, sh2, fy, w, lane);
    __syncthreads();
    STAMP(DBGC, 3);
    {
        f32x16 a = (f32x16){}, b = (f32x16){}, c = (f32x16){}, d = (f32x16){};
        u32x4 bxn = fx[lane], byn = fy[lane];
#pragma unroll
        for (int ks = 0; ks < 16; ++ks) {
            const int F = 16 + 2 * ks;
            const u32x4 bx = bxn, by = byn;
            if (ks + 1 < 16) { bxn = fx[(ks + 1) * 64 + lane]; byn = fy[(ks + 1) * 64 + lane]; }
            a = MFMA(ring[F & 31], bx, a); c = MFMA(ring[F & 31], by, c); REFILL(F, 0);
            b = MFMA(ring[(F + 1) & 31], bx, b); d = MFMA(ring[(F + 1) & 31], by, d); REFILL(F + 1, 0);
            SB();
        }
        STAMP(DBGC, 4);
#pragma unroll
        for (int r = 0; r < 16; ++r) {
            a[r] = a[r] * sigmoidf_(b[r]);
            c[r] = hvalid ? c[r] * sigmoidf_(d[r]) : 0.f;
        }
        f32x4* cs = (f32x4*)fh + w * (4 * 2 * 36);
#pragma unroll
        for (int g = 0; g < 4; ++g) {
            cs[(g * 2 + h) * 36 + m + 2] = (f32x4){a[4 * g], a[4 * g + 1], a[4 * g + 2], a[4 * g + 3]};
            if (m < 4) cs[(g * 2 + h) * 36 + (m < 2 ? m : 32 + m)] = (f32x4){c[4 * g], c[4 * g + 1], c[4 * g + 2], c[4 * g + 3]};
        }
        __syncthreads();
        const uint16_t* dp = (const uint16_t*)dwl + 32 * w + 4 * h;
#pragma unroll
        for (int g = 0; g < 4; ++g) {
            f32x4 acc = (f32x4){0.f, 0.f, 0.f, 0.f};
#pragma unroll
            for (int i = 0; i < 5; ++i) {
                const f32x4 hv = cs[(g * 2 + h) * 36 + m + i];
                const u32x2 t = *(const u32x2*)(dp + i * DM + 8 * g);
                acc[0] += hv[0] * bflo(t[0]); acc[1] += hv[1] * bfhi(t[0]); acc[2] += hv[2] * bflo(t[1]); acc[3] += hv[3] * bfhi(t[1]);
            }
#pragma unroll
            for (int i = 0; i < 4; ++i) a[4 * g + i] = siluf_(acc[i]);
        }
        fx[(2 * w) * 64 + lane] = pack8(a, 0);
        fx[(2 * w + 1) * 64 + lane] = pack8(a, 1);
    }
    __syncthreads();
    STAMP(DBGC, 5);
    {
        f32x16 a;
        GEMM16(a, 48, 64, fx, false);
        STAMP(DBGC, 6);
#pragma unroll
        for (int r = 0; r < 16; ++r) xv[r] = rbf(xv[r] + rbf(a[r]));
    }
    ln_partial(xv, st2, w, lane);
    __syncthreads();
    ln_finish(st2, lane, sc, sh);
    norm_store(xv, sc, sh, fx, w, lane);
    __syncthreads();
    STAMP(DBGC, 7);
    {
        f32x16 acc;
        FFN(acc, 64, DBGC, 8);
#pragma unroll
        for (int r = 0; r < 16; ++r) xv[r] = rbf(__builtin_fmaf(0.5f, rbf(acc[r]), xv[r]));
    }
    ln_partial(xv, st0, w, lane);
    __syncthreads();
    ln_finish(st0, lane, sc, sh);
    {
        uint16_t* op = out + (size_t)(tile * 32 + m) * DM + 32 * w + 4 * h;
#pragma unroll
        for (int g = 0; g < 4; ++g) {
            u32x2 t;
            t[0] = pack2(__builtin_fmaf(xv[4 * g], sc, sh), __builtin_fmaf(xv[4 * g + 1], sc, sh));
            t[1] = pack2(__builtin_fmaf(xv[4 * g + 2], sc, sh), __builtin_fmaf(xv[4 * g + 3], sc, sh));
            *(u32x2*)(op + 8 * g) = t;
        }
    }
    STAMP(DBGC, 11); STAMPR(DBGC, 15);
}

extern "C" void kernel_launch(void* const* d_in, const int* in_sizes, int n_in,
                              void* d_out, int out_size, void* d_ws, size_t ws_size,
                              hipStream_t stream) {
    WPtrs p;
    p.w1a = (const uint16_t*)d_in[1]; p.w1b = (const uint16_t*)d_in[2]; p.wqkv = (const uint16_t*)d_in[3]; p.wo = (const uint16_t*)d_in[4];
    p.wp1 = (const uint16_t*)d_in[5]; p.wp2 = (const uint16_t*)d_in[7]; p.w2a = (const uint16_t*)d_in[8]; p.w2b = (const uint16_t*)d_in[9];
    u32x4* ws = (u32x4*)d_ws;
    k_stageA<<<NTILE + 6, 512, 0, stream>>>((const uint16_t*)d_in[0], p, ws);
    k_attn<<<256, 512, 0, stream>>>(ws, ws);
    k_stageC<<<NTILE, 512, 0, stream>>>(ws, (const uint16_t*)d_in[6], (uint16_t*)d_out);
}
```

```cpp
#include <hip/hip_runtime.h>
#include <hip/hip_bf16.h>
#include <stdint.h>

typedef short bf16x8 __attribute__((ext_vector_type(8)));
typedef float f32x16 __attribute__((ext_vector_type(16)));
typedef float f32x2 __attribute__((ext_vector_type(2)));
typedef float f32x4 __attribute__((ext_vector_type(4)));
typedef unsigned u32x4 __attribute__((ext_vector_type(4)));
typedef unsigned u32x2 __attribute__((ext_vector_type(2)));
typedef __bf16 bf16v2 __attribute__((ext_vector_type(2)));

#define DI __device__ __forceinline__
#define SB() __builtin_amdgcn_sched_barrier(0)
#define STAMPR(base, i) do {} while (0)
#define STAMP(base, i) do {} while (0)
#define MFMA(a, b, c) __builtin_amdgcn_mfma_f32_32x32x16_bf16(__builtin_bit_cast(bf16x8, (a)), __builtin_bit_cast(bf16x8, (b)), (c), 0, 0, 0)

constexpr int T_SEQ = 4000, DM = 256, NTOK = 8000, NTILE = 250, TPB = 125;
constexpr int NFA = 176;
constexpr int NFC = 192;

constexpr size_t WS_WA = 0;
constexpr size_t WS_WC = WS_WA + (size_t)8 * NFA * 64;
constexpr size_t WS_X1 = WS_WC + (size_t)8 * NFC * 64;
constexpr size_t WS_Q  = WS_X1 + (size_t)NTILE * 16 * 64;
constexpr size_t WS_KV = WS_Q + (size_t)8 * TPB * 4 * 64;
constexpr size_t WS_AT = WS_KV + (size_t)8 * TPB * 8 * 64 + 4096;

DI unsigned pack2(float lo, float hi) {
    f32x2 v = {lo, hi};
    bf16v2 r = __builtin_convertvector(v, bf16v2);
    return __builtin_bit_cast(unsigned, r);
}
DI float bflo(unsigned u) { return __uint_as_float(u << 16); }
DI float bfhi(unsigned u) { return __uint_as_float(u & 0xffff0000u); }
DI u32x4 pack8(const f32x16& a, int s) {
    u32x4 r;
    r[0] = pack2(a[8 * s + 0], a[8 * s + 1]);
    r[1] = pack2(a[8 * s + 2], a[8 * s + 3]);
    r[2] = pack2(a[8 * s + 4], a[8 * s + 5]);
    r[3] = pack2(a[8 * s + 6], a[8 * s + 7]);
    return r;
}
DI void unpack16(f32x16& a, const u32x4& f0, const u32x4& f1) {
#pragma unroll
    for (int i = 0; i < 4; ++i) {
        a[2 * i] = bflo(f0[i]); a[2 * i + 1] = bfhi(f0[i]);
        a[8 + 2 * i] = bflo(f1[i]); a[8 + 2 * i + 1] = bfhi(f1[i]);
    }
}
DI float xor32(float v) { return __shfl_xor(v, 32); }
DI float sigmoidf_(float x) { return __builtin_amdgcn_rcpf(1.0f + __builtin_amdgcn_exp2f(-1.4426950408889634f * x)); }
DI float siluf_(float x) { return x * sigmoidf_(x); }

struct PackArgs {
    const uint16_t* w1a; const uint16_t* w1b; const uint16_t* wqkv; const uint16_t* wo;
    const uint16_t* wp1; const uint16_t* wp2; const uint16_t* w2a; const uint16_t* w2b;
    u32x4* ws;
};

__global__ __launch_bounds__(256) void k_pack(PackArgs p) {
    const int gid = blockIdx.x * 256 + threadIdx.x;
    const int lane = gid & 63, fg = gid >> 6;
    if (fg >= 8 * (NFA + NFC)) return;
    const uint16_t* W; int N, nt, ks;
    if (fg < 8 * NFA) {
        const int w = fg / NFA, f = fg % NFA;
        if (f < 64)       { W = p.w1a; N = 1024; ks = f & 15; nt = 4 * w + (f >> 4); }
        else if (f < 128) { W = p.w1b; N = 256; ks = f - 64; nt = w; }
        else              { const int g = f - 128; W = p.wqkv; N = 768; ks = g & 15; nt = w + 8 * (g >> 4); }
    } else {
        const int fg2 = fg - 8 * NFA, w = fg2 / NFC, f = fg2 % NFC;
        if (f < 16)       { W = p.wo; N = 256; ks = f; nt = w; }
        else if (f < 48)  { const int g = f - 16; W = p.wp1; N = 512; ks = g >> 1; nt = w + 8 * (g & 1); }
        else if (f < 64)  { W = p.wp2; N = 256; ks = f - 48; nt = w; }
        else if (f < 128) { const int g = f - 64; W = p.w2a; N = 1024; ks = g & 15; nt = 4 * w + (g >> 4); }
        else              { W = p.w2b; N = 256; ks = f - 128; nt = w; }
    }
    const int r = lane & 31, h = lane >> 5;
    const uint16_t* src = W + (size_t)(16 * ks + 4 * h) * N + 32 * nt + r;
    unsigned e[8];
#pragma unroll
    for (int j = 0; j < 8; ++j) e[j] = src[(size_t)(8 * (j >> 2) + (j & 3)) * N];
    u32x4 o;
#pragma unroll
    for (int i = 0; i < 4; ++i) o[i] = e[2 * i] | (e[2 * i + 1] << 16);
    p.ws[gid] = o;
}

DI void glds16(const void* g, unsigned lds_base) {
    unsigned sv;
    asm volatile("s_mov_b32 %0, m0\n\ts_mov_b32 m0, %2\n\ts_nop 0\n\tglobal_load_lds_dwordx4 %1, off\n\ts_mov_b32 m0, %0"
                 : "=&s"(sv) : "v"(g), "s"(lds_base) : "memory");
}
template <int NI> DI void l2_warm(const u32x4* base, int total_chunks, int rank, int w, int lane, unsigned lds_scratch) {
#pragma unroll
    for (int i = 0; i < NI; ++i) {
        int c = ((rank * NI + i) * 8 + w) * 64;
        c = c + 64 <= total_chunks ? c : total_chunks - 64;
        glds16(base + c + lane, lds_scratch);
    }
}
#define WLOAD(F) ring[(F) & 31] = wp[(F) * 64]
#define REFILL(F, PE) do { if ((F) < 16) WLOAD((F) + 16); if ((F) + 32 < (NFX)) WLOAD((F) + 32); } while (0)
#define LD8(F) do { SB(); _Pragma("unroll") for (int i_ = 0; i_ < 8; ++i_) WLOAD((F) + i_); SB(); } while (0)
#define RAWBAR() do { SB(); __builtin_amdgcn_s_barrier(); SB(); } while (0)

DI void ln_partial(const f32x16& v, float* st, int w, int lane) {
    float s = 0.f, q = 0.f;
#pragma unroll
    for (int i = 0; i < 16; ++i) { s += v[i]; q = __builtin_fmaf(v[i], v[i], q); }
    auto r = __builtin_amdgcn_permlane32_swap(__float_as_uint(s), __float_as_uint(q), false, false);
    st[w * 64 + lane] = __uint_as_float(r[0]) + __uint_as_float(r[1]);
}
DI void ln_finish(const float* st, int lane, float& scale, float& shift) {
    const float* p = st + (lane & 31);
    float s = 0.f, q = 0.f;
#pragma unroll
    for (int i = 0; i < 8; ++i) { s += p[i * 64]; q += p[i * 64 + 32]; }
    const float m = s * (1.0f / 256.0f);
    const float var = fmaxf(q * (1.0f / 256.0f) - m * m, 0.f);
    scale = __builtin_amdgcn_rsqf(var + 1e-5f);
    shift = -m * scale;
}
DI void norm_store(const f32x16& v, float scale, float shift, u32x4* dst  , int w, int lane) {
    f32x16 n;
#pragma unroll
    for (int i = 0; i < 16; ++i) n[i] = __builtin_fmaf(v[i], scale, shift);
    dst[(2 * w) * 64 + lane] = pack8(n, 0);
    dst[(2 * w + 1) * 64 + lane] = pack8(n, 1);
}
DI float rbf(float x) { return bflo(pack2(x, 0.f) & 0xffffu); }

#define GEMM16(acc, F0, PE, fxp, SWAP) do { \
        acc = (f32x16){}; u32x4 bq_[4]; \
        _Pragma("unroll") for (int i_ = 0; i_ < 4; ++i_) bq_[i_] = (fxp)[i_ * 64 + lane]; \
        _Pragma("unroll") for (int ks_ = 0; ks_ < 16; ++ks_) { \
            if (SWAP) acc = MFMA(bq_[ks_ & 3], ring[((F0) + ks_) & 31], acc); else acc = MFMA(ring[((F0) + ks_) & 31], bq_[ks_ & 3], acc); \
            if (ks_ + 4 < 16) bq_[ks_ & 3] = (fxp)[(ks_ + 4) * 64 + lane]; \
            REFILL((F0) + ks_, PE); SB(); } } while (0)

#define FFN(acc, F0, DBG, ST) do { \
        _Pragma("unroll") for (int t_ = 0; t_ < 4; ++t_) { \
            f32x16 a_; GEMM16(a_, (F0) + 16 * t_, (F0) + 64, fx, false); \
            if (t_ == 3) { STAMP(DBG, ST); } \
            _Pragma("unroll") for (int r_ = 0; r_ < 8; ++r_) a_[r_] = siluf_(a_[r_]); \
            fh[((4 * w + t_) * 2 + 0) * 64 + lane] = pack8(a_, 0); \
            _Pragma("unroll") for (int r_ = 8; r_ < 16; ++r_) a_[r_] = siluf_(a_[r_]); \
            fh[((4 * w + t_) * 2 + 1) * 64 + lane] = pack8(a_, 1); } \
        __syncthreads(); \
        STAMP(DBG, ST + 1); \
        acc = (f32x16){}; u32x4 bq_[4]; \
        _Pragma("unroll") for (int i_ = 0; i_ < 4; ++i_) bq_[i_] = fh[i_ * 64 + lane]; \
        _Pragma("unroll") for (int ks_ = 0; ks_ < 64; ++ks_) { \
            acc = MFMA(ring[((F0) + 64 + ks_) & 31], bq_[ks_ & 3], acc); \
            if (ks_ + 4 < 64) bq_[ks_ & 3] = fh[(ks_ + 4) * 64 + lane]; \
            REFILL((F0) + 64 + ks_, (F0) + 128); SB(); } \
        STAMP(DBG, ST + 2); } while (0)

#define DBGA (ws + (8u<<20))
__global__ __launch_bounds__(512, 2) void k_stageA(const uint16_t* __restrict__ x, u32x4* __restrict__ ws) {
    __shared__ u32x4 lds[16 * 64 + 64 * 64 + 256 + 512];
    u32x4* fx = lds;
    u32x4* fh = lds + 16 * 64;
    float* st0 = (float*)(lds + 16 * 64 + 64 * 64);
    float* st1 = st0 + 512;
    const int tile = blockIdx.x, tid = threadIdx.x, lane = tid & 63, h = lane >> 5, m = lane & 31;
    const int w = __builtin_amdgcn_readfirstlane(tid >> 6);
    STAMPR(DBGA, 14); STAMP(DBGA, 0);
    u32x2 xr[4];
    {
        const uint16_t* xp = x + (size_t)(tile * 32 + m) * DM + 32 * w + 4 * h;
#pragma unroll
        for (int g = 0; g < 4; ++g) xr[g] = *(const u32x2*)(xp + 8 * g);
    }
    RAWBAR();
    constexpr int NFX = NFA;
    const u32x4* wp = ws + WS_WA + (size_t)w * NFA * 64 + lane;
    u32x4 ring[32];
    LD8(0); LD8(8);
    f32x16 xv;
#pragma unroll
    for (int g = 0; g < 4; ++g) { xv[4 * g + 0] = bflo(xr[g][0]); xv[4 * g + 1] = bfhi(xr[g][0]); xv[4 * g + 2] = bflo(xr[g][1]); xv[4 * g + 3] = bfhi(xr[g][1]); }
    float sc, sh;
    ln_partial(xv, st0, w, lane);
    __syncthreads();
    ln_finish(st0, lane, sc, sh);
    norm_store(xv, sc, sh, fx, w, lane);
    __syncthreads();
    STAMP(DBGA, 1);
    {
        f32x16 acc;
        FFN(acc, 0, DBGA, 2);
#pragma unroll
        for (int r = 0; r < 16; ++r) xv[r] = rbf(__builtin_fmaf(0.5f, rbf(acc[r]), xv[r]));
    }
    ln_partial(xv, st1, w, lane);
    {
        u32x4* xo = ws + WS_X1 + (size_t)tile * 16 * 64;
        xo[(2 * w) * 64 + lane] = pack8(xv, 0);
        xo[(2 * w + 1) * 64 + lane] = pack8(xv, 1);
    }
    __syncthreads();
    ln_finish(st1, lane, sc, sh);
    norm_store(xv, sc, sh, fx, w, lane);
    __syncthreads();
    STAMP(DBGA, 5);
    {
        const int b = tile / TPB, tt = tile % TPB, bh = b * 4 + (w >> 1), t2 = w & 1;
        u32x4* qo = ws + WS_Q + ((size_t)(bh * TPB + tt) * 4 + 2 * t2) * 64 + lane;
        u32x4* ko = ws + WS_KV + ((size_t)(bh * TPB + tt) * 8 + 2 * t2) * 64 + lane;
        f32x16 a;
        GEMM16(a, 128, NFA, fx, false);
        const float qs = 0.125f * 1.4426950408889634f;
#pragma unroll
        for (int r = 0; r < 16; ++r) a[r] *= qs;
        qo[0] = pack8(a, 0); qo[64] = pack8(a, 1);
        GEMM16(a, 144, NFA, fx, false);
        ko[0] = pack8(a, 0); ko[64] = pack8(a, 1);
        GEMM16(a, 160, NFA, fx, true);
        STAMP(DBGA, 6);
        ko[4 * 64] = pack8(a, 0); ko[5 * 64] = pack8(a, 1);
    }
    STAMP(DBGA, 7); STAMPR(DBGA, 15);
}

#define WAIT_BAR(N) asm volatile("s_waitcnt vmcnt(" #N ") lgkmcnt(0)\n\ts_barrier" ::: "memory")

constexpr int AT_SLOT = 1024;
constexpr float AT_BIG = 1.0e30f;
#define ABL_NODMA 0
#define ABL_NOBAR 0
#define ABL_NOLDS 0
#define ABL_NOEXP 0
#define ABL_NOADD 0
#define PIN(x) asm volatile("" : "+v"(x))
DI float vadd(float a, float b) { float r; asm("v_add_f32 %0, %1, %2" : "=v"(r) : "v"(a), "v"(b)); return r; }
#define EX2(x) __builtin_amdgcn_exp2f(x)

__global__ __launch_bounds__(512, 2) void k_attn(const u32x4* __restrict__ ws_in, u32x4* __restrict__ ws) {
    __shared__ u32x4 lds[4 * 2048];
    const int tid = threadIdx.x, lane = tid & 63;
    const int w = __builtin_amdgcn_readfirstlane(tid >> 6);
    STAMP((ws + (10u<<20)), 0);
    const int bh = blockIdx.x & 7, qb = blockIdx.x >> 3, qi = w & 3, g = w >> 2;
    int qt = 4 * qb + qi; const bool qvalid = qt < TPB; if (!qvalid) qt = TPB - 1;
    const u32x4* kv = ws_in + WS_KV + (size_t)bh * TPB * 8 * 64;
    const unsigned lds0 = (unsigned)(uintptr_t)lds;
    const u32x4* src = kv + (w >> 1) * 512 + (w & 1) * 128 + lane;
    const u32x4* src31 = kv + 124 * 512 + (w & 1) * 128 + lane;
    const unsigned dstk = lds0 + (w >> 1) * 4096 + (w & 1) * 2048;
    const u32x4* srcw = kv + qi * 512 + lane;
    const u32x4* srcw31 = kv + 124 * 512 + lane;
    const unsigned dstw = lds0 + qi * 4096;
#define DMA2(sp, dst) do { glds16((sp), (unsigned)__builtin_amdgcn_readfirstlane(dst)); glds16((sp) + 64, (unsigned)__builtin_amdgcn_readfirstlane((dst) + 1024)); } while (0)
#define DMA_K(step) do { const int st_ = (step); const u32x4* s_ = st_ < 31 ? src + (size_t)st_ * 2048 : src31; DMA2(s_, dstk + (st_ & 3) * 32768); } while (0)
#define DMA_V(step) do { const int st_ = (step); const u32x4* s_ = st_ < 31 ? src + (size_t)st_ * 2048 : src31; DMA2(s_ + 256, dstk + (st_ & 3) * 32768 + 16384); } while (0)
    DMA_K(0); DMA_V(0); DMA_K(1);
    u32x4 q[4];
    {
        const u32x4* qp = ws_in + WS_Q + (size_t)(bh * TPB + qt) * 4 * 64 + lane;
#pragma unroll
        for (int f = 0; f < 4; ++f) q[f] = qp[f * 64];
    }
    DMA_K(2); DMA_V(1);
    const char* lbase = (const char*)lds + (2 * g) * 4096 + lane * 16;
#define KF(off, i) (*(const u32x4*)(lbase + (off) + (i) * 1024))
#define VF(off, i) (*(const u32x4*)(lbase + (off) + 16384 + (i) * 1024))
    f32x16 o0 = (f32x16){}, o1 = (f32x16){};
    float negm;
    f32x4 lsum = (f32x4){0.f, 0.f, 0.f, 0.f};
    u32x4 onesA;
    { const unsigned o_ = ((lane & 15) == ((lane >> 4) & 1)) ? 0x3f803f80u : 0u; onesA = (u32x4){o_, o_, o_, o_}; }
    f32x16 cm, sA, sB;
    u32x4 kf[4], vf[4], pA0, pA1, pB0, pB1;
    WAIT_BAR(4);
    {
#pragma unroll
        for (int i = 0; i < 4; ++i) kf[i] = KF(0, i);
        const f32x16 z = (f32x16){};
        sA = MFMA(kf[0], q[0], z); sA = MFMA(kf[1], q[1], sA); sA = MFMA(kf[2], q[2], sA); sA = MFMA(kf[3], q[3], sA);
#pragma unroll
        for (int i = 0; i < 4; ++i) kf[i] = KF(4096, i);
        float mx = sA[0];
#pragma unroll
        for (int r = 1; r < 16; ++r) mx = fmaxf(mx, sA[r]);
        mx = fmaxf(mx, xor32(mx));
        negm = -mx;
#pragma unroll
        for (int r = 0; r < 16; ++r) { sA[r] += negm; cm[r] = negm; sB[r] = 0.f; }
        pB0 = (u32x4){0u, 0u, 0u, 0u}; pB1 = pB0;
    }
#define FIN(S, i, PK, e) do { PK[e] = pack2(S[i], S[(i) + 1]); PIN(PK); } while (0)
#define EXP2(S, i) do { S[i] = EX2(S[i]); S[(i) + 1] = EX2(S[(i) + 1]); PIN(S); } while (0)
#define LSUM(PK) do { lsum = __builtin_amdgcn_mfma_f32_16x16x32_bf16(__builtin_bit_cast(bf16x8, onesA), __builtin_bit_cast(bf16x8, PK), lsum, 0, 0, 0); PIN(lsum); } while (0)
#define PERIOD(SC, SN, PP0, PP1, PC0, PC1, koff, voff, D0, D1) do { \
        vf[0] = VF(voff, 0); FIN(SN, 14, PP1, 3); SB(); SN = MFMA(kf[0], q[0], cm); PIN(SN); EXP2(SC, 0); SB(); \
        vf[2] = VF(voff, 2); SB(); SN = MFMA(kf[1], q[1], SN); PIN(SN); EXP2(SC, 2); FIN(SC, 0, PC0, 0); SB(); \
        vf[1] = VF(voff, 1); SB(); SN = MFMA(kf[2], q[2], SN); PIN(SN); EXP2(SC, 4); FIN(SC, 2, PC0, 1); SB(); D0; SB(); \
        vf[3] = VF(voff, 3); SB(); SN = MFMA(kf[3], q[3], SN); PIN(SN); EXP2(SC, 6); FIN(SC, 4, PC0, 2); SB(); \
        kf[0] = KF(koff, 0); SB(); o0 = MFMA(vf[0], PP0, o0); PIN(o0); EXP2(SC, 8); FIN(SC, 6, PC0, 3); SB(); LSUM(PP0); SB(); \
        kf[1] = KF(koff, 1); SB(); o1 = MFMA(vf[2], PP0, o1); PIN(o1); EXP2(SC, 10); FIN(SC, 8, PC1, 0); SB(); \
        kf[2] = KF(koff, 2); SB(); o0 = MFMA(vf[1], PP1, o0); PIN(o0); EXP2(SC, 12); FIN(SC, 10, PC1, 1); SB(); LSUM(PP1); SB(); D1; SB(); \
        kf[3] = KF(koff, 3); SB(); o1 = MFMA(vf[3], PP1, o1); PIN(o1); EXP2(SC, 14); FIN(SC, 12, PC1, 2); SB(); \
    } while (0)
    int tk0 = 0, tk1 = 0, tk2 = 0, acc_c = 0, acc_w = 0;
#define TICK(x) do {} while (0)
    int vprev = 0;
    for (int i = 0; i < 31; ++i) {
        const int scur = (i & 3) * 32768, snext = ((i + 1) & 3) * 32768;
        const int sk = i + 3 < 32 ? i + 3 : 31, sv = i + 2 < 32 ? i + 2 : 31;
        const u32x4* pk_ = sk < 31 ? srcw + (size_t)sk * 2048 : srcw31;
        const u32x4* pv_ = (sv < 31 ? srcw + (size_t)sv * 2048 : srcw31) + 256;
        const unsigned dk_ = dstw + (sk & 3) * 32768, dv_ = dstw + (sv & 3) * 32768 + 16384;
#define G2(p, d) do { if (!ABL_NODMA && g == 0) { glds16((p), (unsigned)__builtin_amdgcn_readfirstlane(d)); glds16((p) + 64, (unsigned)__builtin_amdgcn_readfirstlane((d) + 1024)); } } while (0)
        SB();
        PERIOD(sA, sB, pB0, pB1, pA0, pA1, snext, vprev, G2(pk_, dk_), G2(pk_ + 128, dk_ + 2048));
        PERIOD(sB, sA, pA0, pA1, pB0, pB1, snext + 4096, scur, G2(pv_, dv_), G2(pv_ + 128, dv_ + 2048));
        vprev = scur + 4096;
        TICK(tk1); if (!ABL_NOBAR) { if (g) WAIT_BAR(0); else WAIT_BAR(8); } TICK(tk2); acc_c += tk1 - tk0; acc_w += tk2 - tk1; tk0 = tk2;
    }
    {
        if (g == 1) {
#pragma unroll
            for (int r = 0; r < 16; ++r) sA[r] = -INFINITY;
        }
        PERIOD(sA, sB, pB0, pB1, pA0, pA1, 0, vprev, (void)0, (void)0);
#pragma unroll
        for (int r = 0; r < 16; ++r) sB[r] = -INFINITY;
        PERIOD(sB, sA, pA0, pA1, pB0, pB1, 0, 3 * 32768, (void)0, (void)0);
    }
    float l;
    { const float la = __shfl(lsum[0], lane & 15), lb = __shfl(lsum[1], lane & 15); l = 0.5f * ((lane & 16) ? lb : la); }
    WAIT_BAR(0);
#undef DMA_K
#undef DMA_V
#undef DMA2
#undef PERIOD
#undef FIN
#undef EXP2
#undef LSUM
#undef G2
#undef KF
#undef VF
    {
        bool bad = !(l < AT_BIG);
#pragma unroll
        for (int r = 0; r < 16; ++r) bad = bad || !(fabsf(o0[r]) < INFINITY) || !(fabsf(o1[r]) < INFINITY);
        if (__builtin_expect(__any(bad), 0)) {
            o0 = (f32x16){}; o1 = (f32x16){}; l = 0.f; float mrun = -INFINITY;
            for (int kt = 0; kt < TPB; ++kt) {
                if (((kt >> 1) & 1) != g) continue;
                const u32x4* kg = kv + (size_t)kt * 512 + lane;
                f32x16 sc = (f32x16){};
#pragma unroll
                for (int f = 0; f < 4; ++f) sc = MFMA(kg[f * 64], q[f], sc);
                float mx = sc[0];
#pragma unroll
                for (int r = 1; r < 16; ++r) mx = fmaxf(mx, sc[r]);
                mx = fmaxf(mx, xor32(mx));
                const float mn = fmaxf(mrun, mx), f = EX2(mrun - mn);
                mrun = mn; l *= f;
#pragma unroll
                for (int r = 0; r < 16; ++r) { o0[r] *= f; o1[r] *= f; sc[r] = EX2(sc[r] - mn); l += sc[r]; }
                const u32x4 p0 = pack8(sc, 0), p1 = pack8(sc, 1);
                o0 = MFMA(kg[256], p0, o0); o1 = MFMA(kg[256 + 128], p0, o1);
                o0 = MFMA(kg[256 + 64], p1, o0); o1 = MFMA(kg[256 + 192], p1, o1);
            }
            negm = -mrun;
        }
    }
    l += xor32(l);
    float* cf = (float*)lds;
    if (g == 1) {
        float* dst = cf + qi * 34 * 64 + lane;
#pragma unroll
        for (int r = 0; r < 16; ++r) { dst[r * 64] = o0[r]; dst[(16 + r) * 64] = o1[r]; }
        dst[32 * 64] = negm; dst[33 * 64] = l;
    }
    __syncthreads();
    if (g == 0) {
        const float* s = cf + qi * 34 * 64 + lane;
        const float negm1 = s[32 * 64], l1 = s[33 * 64];
        const float nm = fminf(negm, negm1);
        const float f0 = __builtin_amdgcn_exp2f(nm - negm), f1 = __builtin_amdgcn_exp2f(nm - negm1);
        const float inv = __builtin_amdgcn_rcpf(l * f0 + l1 * f1);
        const float c0 = f0 * inv, c1 = f1 * inv;
#pragma unroll
        for (int r = 0; r < 16; ++r) { o0[r] = o0[r] * c0 + s[r * 64] * c1; o1[r] = o1[r] * c0 + s[(16 + r) * 64] * c1; }
        if (qvalid) {
            const int b = bh >> 2, hd = bh & 3;
            u32x4* ao = ws + WS_AT + ((size_t)(b * TPB + qt) * 16 + 4 * hd) * 64 + lane;
            ao[0] = pack8(o0, 0); ao[64] = pack8(o0, 1); ao[128] = pack8(o1, 0); ao[192] = pack8(o1, 1);
        }
    }
    STAMPR((ws + (10u<<20)), 15);
}

#define DBGC ((u32x4*)ws + (9u<<20))
__global__ __launch_bounds__(512, 2) void k_stageC(const u32x4* __restrict__ ws, const uint16_t* __restrict__ dw, uint16_t* __restrict__ out) {
    __shared__ u32x4 lds[32 * 64 + 64 * 64 + 384 + 160 + 512];
    u32x4* fx = lds;
    u32x4* fy = lds + 16 * 64;
    u32x4* fh = lds + 32 * 64;
    float* st0 = (float*)(lds + 32 * 64 + 64 * 64);
    float* st1 = st0 + 512;
    float* st2 = st0 + 1024;
    u32x4* dwl = lds + 32 * 64 + 64 * 64 + 384;
    const int tile = blockIdx.x, tid = threadIdx.x, lane = tid & 63, h = lane >> 5, m = lane & 31;
    const int w = __builtin_amdgcn_readfirstlane(tid >> 6);
    STAMPR(DBGC, 14); STAMP(DBGC, 0);

    const int tt = tile % TPB;
    const int hm = m & 3;
    int toff = hm < 2 ? hm - 2 : 30 + hm;
    const int tl = tt * 32 + toff;
    const bool hvalid = (tl >= 0) && (tl < T_SEQ);
    if (!hvalid) toff = 0;
    const int gtok = tile * 32 + toff;
    const size_t hbase = (size_t)(gtok >> 5) * 16 * 64 + h * 32 + (gtok & 31);

    u32x4 in[8];
    {
        const u32x4* at = ws + WS_AT;
        const u32x4* x1 = ws + WS_X1;
        const size_t mb = (size_t)tile * 16 * 64 + lane;
        in[0] = at[mb + (2 * w) * 64]; in[1] = at[mb + (2 * w + 1) * 64];
        in[2] = at[hbase + (2 * w) * 64]; in[3] = at[hbase + (2 * w + 1) * 64];
        in[4] = x1[mb + (2 * w) * 64]; in[5] = x1[mb + (2 * w + 1) * 64];
        in[6] = x1[hbase + (2 * w) * 64]; in[7] = x1[hbase + (2 * w + 1) * 64];
    }
    u32x4 dwr;
    if (tid < 160) dwr = ((const u32x4*)dw)[tid];
    RAWBAR();
    constexpr int NFX = NFC;
    const u32x4* wp = ws + WS_WC + (size_t)w * NFC * 64 + lane;
    u32x4 ring[32];
    LD8(0); LD8(8);
    f32x16 xv, yv;
    fx[(2 * w) * 64 + lane] = in[0]; fx[(2 * w + 1) * 64 + lane] = in[1];
    fy[(2 * w) * 64 + lane] = in[2]; fy[(2 * w + 1) * 64 + lane] = in[3];
    if (tid < 160) dwl[tid] = dwr;
    unpack16(xv, in[4], in[5]);
    unpack16(yv, in[6], in[7]);
    __syncthreads();
    STAMP(DBGC, 1);
    {
        f32x16 a = (f32x16){}, b = (f32x16){};
        u32x4 bx[2], by[2];
        bx[0] = fx[lane]; by[0] = fy[lane]; bx[1] = fx[64 + lane]; by[1] = fy[64 + lane];
#pragma unroll
        for (int ks = 0; ks < 16; ++ks) {
            a = MFMA(ring[ks & 31], bx[ks & 1], a);
            b = MFMA(ring[ks & 31], by[ks & 1], b);
            if (ks + 2 < 16) { bx[ks & 1] = fx[(ks + 2) * 64 + lane]; by[ks & 1] = fy[(ks + 2) * 64 + lane]; }
            REFILL(ks, 0);
            SB();
        }
        STAMP(DBGC, 2);
#pragma unroll
        for (int r = 0; r < 16; ++r) { xv[r] = rbf(xv[r] + rbf(a[r])); yv[r] = rbf(yv[r] + rbf(b[r])); }
    }
    float sc, sh, sc2, sh2;
    ln_partial(xv, st0, w, lane);
    ln_partial(yv, st1, w, lane);
    __syncthreads();
    ln_finish(st0, lane, sc, sh);
    ln_finish(st1, lane, sc2, sh2);
    norm_store(xv, sc, sh, fx, w, lane);
    norm_store(yv, sc2, sh2, fy, w, lane);
    __syncthreads();
    STAMP(DBGC, 3);
    {
        f32x16 a = (f32x16){}, b = (f32x16){}, c = (f32x16){}, d = (f32x16){};
        u32x4 bxn = fx[lane], byn = fy[lane];
#pragma unroll
        for (int ks = 0; ks < 16; ++ks) {
            const int F = 16 + 2 * ks;
            const u32x4 bx = bxn, by = byn;
            if (ks + 1 < 16) { bxn = fx[(ks + 1) * 64 + lane]; byn = fy[(ks + 1) * 64 + lane]; }
            a = MFMA(ring[F & 31], bx, a); c = MFMA(ring[F & 31], by, c); REFILL(F, 0);
            b = MFMA(ring[(F + 1) & 31], bx, b); d = MFMA(ring[(F + 1) & 31], by, d); REFILL(F + 1, 0);
            SB();
        }
        STAMP(DBGC, 4);
#pragma unroll
        for (int r = 0; r < 16; ++r) {
            a[r] = a[r] * sigmoidf_(b[r]);
            c[r] = hvalid ? c[r] * sigmoidf_(d[r]) : 0.f;
        }
        f32x4* cs = (f32x4*)fh + w * (4 * 2 * 36);
#pragma unroll
        for (int g = 0; g < 4; ++g) {
            cs[(g * 2 + h) * 36 + m + 2] = (f32x4){a[4 * g], a[4 * g + 1], a[4 * g + 2], a[4 * g + 3]};
            if (m < 4) cs[(g * 2 + h) * 36 + (m < 2 ? m : 32 + m)] = (f32x4){c[4 * g], c[4 * g + 1], c[4 * g + 2], c[4 * g + 3]};
        }
        __syncthreads();
        const uint16_t* dp = (const uint16_t*)dwl + 32 * w + 4 * h;
#pragma unroll
        for (int g = 0; g < 4; ++g) {
            f32x4 acc = (f32x4){0.f, 0.f, 0.f, 0.f};
#pragma unroll
            for (int i = 0; i < 5; ++i) {
                const f32x4 hv = cs[(g * 2 + h) * 36 + m + i];
                const u32x2 t = *(const u32x2*)(dp + i * DM + 8 * g);
                acc[0] += hv[0] * bflo(t[0]); acc[1] += hv[1] * bfhi(t[0]); acc[2] += hv[2] * bflo(t[1]); acc[3] += hv[3] * bfhi(t[1]);
            }
#pragma unroll
            for (int i = 0; i < 4; ++i) a[4 * g + i] = siluf_(acc[i]);
        }
        fx[(2 * w) * 64 + lane] = pack8(a, 0);
        fx[(2 * w + 1) * 64 + lane] = pack8(a, 1);
    }
    __syncthreads();
    STAMP(DBGC, 5);
    {
        f32x16 a;
        GEMM16(a, 48, 64, fx, false);
        STAMP(DBGC, 6);
#pragma unroll
        for (int r = 0; r < 16; ++r) xv[r] = rbf(xv[r] + rbf(a[r]));
    }
    ln_partial(xv, st2, w, lane);
    __syncthreads();
    ln_finish(st2, lane, sc, sh);
    norm_store(xv, sc, sh, fx, w, lane);
    __syncthreads();
    STAMP(DBGC, 7);
    {
        f32x16 acc;
        FFN(acc, 64, DBGC, 8);
#pragma unroll
        for (int r = 0; r < 16; ++r) xv[r] = rbf(__builtin_fmaf(0.5f, rbf(acc[r]), xv[r]));
    }
    ln_partial(xv, st0, w, lane);
    __syncthreads();
    ln_finish(st0, lane, sc, sh);
    {
        uint16_t* op = out + (size_t)(tile * 32 + m) * DM + 32 * w + 4 * h;
#pragma unroll
        for (int g = 0; g < 4; ++g) {
            u32x2 t;
            t[0] = pack2(__builtin_fmaf(xv[4 * g], sc, sh), __builtin_fmaf(xv[4 * g + 1], sc, sh));
            t[1] = pack2(__builtin_fmaf(xv[4 * g + 2], sc, sh), __builtin_fmaf(xv[4 * g + 3], sc, sh));
            *(u32x2*)(op + 8 * g) = t;
        }
    }
    STAMP(DBGC, 11); STAMPR(DBGC, 15);
}

extern "C" void kernel_launch(void* const* d_in, const int* in_sizes, int n_in,
                              void* d_out, int out_size, void* d_ws, size_t ws_size,
                              hipStream_t stream) {
    PackArgs p;
    p.w1a = (const uint16_t*)d_in[1]; p.w1b = (const uint16_t*)d_in[2]; p.wqkv = (const uint16_t*)d_in[3]; p.wo = (const uint16_t*)d_in[4];
    p.wp1 = (const uint16_t*)d_in[5]; p.wp2 = (const uint16_t*)d_in[7]; p.w2a = (const uint16_t*)d_in[8]; p.w2b = (const uint16_t*)d_in[9];
    p.ws = (u32x4*)d_ws;
    u32x4* ws = (u32x4*)d_ws;
    const int nchunk = 8 * (NFA + NFC) * 64;
    k_pack<<<(nchunk + 255) / 256, 256, 0, stream>>>(p);
    k_stageA<<<NTILE, 512, 0, stream>>>((const uint16_t*)d_in[0], ws);
    k_attn<<<256, 512, 0, stream>>>(ws, ws);
    k_stageC<<<NTILE, 512, 0, stream>>>(ws, (const uint16_t*)d_in[6], (uint16_t*)d_out);
}
```

```cpp
#include <hip/hip_runtime.h>
#include <hip/hip_bf16.h>
#include <stdint.h>

typedef short bf16x8 __attribute__((ext_vector_type(8)));
typedef float f32x16 __attribute__((ext_vector_type(16)));
typedef float f32x2 __attribute__((ext_vector_type(2)));
typedef float f32x4 __attribute__((ext_vector_type(4)));
typedef unsigned u32x4 __attribute__((ext_vector_type(4)));
typedef unsigned u32x2 __attribute__((ext_vector_type(2)));
typedef __bf16 bf16v2 __attribute__((ext_vector_type(2)));

#define DI __device__ __forceinline__
#define SB() __builtin_amdgcn_sched_barrier(0)
#define STAMPR(base, i) do {} while (0)
#define STAMP(base, i) do {} while (0)
#define MFMA(a, b, c) __builtin_amdgcn_mfma_f32_32x32x16_bf16(__builtin_bit_cast(bf16x8, (a)), __builtin_bit_cast(bf16x8, (b)), (c), 0, 0, 0)

constexpr int T_SEQ = 4000, DM = 256, NTOK = 8000, NTILE = 250, TPB = 125;
constexpr int NFA = 112;
constexpr int NFC = 192;

constexpr size_t WS_WA = 0;
constexpr size_t WS_WC = WS_WA + (size_t)8 * NFA * 64;
constexpr size_t WS_X1 = WS_WC + (size_t)8 * NFC * 64;
constexpr size_t WS_Q  = WS_X1 + (size_t)NTILE * 16 * 64;
constexpr size_t WS_KV = WS_Q + (size_t)8 * TPB * 4 * 64;
constexpr size_t WS_AT = WS_KV + (size_t)8 * TPB * 8 * 64 + 4096;
constexpr size_t WS_H  = WS_AT + (size_t)NTILE * 16 * 64;

DI unsigned pack2(float lo, float hi) {
    f32x2 v = {lo, hi};
    bf16v2 r = __builtin_convertvector(v, bf16v2);
    return __builtin_bit_cast(unsigned, r);
}
DI float bflo(unsigned u) { return __uint_as_float(u << 16); }
DI float bfhi(unsigned u) { return __uint_as_float(u & 0xffff0000u); }
DI u32x4 pack8(const f32x16& a, int s) {
    u32x4 r;
    r[0] = pack2(a[8 * s + 0], a[8 * s + 1]);
    r[1] = pack2(a[8 * s + 2], a[8 * s + 3]);
    r[2] = pack2(a[8 * s + 4], a[8 * s + 5]);
    r[3] = pack2(a[8 * s + 6], a[8 * s + 7]);
    return r;
}
DI void unpack16(f32x16& a, const u32x4& f0, const u32x4& f1) {
#pragma unroll
    for (int i = 0; i < 4; ++i) {
        a[2 * i] = bflo(f0[i]); a[2 * i + 1] = bfhi(f0[i]);
        a[8 + 2 * i] = bflo(f1[i]); a[8 + 2 * i + 1] = bfhi(f1[i]);
    }
}
DI float xor32(float v) { return __shfl_xor(v, 32); }
DI float sigmoidf_(float x) { return __builtin_amdgcn_rcpf(1.0f + __builtin_amdgcn_exp2f(-1.4426950408889634f * x)); }
DI float siluf_(float x) { return x * sigmoidf_(x); }

struct PackArgs {
    const uint16_t* x; const uint16_t* w1a; const uint16_t* w1b; const uint16_t* wqkv; const uint16_t* wo;
    const uint16_t* wp1; const uint16_t* wp2; const uint16_t* w2a; const uint16_t* w2b;
    u32x4* ws;
};
DI void glds16(const void* g, unsigned lds_base) {
    unsigned sv;
    asm volatile("s_mov_b32 %0, m0\n\ts_mov_b32 m0, %2\n\ts_nop 0\n\tglobal_load_lds_dwordx4 %1, off\n\ts_mov_b32 m0, %0"
                 : "=&s"(sv) : "v"(g), "s"(lds_base) : "memory");
}
typedef short s16x4 __attribute__((ext_vector_type(4)));
typedef __attribute__((address_space(3))) const char* lds_cptr;
DI u32x2 trread(lds_cptr p) { return __builtin_bit_cast(u32x2, __builtin_amdgcn_ds_read_tr16_b64_v4i16((__attribute__((address_space(3))) s16x4*)p)); }

DI void pack_chunk(const PackArgs& p, int gid) {
    const int lane = gid & 63, fg = gid >> 6;
    const uint16_t* W; int N, nt, ks;
    if (fg < 8 * NFA) {
        const int w = fg / NFA, f = fg % NFA;
        if (f < 64)       { W = p.w1b; N = 256; ks = f; nt = w; }
        else              { const int g = f - 64; W = p.wqkv; N = 768; ks = g & 15; nt = w + 8 * (g >> 4); }
    } else {
        const int fg2 = fg - 8 * NFA, w = fg2 / NFC, f = fg2 % NFC;
        if (f < 16)       { W = p.wo; N = 256; ks = f; nt = w; }
        else if (f < 48)  { const int g = f - 16; W = p.wp1; N = 512; ks = g >> 1; nt = w + 8 * (g & 1); }
        else if (f < 64)  { W = p.wp2; N = 256; ks = f - 48; nt = w; }
        else if (f < 128) { const int g = f - 64; W = p.w2a; N = 1024; ks = g & 15; nt = 4 * w + (g >> 4); }
        else              { W = p.w2b; N = 256; ks = f - 128; nt = w; }
    }
    const int r = lane & 31, h = lane >> 5;
    const uint16_t* src = W + (size_t)(16 * ks + 4 * h) * N + 32 * nt + r;
    unsigned e[8];
#pragma unroll
    for (int j = 0; j < 8; ++j) e[j] = src[(size_t)(8 * (j >> 2) + (j & 3)) * N];
    u32x4 o;
#pragma unroll
    for (int i = 0; i < 4; ++i) o[i] = e[2 * i] | (e[2 * i + 1] << 16);
    p.ws[gid] = o;
}

constexpr int K0_BLOCKS = 252;
__global__ __launch_bounds__(512, 2) void k_ff1a(PackArgs p) {
    __shared__ __attribute__((aligned(16))) char smem[131072];
    const int tid = threadIdx.x, lane = tid & 63, h = lane >> 5, m = lane & 31;
    const int w = __builtin_amdgcn_readfirstlane(tid >> 6);
    const int tb = blockIdx.x >> 2, hb = blockIdx.x & 3, tw = w & 3, nh = w >> 2;
    int tile = 4 * tb + tw; const bool valid = tile < NTILE; if (!valid) tile = NTILE - 1;
    u32x2 xr[32];
    {
        const uint16_t* xp = p.x + (size_t)(tile * 32 + m) * DM + 4 * h;
#pragma unroll
        for (int i = 0; i < 32; ++i) xr[i] = *(const u32x2*)(xp + 8 * i);
    }
    SB();
    {
        const char* src = (const char*)p.w1a + hb * 512 + (lane >> 3) * 2048 + (((lane & 7) ^ ((lane >> 4 & 1) << 2)) * 16);
        const unsigned l0 = (unsigned)(uintptr_t)smem;
#pragma unroll
        for (int i = 0; i < 16; ++i) {
            const int pc = 16 * w + i;
            glds16(src + (size_t)(pc >> 2) * 8 * 2048 + (pc & 3) * 128, (unsigned)__builtin_amdgcn_readfirstlane(l0 + pc * 1024));
        }
    }
    SB();
    for (int c = blockIdx.x * 512 + tid; c < 8 * (NFA + NFC) * 64; c += K0_BLOCKS * 512) pack_chunk(p, c);
    SB();
    u32x4 bf[16];
    {
        float s = 0.f, q = 0.f;
#pragma unroll
        for (int i = 0; i < 32; ++i) {
            const float a0 = bflo(xr[i][0]), a1 = bfhi(xr[i][0]), a2 = bflo(xr[i][1]), a3 = bfhi(xr[i][1]);
            s += (a0 + a1) + (a2 + a3);
            q = __builtin_fmaf(a0, a0, q); q = __builtin_fmaf(a1, a1, q); q = __builtin_fmaf(a2, a2, q); q = __builtin_fmaf(a3, a3, q);
        }
        s += xor32(s); q += xor32(q);
        const float mean = s * (1.0f / 256.0f);
        const float sc = __builtin_amdgcn_rsqf(fmaxf(q * (1.0f / 256.0f) - mean * mean, 0.f) + 1e-5f), sh = -mean * sc;
#pragma unroll
        for (int i = 0; i < 32; ++i) {
            bf[i >> 1][2 * (i & 1)] = pack2(__builtin_fmaf(bflo(xr[i][0]), sc, sh), __builtin_fmaf(bfhi(xr[i][0]), sc, sh));
            bf[i >> 1][2 * (i & 1) + 1] = pack2(__builtin_fmaf(bflo(xr[i][1]), sc, sh), __builtin_fmaf(bfhi(xr[i][1]), sc, sh));
        }
    }
    asm volatile("s_waitcnt vmcnt(0)\n\ts_barrier" ::: "memory");
    const int G = lane >> 4, q4 = (lane >> 2) & 3, p4 = lane & 3;
    const lds_cptr a0p = (lds_cptr)smem + nh * 2048 + (4 * (G >> 1) + q4) * 128 + ((q4 >> 1) * 64) + (G & 1) * 32 + p4 * 8;
    const lds_cptr a1p = (lds_cptr)smem + nh * 2048 + (((4 * (G >> 1) + q4) * 128 + ((q4 >> 1) * 64) + (G & 1) * 32 + p4 * 8) ^ 64);
    f32x16 acc[4];
#pragma unroll
    for (int t = 0; t < 4; ++t) acc[t] = (f32x16){};
#pragma unroll
    for (int ks = 0; ks < 16; ++ks) {
#pragma unroll
        for (int t = 0; t < 4; ++t) {
            const lds_cptr ap = ((t & 1) ? a1p : a0p) + ks * 8192 + (t >> 1) * 1024;
            const u32x2 lo = trread(ap), hi = trread(ap + 4096);
            acc[t] = MFMA(((u32x4){lo[0], lo[1], hi[0], hi[1]}), bf[ks], acc[t]);
        }
    }
    if (valid) {
        u32x4* ho = p.ws + WS_H + ((size_t)tile * 64 + 2 * (8 * hb + 4 * nh)) * 64 + lane;
#pragma unroll
        for (int t = 0; t < 4; ++t) {
#pragma unroll
            for (int r = 0; r < 16; ++r) acc[t][r] = siluf_(acc[t][r]);
            ho[(2 * t) * 64] = pack8(acc[t], 0);
            ho[(2 * t + 1) * 64] = pack8(acc[t], 1);
        }
    }
}

template <int NI> DI void l2_warm(const u32x4* base, int total_chunks, int rank, int w, int lane, unsigned lds_scratch) {
#pragma unroll
    for (int i = 0; i < NI; ++i) {
        int c = ((rank * NI + i) * 8 + w) * 64;
        c = c + 64 <= total_chunks ? c : total_chunks - 64;
        glds16(base + c + lane, lds_scratch);
    }
}
#define WLOAD(F) ring[(F) & 31] = wp[(F) * 64]
#define REFILL(F, PE) do { if ((F) < 16) WLOAD((F) + 16); if ((F) + 32 < (NFX)) WLOAD((F) + 32); } while (0)
#define LD8(F) do { SB(); _Pragma("unroll") for (int i_ = 0; i_ < 8; ++i_) WLOAD((F) + i_); SB(); } while (0)
#define RAWBAR() do { SB(); __builtin_amdgcn_s_barrier(); SB(); } while (0)

DI void ln_partial(const f32x16& v, float* st, int w, int lane) {
    float s = 0.f, q = 0.f;
#pragma unroll
    for (int i = 0; i < 16; ++i) { s += v[i]; q = __builtin_fmaf(v[i], v[i], q); }
    auto r = __builtin_amdgcn_permlane32_swap(__float_as_uint(s), __float_as_uint(q), false, false);
    st[w * 64 + lane] = __uint_as_float(r[0]) + __uint_as_float(r[1]);
}
DI void ln_finish(const float* st, int lane, float& scale, float& shift) {
    const float* p = st + (lane & 31);
    float s = 0.f, q = 0.f;
#pragma unroll
    for (int i = 0; i < 8; ++i) { s += p[i * 64]; q += p[i * 64 + 32]; }
    const float m = s * (1.0f / 256.0f);
    const float var = fmaxf(q * (1.0f / 256.0f) - m * m, 0.f);
    scale = __builtin_amdgcn_rsqf(var + 1e-5f);
    shift = -m * scale;
}
DI void norm_store(const f32x16& v, float scale, float shift, u32x4* dst  , int w, int lane) {
    f32x16 n;
#pragma unroll
    for (int i = 0; i < 16; ++i) n[i] = __builtin_fmaf(v[i], scale, shift);
    dst[(2 * w) * 64 + lane] = pack8(n, 0);
    dst[(2 * w + 1) * 64 + lane] = pack8(n, 1);
}
DI float rbf(float x) { return bflo(pack2(x, 0.f) & 0xffffu); }

#define GEMM16(acc, F0, PE, fxp, SWAP) do { \
        acc = (f32x16){}; u32x4 bq_[4]; \
        _Pragma("unroll") for (int i_ = 0; i_ < 4; ++i_) bq_[i_] = (fxp)[i_ * 64 + lane]; \
        _Pragma("unroll") for (int ks_ = 0; ks_ < 16; ++ks_) { \
            if (SWAP) acc = MFMA(bq_[ks_ & 3], ring[((F0) + ks_) & 31], acc); else acc = MFMA(ring[((F0) + ks_) & 31], bq_[ks_ & 3], acc); \
            if (ks_ + 4 < 16) bq_[ks_ & 3] = (fxp)[(ks_ + 4) * 64 + lane]; \
            REFILL((F0) + ks_, PE); SB(); } } while (0)

#define FFN(acc, F0, DBG, ST) do { \
        _Pragma("unroll") for (int t_ = 0; t_ < 4; ++t_) { \
            f32x16 a_; GEMM16(a_, (F0) + 16 * t_, (F0) + 64, fx, false); \
            if (t_ == 3) { STAMP(DBG, ST); } \
            _Pragma("unroll") for (int r_ = 0; r_ < 8; ++r_) a_[r_] = siluf_(a_[r_]); \
            fh[((4 * w + t_) * 2 + 0) * 64 + lane] = pack8(a_, 0); \
            _Pragma("unroll") for (int r_ = 8; r_ < 16; ++r_) a_[r_] = siluf_(a_[r_]); \
            fh[((4 * w + t_) * 2 + 1) * 64 + lane] = pack8(a_, 1); } \
        __syncthreads(); \
        STAMP(DBG, ST + 1); \
        acc = (f32x16){}; u32x4 bq_[4]; \
        _Pragma("unroll") for (int i_ = 0; i_ < 4; ++i_) bq_[i_] = fh[i_ * 64 + lane]; \
        _Pragma("unroll") for (int ks_ = 0; ks_ < 64; ++ks_) { \
            acc = MFMA(ring[((F0) + 64 + ks_) & 31], bq_[ks_ & 3], acc); \
            if (ks_ + 4 < 64) bq_[ks_ & 3] = fh[(ks_ + 4) * 64 + lane]; \
            REFILL((F0) + 64 + ks_, (F0) + 128); SB(); } \
        STAMP(DBG, ST + 2); } while (0)

#define DBGA (ws + (8u<<20))
__global__ __launch_bounds__(512, 2) void k_stageA(const uint16_t* __restrict__ x, u32x4* __restrict__ ws) {
    __shared__ u32x4 lds[16 * 64 + 64 * 64 + 256 + 512];
    u32x4* fx = lds;
    u32x4* fh = lds + 16 * 64;
    float* st0 = (float*)(lds + 16 * 64 + 64 * 64);
    float* st1 = st0 + 512;
    const int tile = blockIdx.x, tid = threadIdx.x, lane = tid & 63, h = lane >> 5, m = lane & 31;
    const int w = __builtin_amdgcn_readfirstlane(tid >> 6);
    STAMPR(DBGA, 14); STAMP(DBGA, 0);
    u32x2 xr[4];
    {
        const uint16_t* xp = x + (size_t)(tile * 32 + m) * DM + 32 * w + 4 * h;
#pragma unroll
        for (int g = 0; g < 4; ++g) xr[g] = *(const u32x2*)(xp + 8 * g);
    }
    {
        const u32x4* hp = ws + WS_H + ((size_t)tile * 64 + 8 * w) * 64 + lane;
        const unsigned l0 = (unsigned)(uintptr_t)(fh + 8 * w * 64);
#pragma unroll
        for (int i = 0; i < 8; ++i) glds16(hp + i * 64, (unsigned)__builtin_amdgcn_readfirstlane(l0 + i * 1024));
    }
    RAWBAR();
    constexpr int NFX = NFA;
    const u32x4* wp = ws + WS_WA + (size_t)w * NFA * 64 + lane;
    u32x4 ring[32];
    LD8(0); LD8(8);
    f32x16 xv;
#pragma unroll
    for (int g = 0; g < 4; ++g) { xv[4 * g + 0] = bflo(xr[g][0]); xv[4 * g + 1] = bfhi(xr[g][0]); xv[4 * g + 2] = bflo(xr[g][1]); xv[4 * g + 3] = bfhi(xr[g][1]); }
    float sc, sh;
    asm volatile("s_waitcnt vmcnt(16)\n\ts_barrier" ::: "memory");
    STAMP(DBGA, 1); STAMP(DBGA, 2); STAMP(DBGA, 3);
    {
        f32x16 acc = (f32x16){}; u32x4 bq[4];
#pragma unroll
        for (int i = 0; i < 4; ++i) bq[i] = fh[i * 64 + lane];
#pragma unroll
        for (int ks = 0; ks < 64; ++ks) {
            acc = MFMA(ring[ks & 31], bq[ks & 3], acc);
            if (ks + 4 < 64) bq[ks & 3] = fh[(ks + 4) * 64 + lane];
            REFILL(ks, 0); SB();
        }
        STAMP(DBGA, 4);
#pragma unroll
        for (int r = 0; r < 16; ++r) xv[r] = rbf(__builtin_fmaf(0.5f, rbf(acc[r]), xv[r]));
    }
    ln_partial(xv, st1, w, lane);
    {
        u32x4* xo = ws + WS_X1 + (size_t)tile * 16 * 64;
        xo[(2 * w) * 64 + lane] = pack8(xv, 0);
        xo[(2 * w + 1) * 64 + lane] = pack8(xv, 1);
    }
    __syncthreads();
    ln_finish(st1, lane, sc, sh);
    norm_store(xv, sc, sh, fx, w, lane);
    __syncthreads();
    STAMP(DBGA, 5);
    {
        const int b = tile / TPB, tt = tile % TPB, bh = b * 4 + (w >> 1), t2 = w & 1;
        u32x4* qo = ws + WS_Q + ((size_t)(bh * TPB + tt) * 4 + 2 * t2) * 64 + lane;
        u32x4* ko = ws + WS_KV + ((size_t)(bh * TPB + tt) * 8 + 2 * t2) * 64 + lane;
        f32x16 a;
        GEMM16(a, 64, NFA, fx, false);
        const float qs = 0.125f * 1.4426950408889634f;
#pragma unroll
        for (int r = 0; r < 16; ++r) a[r] *= qs;
        qo[0] = pack8(a, 0); qo[64] = pack8(a, 1);
        GEMM16(a, 80, NFA, fx, false);
        ko[0] = pack8(a, 0); ko[64] = pack8(a, 1);
        GEMM16(a, 96, NFA, fx, true);
        STAMP(DBGA, 6);
        ko[4 * 64] = pack8(a, 0); ko[5 * 64] = pack8(a, 1);
    }
    STAMP(DBGA, 7); STAMPR(DBGA, 15);
}

#define WAIT_BAR(N) asm volatile("s_waitcnt vmcnt(" #N ") lgkmcnt(0)\n\ts_barrier" ::: "memory")

constexpr int AT_SLOT = 1024;
constexpr float AT_BIG = 1.0e30f;
#define ABL_NODMA 0
#define ABL_NOBAR 0
#define ABL_NOLDS 0
#define ABL_NOEXP 0
#define ABL_NOADD 0
#define PIN(x) asm volatile("" : "+v"(x))
DI float vadd(float a, float b) { float r; asm("v_add_f32 %0, %1, %2" : "=v"(r) : "v"(a), "v"(b)); return r; }
#define EX2(x) __builtin_amdgcn_exp2f(x)

__global__ __launch_bounds__(512, 2) void k_attn(const u32x4* __restrict__ ws_in, u32x4* __restrict__ ws) {
    __shared__ u32x4 lds[4 * 2048];
    const int tid = threadIdx.x, lane = tid & 63;
    const int w = __builtin_amdgcn_readfirstlane(tid >> 6);
    STAMP((ws + (10u<<20)), 0);
    const int bh = blockIdx.x & 7, qb = blockIdx.x >> 3, qi = w & 3, g = w >> 2;
    int qt = 4 * qb + qi; const bool qvalid = qt < TPB; if (!qvalid) qt = TPB - 1;
    const u32x4* kv = ws_in + WS_KV + (size_t)bh * TPB * 8 * 64;
    const unsigned lds0 = (unsigned)(uintptr_t)lds;
    const u32x4* src = kv + (w >> 1) * 512 + (w & 1) * 128 + lane;
    const u32x4* src31 = kv + 124 * 512 + (w & 1) * 128 + lane;
    const unsigned dstk = lds0 + (w >> 1) * 4096 + (w & 1) * 2048;
    const u32x4* srcw = kv + qi * 512 + lane;
    const u32x4* srcw31 = kv + 124 * 512 + lane;
    const unsigned dstw = lds0 + qi * 4096;
#define DMA2(sp, dst) do { glds16((sp), (unsigned)__builtin_amdgcn_readfirstlane(dst)); glds16((sp) + 64, (unsigned)__builtin_amdgcn_readfirstlane((dst) + 1024)); } while (0)
#define DMA_K(step) do { const int st_ = (step); const u32x4* s_ = st_ < 31 ? src + (size_t)st_ * 2048 : src31; DMA2(s_, dstk + (st_ & 3) * 32768); } while (0)
#define DMA_V(step) do { const int st_ = (step); const u32x4* s_ = st_ < 31 ? src + (size_t)st_ * 2048 : src31; DMA2(s_ + 256, dstk + (st_ & 3) * 32768 + 16384); } while (0)
    DMA_K(0); DMA_V(0); DMA_K(1);
    u32x4 q[4];
    {
        const u32x4* qp = ws_in + WS_Q + (size_t)(bh * TPB + qt) * 4 * 64 + lane;
#pragma unroll
        for (int f = 0; f < 4; ++f) q[f] = qp[f * 64];
    }
    DMA_K(2); DMA_V(1);
    const char* lbase = (const char*)lds + (2 * g) * 4096 + lane * 16;
#define KF(off, i) (*(const u32x4*)(lbase + (off) + (i) * 1024))
#define VF(off, i) (*(const u32x4*)(lbase + (off) + 16384 + (i) * 1024))
    f32x16 o0 = (f32x16){}, o1 = (f32x16){};
    float negm;
    f32x4 lsum = (f32x4){0.f, 0.f, 0.f, 0.f};
    u32x4 onesA;
    { const unsigned o_ = ((lane & 15) == ((lane >> 4) & 1)) ? 0x3f803f80u : 0u; onesA = (u32x4){o_, o_, o_, o_}; }
    f32x16 cm, sA, sB;
    u32x4 kf[4], vf[4], pA0, pA1, pB0, pB1;
    WAIT_BAR(4);
    {
#pragma unroll
        for (int i = 0; i < 4; ++i) kf[i] = KF(0, i);
        const f32x16 z = (f32x16){};
        sA = MFMA(kf[0], q[0], z); sA = MFMA(kf[1], q[1], sA); sA = MFMA(kf[2], q[2], sA); sA = MFMA(kf[3], q[3], sA);
#pragma unroll
        for (int i = 0; i < 4; ++i) kf[i] = KF(4096, i);
        float mx = sA[0];
#pragma unroll
        for (int r = 1; r < 16; ++r) mx = fmaxf(mx, sA[r]);
        mx = fmaxf(mx, xor32(mx));
        negm = -mx;
#pragma unroll
        for (int r = 0; r < 16; ++r) { sA[r] += negm; cm[r] = negm; sB[r] = 0.f; }
        pB0 = (u32x4){0u, 0u, 0u, 0u}; pB1 = pB0;
    }
#define FIN(S, i, PK, e) do { PK[e] = pack2(S[i], S[(i) + 1]); PIN(PK); } while (0)
#define EXP2(S, i) do { S[i] = EX2(S[i]); S[(i) + 1] = EX2(S[(i) + 1]); PIN(S); } while (0)
#define LSUM(PK) do { lsum = __builtin_amdgcn_mfma_f32_16x16x32_bf16(__builtin_bit_cast(bf16x8, onesA), __builtin_bit_cast(bf16x8, PK), lsum, 0, 0, 0); PIN(lsum); } while (0)
#define PERIOD(SC, SN, PP0, PP1, PC0, PC1, koff, voff, D0, D1) do { \
        vf[0] = VF(voff, 0); FIN(SN, 14, PP1, 3); SB(); SN = MFMA(kf[0], q[0], cm); PIN(SN); EXP2(SC, 0); SB(); \
        vf[2] = VF(voff, 2); SB(); SN = MFMA(kf[1], q[1], SN); PIN(SN); EXP2(SC, 2); FIN(SC, 0, PC0, 0); SB(); \
        vf[1] = VF(voff, 1); SB(); SN = MFMA(kf[2], q[2], SN); PIN(SN); EXP2(SC, 4); FIN(SC, 2, PC0, 1); SB(); D0; SB(); \
        vf[3] = VF(voff, 3); SB(); SN = MFMA(kf[3], q[3], SN); PIN(SN); EXP2(SC, 6); FIN(SC, 4, PC0, 2); SB(); \
        kf[0] = KF(koff, 0); SB(); o0 = MFMA(vf[0], PP0, o0); PIN(o0); EXP2(SC, 8); FIN(SC, 6, PC0, 3); SB(); LSUM(PP0); SB(); \
        kf[1] = KF(koff, 1); SB(); o1 = MFMA(vf[2], PP0, o1); PIN(o1); EXP2(SC, 10); FIN(SC, 8, PC1, 0); SB(); \
        kf[2] = KF(koff, 2); SB(); o0 = MFMA(vf[1], PP1, o0); PIN(o0); EXP2(SC, 12); FIN(SC, 10, PC1, 1); SB(); LSUM(PP1); SB(); D1; SB(); \
        kf[3] = KF(koff, 3); SB(); o1 = MFMA(vf[3], PP1, o1); PIN(o1); EXP2(SC, 14); FIN(SC, 12, PC1, 2); SB(); \
    } while (0)
    int tk0 = 0, tk1 = 0, tk2 = 0, acc_c = 0, acc_w = 0;
#define TICK(x) do {} while (0)
    int vprev = 0;
    for (int i = 0; i < 31; ++i) {
        const int scur = (i & 3) * 32768, snext = ((i + 1) & 3) * 32768;
        const int sk = i + 3 < 32 ? i + 3 : 31, sv = i + 2 < 32 ? i + 2 : 31;
        const u32x4* pk_ = sk < 31 ? srcw + (size_t)sk * 2048 : srcw31;
        const u32x4* pv_ = (sv < 31 ? srcw + (size_t)sv * 2048 : srcw31) + 256;
        const unsigned dk_ = dstw + (sk & 3) * 32768, dv_ = dstw + (sv & 3) * 32768 + 16384;
#define G2(p, d) do { if (!ABL_NODMA && g == 0) { glds16((p), (unsigned)__builtin_amdgcn_readfirstlane(d)); glds16((p) + 64, (unsigned)__builtin_amdgcn_readfirstlane((d) + 1024)); } } while (0)
        SB();
        PERIOD(sA, sB, pB0, pB1, pA0, pA1, snext, vprev, G2(pk_, dk_), G2(pk_ + 128, dk_ + 2048));
        PERIOD(sB, sA, pA0, pA1, pB0, pB1, snext + 4096, scur, G2(pv_, dv_), G2(pv_ + 128, dv_ + 2048));
        vprev = scur + 4096;
        TICK(tk1); if (!ABL_NOBAR) { if (g) WAIT_BAR(0); else WAIT_BAR(8); } TICK(tk2); acc_c += tk1 - tk0; acc_w += tk2 - tk1; tk0 = tk2;
    }
    {
        if (g == 1) {
#pragma unroll
            for (int r = 0; r < 16; ++r) sA[r] = -INFINITY;
        }
        PERIOD(sA, sB, pB0, pB1, pA0, pA1, 0, vprev, (void)0, (void)0);
#pragma unroll
        for (int r = 0; r < 16; ++r) sB[r] = -INFINITY;
        PERIOD(sB, sA, pA0, pA1, pB0, pB1, 0, 3 * 32768, (void)0, (void)0);
    }
    float l;
    { const float la = __shfl(lsum[0], lane & 15), lb = __shfl(lsum[1], lane & 15); l = 0.5f * ((lane & 16) ? lb : la); }
    WAIT_BAR(0);
#undef DMA_K
#undef DMA_V
#undef DMA2
#undef PERIOD
#undef FIN
#undef EXP2
#undef LSUM
#undef G2
#undef KF
#undef VF
    {
        bool bad = !(l < AT_BIG);
#pragma unroll
        for (int r = 0; r < 16; ++r) bad = bad || !(fabsf(o0[r]) < INFINITY) || !(fabsf(o1[r]) < INFINITY);
        if (__builtin_expect(__any(bad), 0)) {
            o0 = (f32x16){}; o1 = (f32x16){}; l = 0.f; float mrun = -INFINITY;
            for (int kt = 0; kt < TPB; ++kt) {
                if (((kt >> 1) & 1) != g) continue;
                const u32x4* kg = kv + (size_t)kt * 512 + lane;
                f32x16 sc = (f32x16){};
#pragma unroll
                for (int f = 0; f < 4; ++f) sc = MFMA(kg[f * 64], q[f], sc);
                float mx = sc[0];
#pragma unroll
                for (int r = 1; r < 16; ++r) mx = fmaxf(mx, sc[r]);
                mx = fmaxf(mx, xor32(mx));
                const float mn = fmaxf(mrun, mx), f = EX2(mrun - mn);
                mrun = mn; l *= f;
#pragma unroll
                for (int r = 0; r < 16; ++r) { o0[r] *= f; o1[r] *= f; sc[r] = EX2(sc[r] - mn); l += sc[r]; }
                const u32x4 p0 = pack8(sc, 0), p1 = pack8(sc, 1);
                o0 = MFMA(kg[256], p0, o0); o1 = MFMA(kg[256 + 128], p0, o1);
                o0 = MFMA(kg[256 + 64], p1, o0); o1 = MFMA(kg[256 + 192], p1, o1);
            }
            negm = -mrun;
        }
    }
    l += xor32(l);
    float* cf = (float*)lds;
    if (g == 1) {
        float* dst = cf + qi * 34 * 64 + lane;
#pragma unroll
        for (int r = 0; r < 16; ++r) { dst[r * 64] = o0[r]; dst[(16 + r) * 64] = o1[r]; }
        dst[32 * 64] = negm; dst[33 * 64] = l;
    }
    __syncthreads();
    if (g == 0) {
        const float* s = cf + qi * 34 * 64 + lane;
        const float negm1 = s[32 * 64], l1 = s[33 * 64];
        const float nm = fminf(negm, negm1);
        const float f0 = __builtin_amdgcn_exp2f(nm - negm), f1 = __builtin_amdgcn_exp2f(nm - negm1);
        const float inv = __builtin_amdgcn_rcpf(l * f0 + l1 * f1);
        const float c0 = f0 * inv, c1 = f1 * inv;
#pragma unroll
        for (int r = 0; r < 16; ++r) { o0[r] = o0[r] * c0 + s[r * 64] * c1; o1[r] = o1[r] * c0 + s[(16 + r) * 64] * c1; }
        if (qvalid) {
            const int b = bh >> 2, hd = bh & 3;
            u32x4* ao = ws + WS_AT + ((size_t)(b * TPB + qt) * 16 + 4 * hd) * 64 + lane;
            ao[0] = pack8(o0, 0); ao[64] = pack8(o0, 1); ao[128] = pack8(o1, 0); ao[192] = pack8(o1, 1);
        }
    }
    STAMPR((ws + (10u<<20)), 15);
}

#define DBGC ((u32x4*)ws + (9u<<20))
__global__ __launch_bounds__(512, 2) void k_stageC(const u32x4* __restrict__ ws, const uint16_t* __restrict__ dw, uint16_t* __restrict__ out) {
    __shared__ u32x4 lds[32 * 64 + 64 * 64 + 384 + 160 + 512];
    u32x4* fx = lds;
    u32x4* fy = lds + 16 * 64;
    u32x4* fh = lds + 32 * 64;
    float* st0 = (float*)(lds + 32 * 64 + 64 * 64);
    float* st1 = st0 + 512;
    float* st2 = st0 + 1024;
    u32x4* dwl = lds + 32 * 64 + 64 * 64 + 384;
    const int tile = blockIdx.x, tid = threadIdx.x, lane = tid & 63, h = lane >> 5, m = lane & 31;
    const int w = __builtin_amdgcn_readfirstlane(tid >> 6);
    STAMPR(DBGC, 14); STAMP(DBGC, 0);

    const int tt = tile % TPB;
    const int hm = m & 3;
    int toff = hm < 2 ? hm - 2 : 30 + hm;
    const int tl = tt * 32 + toff;
    const bool hvalid = (tl >= 0) && (tl < T_SEQ);
    if (!hvalid) toff = 0;
    const int gtok = tile * 32 + toff;
    const size_t hbase = (size_t)(gtok >> 5) * 16 * 64 + h * 32 + (gtok & 31);

    u32x4 in[8];
    {
        const u32x4* at = ws + WS_AT;
        const u32x4* x1 = ws + WS_X1;
        const size_t mb = (size_t)tile * 16 * 64 + lane;
        in[0] = at[mb + (2 * w) * 64]; in[1] = at[mb + (2 * w + 1) * 64];
        in[2] = at[hbase + (2 * w) * 64]; in[3] = at[hbase + (2 * w + 1) * 64];
        in[4] = x1[mb + (2 * w) * 64]; in[5] = x1[mb + (2 * w + 1) * 64];
        in[6] = x1[hbase + (2 * w) * 64]; in[7] = x1[hbase + (2 * w + 1) * 64];
    }
    u32x4 dwr;
    if (tid < 160) dwr = ((const u32x4*)dw)[tid];
    RAWBAR();
    constexpr int NFX = NFC;
    const u32x4* wp = ws + WS_WC + (size_t)w * NFC * 64 + lane;
    u32x4 ring[32];
    LD8(0); LD8(8);
    f32x16 xv, yv;
    fx[(2 * w) * 64 + lane] = in[0]; fx[(2 * w + 1) * 64 + lane] = in[1];
    fy[(2 * w) * 64 + lane] = in[2]; fy[(2 * w + 1) * 64 + lane] = in[3];
    if (tid < 160) dwl[tid] = dwr;
    unpack16(xv, in[4], in[5]);
    unpack16(yv, in[6], in[7]);
    __syncthreads();
    STAMP(DBGC, 1);
    {
        f32x16 a = (f32x16){}, b = (f32x16){};
        u32x4 bx[2], by[2];
        bx[0] = fx[lane]; by[0] = fy[lane]; bx[1] = fx[64 + lane]; by[1] = fy[64 + lane];
#pragma unroll
        for (int ks = 0; ks < 16; ++ks) {
            a = MFMA(ring[ks & 31], bx[ks & 1], a);
            b = MFMA(ring[ks & 31], by[ks & 1], b);
            if (ks + 2 < 16) { bx[ks & 1] = fx[(ks + 2) * 64 + lane]; by[ks & 1] = fy[(ks + 2) * 64 + lane]; }
            REFILL(ks, 0);
            SB();
        }
        STAMP(DBGC, 2);
#pragma unroll
        for (int r = 0; r < 16; ++r) { xv[r] = rbf(xv[r] + rbf(a[r])); yv[r] = rbf(yv[r] + rbf(b[r])); }
    }
    float sc, sh, sc2, sh2;
    ln_partial(xv, st0, w, lane);
    ln_partial(yv, st1, w, lane);
    __syncthreads();
    ln_finish(st0, lane, sc, sh);
    ln_finish(st1, lane, sc2, sh2);
    norm_store(xv, sc, sh, fx, w, lane);
    norm_store(yv, sc2, sh2, fy, w, lane);
    __syncthreads();
    STAMP(DBGC, 3);
    {
        f32x16 a = (f32x16){}, b = (f32x16){}, c = (f32x16){}, d = (f32x16){};
        u32x4 bxn = fx[lane], byn = fy[lane];
#pragma unroll
        for (int ks = 0; ks < 16; ++ks) {
            const int F = 16 + 2 * ks;
            const u32x4 bx = bxn, by = byn;
            if (ks + 1 < 16) { bxn = fx[(ks + 1) * 64 + lane]; byn = fy[(ks + 1) * 64 + lane]; }
            a = MFMA(ring[F & 31], bx, a); c = MFMA(ring[F & 31], by, c); REFILL(F, 0);
            b = MFMA(ring[(F + 1) & 31], bx, b); d = MFMA(ring[(F + 1) & 31], by, d); REFILL(F + 1, 0);
            SB();
        }
        STAMP(DBGC, 4);
#pragma unroll
        for (int r = 0; r < 16; ++r) {
            a[r] = a[r] * sigmoidf_(b[r]);
            c[r] = hvalid ? c[r] * sigmoidf_(d[r]) : 0.f;
        }
        f32x4* cs = (f32x4*)fh + w * (4 * 2 * 36);
#pragma unroll
        for (int g = 0; g < 4; ++g) {
            cs[(g * 2 + h) * 36 + m + 2] = (f32x4){a[4 * g], a[4 * g + 1], a[4 * g + 2], a[4 * g + 3]};
            if (m < 4) cs[(g * 2 + h) * 36 + (m < 2 ? m : 32 + m)] = (f32x4){c[4 * g], c[4 * g + 1], c[4 * g + 2], c[4 * g + 3]};
        }
        __syncthreads();
        const uint16_t* dp = (const uint16_t*)dwl + 32 * w + 4 * h;
#pragma unroll
        for (int g = 0; g < 4; ++g) {
            f32x4 acc = (f32x4){0.f, 0.f, 0.f, 0.f};
#pragma unroll
            for (int i = 0; i < 5; ++i) {
                const f32x4 hv = cs[(g * 2 + h) * 36 + m + i];
                const u32x2 t = *(const u32x2*)(dp + i * DM + 8 * g);
                acc[0] += hv[0] * bflo(t[0]); acc[1] += hv[1] * bfhi(t[0]); acc[2] += hv[2] * bflo(t[1]); acc[3] += hv[3] * bfhi(t[1]);
            }
#pragma unroll
            for (int i = 0; i < 4; ++i) a[4 * g + i] = siluf_(acc[i]);
        }
        fx[(2 * w) * 64 + lane] = pack8(a, 0);
        fx[(2 * w + 1) * 64 + lane] = pack8(a, 1);
    }
    __syncthreads();
    STAMP(DBGC, 5);
    {
        f32x16 a;
        GEMM16(a, 48, 64, fx, false);
        STAMP(DBGC, 6);
#pragma unroll
        for (int r = 0; r < 16; ++r) xv[r] = rbf(xv[r] + rbf(a[r]));
    }
    ln_partial(xv, st2, w, lane);
    __syncthreads();
    ln_finish(st2, lane, sc, sh);
    norm_store(xv, sc, sh, fx, w, lane);
    __syncthreads();
    STAMP(DBGC, 7);
    {
        f32x16 acc;
        FFN(acc, 64, DBGC, 8);
#pragma unroll
        for (int r = 0; r < 16; ++r) xv[r] = rbf(__builtin_fmaf(0.5f, rbf(acc[r]), xv[r]));
    }
    ln_partial(xv, st0, w, lane);
    __syncthreads();
    ln_finish(st0, lane, sc, sh);
    {
        uint16_t* op = out + (size_t)(tile * 32 + m) * DM + 32 * w + 4 * h;
#pragma unroll
        for (int g = 0; g < 4; ++g) {
            u32x2 t;
            t[0] = pack2(__builtin_fmaf(xv[4 * g], sc, sh), __builtin_fmaf(xv[4 * g + 1], sc, sh));
            t[1] = pack2(__builtin_fmaf(xv[4 * g + 2], sc, sh), __builtin_fmaf(xv[4 * g + 3], sc, sh));
            *(u32x2*)(op + 8 * g) = t;
        }
    }
    STAMP(DBGC, 11); STAMPR(DBGC, 15);
}

extern "C" void kernel_launch(void* const* d_in, const int* in_sizes, int n_in,
                              void* d_out, int out_size, void* d_ws, size_t ws_size,
                              hipStream_t stream) {
    PackArgs p;
    p.x = (const uint16_t*)d_in[0]; p.w1a = (const uint16_t*)d_in[1]; p.w1b = (const uint16_t*)d_in[2]; p.wqkv = (const uint16_t*)d_in[3]; p.wo = (const uint16_t*)d_in[4];
    p.wp1 = (const uint16_t*)d_in[5]; p.wp2 = (const uint16_t*)d_in[7]; p.w2a = (const uint16_t*)d_in[8]; p.w2b = (const uint16_t*)d_in[9];
    p.ws = (u32x4*)d_ws;
    u32x4* ws = (u32x4*)d_ws;
    k_ff1a<<<K0_BLOCKS, 512, 0, stream>>>(p);
    k_stageA<<<NTILE, 512, 0, stream>>>((const uint16_t*)d_in[0], ws);
    k_attn<<<256, 512, 0, stream>>>(ws, ws);
    k_stageC<<<NTILE, 512, 0, stream>>>(ws, (const uint16_t*)d_in[6], (uint16_t*)d_out);
}
```

```cpp
#include <hip/hip_runtime.h>
#include <hip/hip_bf16.h>
#include <stdint.h>

typedef short bf16x8 __attribute__((ext_vector_type(8)));
typedef float f32x16 __attribute__((ext_vector_type(16)));
typedef float f32x2 __attribute__((ext_vector_type(2)));
typedef float f32x4 __attribute__((ext_vector_type(4)));
typedef unsigned u32x4 __attribute__((ext_vector_type(4)));
typedef unsigned u32x2 __attribute__((ext_vector_type(2)));
typedef __bf16 bf16v2 __attribute__((ext_vector_type(2)));

#define DI __device__ __forceinline__
#define SB() __builtin_amdgcn_sched_barrier(0)
#define STAMPR(base, i) do {} while (0)
#define STAMP(base, i) do {} while (0)
#define MFMA(a, b, c) __builtin_amdgcn_mfma_f32_32x32x16_bf16(__builtin_bit_cast(bf16x8, (a)), __builtin_bit_cast(bf16x8, (b)), (c), 0, 0, 0)

constexpr int T_SEQ = 4000, DM = 256, NTOK = 8000, NTILE = 250, TPB = 125;
constexpr int NFA = 176;
constexpr int NFC = 192;

constexpr size_t WS_WA = 0;
constexpr size_t WS_WC = WS_WA + (size_t)8 * NFA * 64;
constexpr size_t WS_X1 = WS_WC + (size_t)8 * NFC * 64;
constexpr size_t WS_Q  = WS_X1 + (size_t)NTILE * 16 * 64;
constexpr size_t WS_KV = WS_Q + (size_t)8 * TPB * 4 * 64;
constexpr size_t WS_AT = WS_KV + (size_t)8 * TPB * 8 * 64 + 4096;
constexpr size_t WS_FL = WS_AT + (size_t)NTILE * 16 * 64;

DI unsigned pack2(float lo, float hi) {
    f32x2 v = {lo, hi};
    bf16v2 r = __builtin_convertvector(v, bf16v2);
    return __builtin_bit_cast(unsigned, r);
}
DI float bflo(unsigned u) { return __uint_as_float(u << 16); }
DI float bfhi(unsigned u) { return __uint_as_float(u & 0xffff0000u); }
DI u32x4 pack8(const f32x16& a, int s) {
    u32x4 r;
    r[0] = pack2(a[8 * s + 0], a[8 * s + 1]);
    r[1] = pack2(a[8 * s + 2], a[8 * s + 3]);
    r[2] = pack2(a[8 * s + 4], a[8 * s + 5]);
    r[3] = pack2(a[8 * s + 6], a[8 * s + 7]);
    return r;
}
DI void unpack16(f32x16& a, const u32x4& f0, const u32x4& f1) {
#pragma unroll
    for (int i = 0; i < 4; ++i) {
        a[2 * i] = bflo(f0[i]); a[2 * i + 1] = bfhi(f0[i]);
        a[8 + 2 * i] = bflo(f1[i]); a[8 + 2 * i + 1] = bfhi(f1[i]);
    }
}
DI float xor32(float v) { return __shfl_xor(v, 32); }
DI float sigmoidf_(float x) { return __builtin_amdgcn_rcpf(1.0f + __builtin_amdgcn_exp2f(-1.4426950408889634f * x)); }
DI float siluf_(float x) { return x * sigmoidf_(x); }

struct PackArgs {
    const uint16_t* w1a; const uint16_t* w1b; const uint16_t* wqkv; const uint16_t* wo;
    const uint16_t* wp1; const uint16_t* wp2; const uint16_t* w2a; const uint16_t* w2b;
    u32x4* ws;
};

__global__ __launch_bounds__(256) void k_pack(PackArgs p) {
    const int gid = blockIdx.x * 256 + threadIdx.x;
    const int lane = gid & 63, fg = gid >> 6;
    if (gid < NTILE * 4) p.ws[WS_FL + gid] = (u32x4){0u, 0u, 0u, 0u};
    if (fg >= 8 * (NFA + NFC)) return;
    const uint16_t* W; int N, nt, ks;
    if (fg < 8 * NFA) {
        const int w = fg / NFA, f = fg % NFA;
        if (f < 64)       { W = p.w1a; N = 1024; ks = f & 15; nt = 4 * w + (f >> 4); }
        else if (f < 128) { W = p.w1b; N = 256; ks = f - 64; nt = w; }
        else              { const int g = f - 128; W = p.wqkv; N = 768; ks = g & 15; nt = w + 8 * (g >> 4); }
    } else {
        const int fg2 = fg - 8 * NFA, w = fg2 / NFC, f = fg2 % NFC;
        if (f < 16)       { W = p.wo; N = 256; ks = f; nt = w; }
        else if (f < 48)  { const int g = f - 16; W = p.wp1; N = 512; ks = g >> 1; nt = w + 8 * (g & 1); }
        else if (f < 64)  { W = p.wp2; N = 256; ks = f - 48; nt = w; }
        else if (f < 128) { const int g = f - 64; W = p.w2a; N = 1024; ks = g & 15; nt = 4 * w + (g >> 4); }
        else              { W = p.w2b; N = 256; ks = f - 128; nt = w; }
    }
    const int r = lane & 31, h = lane >> 5;
    const uint16_t* src = W + (size_t)(16 * ks + 4 * h) * N + 32 * nt + r;
    unsigned e[8];
#pragma unroll
    for (int j = 0; j < 8; ++j) e[j] = src[(size_t)(8 * (j >> 2) + (j & 3)) * N];
    u32x4 o;
#pragma unroll
    for (int i = 0; i < 4; ++i) o[i] = e[2 * i] | (e[2 * i + 1] << 16);
    p.ws[gid] = o;
}

DI void glds16(const void* g, unsigned lds_base) {
    unsigned sv;
    asm volatile("s_mov_b32 %0, m0\n\ts_mov_b32 m0, %2\n\ts_nop 0\n\tglobal_load_lds_dwordx4 %1, off\n\ts_mov_b32 m0, %0"
                 : "=&s"(sv) : "v"(g), "s"(lds_base) : "memory");
}
template <int NI> DI void l2_warm(const u32x4* base, int total_chunks, int rank, int w, int lane, unsigned lds_scratch) {
#pragma unroll
    for (int i = 0; i < NI; ++i) {
        int c = ((rank * NI + i) * 8 + w) * 64;
        c = c + 64 <= total_chunks ? c : total_chunks - 64;
        glds16(base + c + lane, lds_scratch);
    }
}
#define WLOAD(F) ring[(F) & 31] = wp[(F) * 64]
#define REFILL(F, PE) do { if ((F) < 16) WLOAD((F) + 16); if ((F) + 32 < (NFX)) WLOAD((F) + 32); } while (0)
#define LD8(F) do { SB(); _Pragma("unroll") for (int i_ = 0; i_ < 8; ++i_) WLOAD((F) + i_); SB(); } while (0)
#define RAWBAR() do { SB(); __builtin_amdgcn_s_barrier(); SB(); } while (0)

DI void ln_partial(const f32x16& v, float* st, int w, int lane) {
    float s = 0.f, q = 0.f;
#pragma unroll
    for (int i = 0; i < 16; ++i) { s += v[i]; q = __builtin_fmaf(v[i], v[i], q); }
    auto r = __builtin_amdgcn_permlane32_swap(__float_as_uint(s), __float_as_uint(q), false, false);
    st[w * 64 + lane] = __uint_as_float(r[0]) + __uint_as_float(r[1]);
}
DI void ln_finish(const float* st, int lane, float& scale, float& shift) {
    const float* p = st + (lane & 31);
    float s = 0.f, q = 0.f;
#pragma unroll
    for (int i = 0; i < 8; ++i) { s += p[i * 64]; q += p[i * 64 + 32]; }
    const float m = s * (1.0f / 256.0f);
    const float var = fmaxf(q * (1.0f / 256.0f) - m * m, 0.f);
    scale = __builtin_amdgcn_rsqf(var + 1e-5f);
    shift = -m * scale;
}
DI void norm_store(const f32x16& v, float scale, float shift, u32x4* dst  , int w, int lane) {
    f32x16 n;
#pragma unroll
    for (int i = 0; i < 16; ++i) n[i] = __builtin_fmaf(v[i], scale, shift);
    dst[(2 * w) * 64 + lane] = pack8(n, 0);
    dst[(2 * w + 1) * 64 + lane] = pack8(n, 1);
}
DI float rbf(float x) { return bflo(pack2(x, 0.f) & 0xffffu); }

#define GEMM16(acc, F0, PE, fxp, SWAP) do { \
        acc = (f32x16){}; u32x4 bq_[4]; \
        _Pragma("unroll") for (int i_ = 0; i_ < 4; ++i_) bq_[i_] = (fxp)[i_ * 64 + lane]; \
        _Pragma("unroll") for (int ks_ = 0; ks_ < 16; ++ks_) { \
            if (SWAP) acc = MFMA(bq_[ks_ & 3], ring[((F0) + ks_) & 31], acc); else acc = MFMA(ring[((F0) + ks_) & 31], bq_[ks_ & 3], acc); \
            if (ks_ + 4 < 16) bq_[ks_ & 3] = (fxp)[(ks_ + 4) * 64 + lane]; \
            REFILL((F0) + ks_, PE); SB(); } } while (0)

#define FFN(acc, F0, DBG, ST) do { \
        _Pragma("unroll") for (int t_ = 0; t_ < 4; ++t_) { \
            f32x16 a_; GEMM16(a_, (F0) + 16 * t_, (F0) + 64, fx, false); \
            if (t_ == 3) { STAMP(DBG, ST); } \
            _Pragma("unroll") for (int r_ = 0; r_ < 8; ++r_) a_[r_] = siluf_(a_[r_]); \
            fh[((4 * w + t_) * 2 + 0) * 64 + lane] = pack8(a_, 0); \
            _Pragma("unroll") for (int r_ = 8; r_ < 16; ++r_) a_[r_] = siluf_(a_[r_]); \
            fh[((4 * w + t_) * 2 + 1) * 64 + lane] = pack8(a_, 1); } \
        __syncthreads(); \
        STAMP(DBG, ST + 1); \
        acc = (f32x16){}; u32x4 bq_[4]; \
        _Pragma("unroll") for (int i_ = 0; i_ < 4; ++i_) bq_[i_] = fh[i_ * 64 + lane]; \
        _Pragma("unroll") for (int ks_ = 0; ks_ < 64; ++ks_) { \
            acc = MFMA(ring[((F0) + 64 + ks_) & 31], bq_[ks_ & 3], acc); \
            if (ks_ + 4 < 64) bq_[ks_ & 3] = fh[(ks_ + 4) * 64 + lane]; \
            REFILL((F0) + 64 + ks_, (F0) + 128); SB(); } \
        STAMP(DBG, ST + 2); } while (0)

#define DBGA (ws + (8u<<20))
__global__ __launch_bounds__(512, 2) void k_stageA(const uint16_t* __restrict__ x, u32x4* __restrict__ ws) {
    __shared__ u32x4 lds[16 * 64 + 64 * 64 + 256 + 512];
    u32x4* fx = lds;
    u32x4* fh = lds + 16 * 64;
    float* st0 = (float*)(lds + 16 * 64 + 64 * 64);
    float* st1 = st0 + 512;
    const int tile = blockIdx.x, tid = threadIdx.x, lane = tid & 63, h = lane >> 5, m = lane & 31;
    const int w = __builtin_amdgcn_readfirstlane(tid >> 6);
    STAMPR(DBGA, 14); STAMP(DBGA, 0);
    u32x2 xr[4];
    {
        const uint16_t* xp = x + (size_t)(tile * 32 + m) * DM + 32 * w + 4 * h;
#pragma unroll
        for (int g = 0; g < 4; ++g) xr[g] = *(const u32x2*)(xp + 8 * g);
    }
    RAWBAR();
    constexpr int NFX = NFA;
    const u32x4* wp = ws + WS_WA + (size_t)w * NFA * 64 + lane;
    u32x4 ring[32];
    LD8(0); LD8(8);
    f32x16 xv;
#pragma unroll
    for (int g = 0; g < 4; ++g) { xv[4 * g + 0] = bflo(xr[g][0]); xv[4 * g + 1] = bfhi(xr[g][0]); xv[4 * g + 2] = bflo(xr[g][1]); xv[4 * g + 3] = bfhi(xr[g][1]); }
    float sc, sh;
    ln_partial(xv, st0, w, lane);
    __syncthreads();
    ln_finish(st0, lane, sc, sh);
    norm_store(xv, sc, sh, fx, w, lane);
    __syncthreads();
    STAMP(DBGA, 1);
    {
        f32x16 acc;
        FFN(acc, 0, DBGA, 2);
#pragma unroll
        for (int r = 0; r < 16; ++r) xv[r] = rbf(__builtin_fmaf(0.5f, rbf(acc[r]), xv[r]));
    }
    ln_partial(xv, st1, w, lane);
    {
        u32x4* xo = ws + WS_X1 + (size_t)tile * 16 * 64;
        xo[(2 * w) * 64 + lane] = pack8(xv, 0);
        xo[(2 * w + 1) * 64 + lane] = pack8(xv, 1);
    }
    __syncthreads();
    ln_finish(st1, lane, sc, sh);
    norm_store(xv, sc, sh, fx, w, lane);
    __syncthreads();
    STAMP(DBGA, 5);
    {
        const int b = tile / TPB, tt = tile % TPB, bh = b * 4 + (w >> 1), t2 = w & 1;
        u32x4* qo = ws + WS_Q + ((size_t)(bh * TPB + tt) * 4 + 2 * t2) * 64 + lane;
        u32x4* ko = ws + WS_KV + ((size_t)(bh * TPB + tt) * 8 + 2 * t2) * 64 + lane;
        f32x16 a;
        GEMM16(a, 128, NFA, fx, false);
        const float qs = 0.125f * 1.4426950408889634f;
#pragma unroll
        for (int r = 0; r < 16; ++r) a[r] *= qs;
        qo[0] = pack8(a, 0); qo[64] = pack8(a, 1);
        GEMM16(a, 144, NFA, fx, false);
        ko[0] = pack8(a, 0); ko[64] = pack8(a, 1);
        GEMM16(a, 160, NFA, fx, true);
        STAMP(DBGA, 6);
        ko[4 * 64] = pack8(a, 0); ko[5 * 64] = pack8(a, 1);
    }
    STAMP(DBGA, 7); STAMPR(DBGA, 15);
}

#define WAIT_BAR(N) asm volatile("s_waitcnt vmcnt(" #N ") lgkmcnt(0)\n\ts_barrier" ::: "memory")

constexpr int AT_SLOT = 1024;
constexpr float AT_BIG = 1.0e30f;
#define ABL_NODMA 0
#define ABL_NOBAR 0
#define ABL_NOLDS 0
#define ABL_NOEXP 0
#define ABL_NOADD 0
#define PIN(x) asm volatile("" : "+v"(x))
DI float vadd(float a, float b) { float r; asm("v_add_f32 %0, %1, %2" : "=v"(r) : "v"(a), "v"(b)); return r; }
#define EX2(x) __builtin_amdgcn_exp2f(x)

DI void attn_body(const u32x4* ws_in, u32x4* ws, u32x4* lds  ) {
    const int tid = threadIdx.x, lane = tid & 63;
    const int w = __builtin_amdgcn_readfirstlane(tid >> 6);
    STAMP((ws + (10u<<20)), 0);
    const int bh = blockIdx.x & 7, qb = blockIdx.x >> 3, qi = w & 3, g = w >> 2;
    int qt = 4 * qb + qi; const bool qvalid = qt < TPB; if (!qvalid) qt = TPB - 1;
    const u32x4* kv = ws_in + WS_KV + (size_t)bh * TPB * 8 * 64;
    const unsigned lds0 = (unsigned)(uintptr_t)lds;
    const u32x4* src = kv + (w >> 1) * 512 + (w & 1) * 128 + lane;
    const u32x4* src31 = kv + 124 * 512 + (w & 1) * 128 + lane;
    const unsigned dstk = lds0 + (w >> 1) * 4096 + (w & 1) * 2048;
    const u32x4* srcw = kv + qi * 512 + lane;
    const u32x4* srcw31 = kv + 124 * 512 + lane;
    const unsigned dstw = lds0 + qi * 4096;
#define DMA2(sp, dst) do { glds16((sp), (unsigned)__builtin_amdgcn_readfirstlane(dst)); glds16((sp) + 64, (unsigned)__builtin_amdgcn_readfirstlane((dst) + 1024)); } while (0)
#define DMA_K(step) do { const int st_ = (step); const u32x4* s_ = st_ < 31 ? src + (size_t)st_ * 2048 : src31; DMA2(s_, dstk + (st_ & 3) * 32768); } while (0)
#define DMA_V(step) do { const int st_ = (step); const u32x4* s_ = st_ < 31 ? src + (size_t)st_ * 2048 : src31; DMA2(s_ + 256, dstk + (st_ & 3) * 32768 + 16384); } while (0)
    DMA_K(0); DMA_V(0); DMA_K(1);
    u32x4 q[4];
    {
        const u32x4* qp = ws_in + WS_Q + (size_t)(bh * TPB + qt) * 4 * 64 + lane;
#pragma unroll
        for (int f = 0; f < 4; ++f) q[f] = qp[f * 64];
    }
    DMA_K(2); DMA_V(1);
    const char* lbase = (const char*)lds + (2 * g) * 4096 + lane * 16;
#define KF(off, i) (*(const u32x4*)(lbase + (off) + (i) * 1024))
#define VF(off, i) (*(const u32x4*)(lbase + (off) + 16384 + (i) * 1024))
    f32x16 o0 = (f32x16){}, o1 = (f32x16){};
    float negm;
    f32x4 lsum = (f32x4){0.f, 0.f, 0.f, 0.f};
    u32x4 onesA;
    { const unsigned o_ = ((lane & 15) == ((lane >> 4) & 1)) ? 0x3f803f80u : 0u; onesA = (u32x4){o_, o_, o_, o_}; }
    f32x16 cm, sA, sB;
    u32x4 kf[4], vf[4], pA0, pA1, pB0, pB1;
    WAIT_BAR(4);
    {
#pragma unroll
        for (int i = 0; i < 4; ++i) kf[i] = KF(0, i);
        const f32x16 z = (f32x16){};
        sA = MFMA(kf[0], q[0], z); sA = MFMA(kf[1], q[1], sA); sA = MFMA(kf[2], q[2], sA); sA = MFMA(kf[3], q[3], sA);
#pragma unroll
        for (int i = 0; i < 4; ++i) kf[i] = KF(4096, i);
        float mx = sA[0];
#pragma unroll
        for (int r = 1; r < 16; ++r) mx = fmaxf(mx, sA[r]);
        mx = fmaxf(mx, xor32(mx));
        negm = -mx;
#pragma unroll
        for (int r = 0; r < 16; ++r) { sA[r] += negm; cm[r] = negm; sB[r] = 0.f; }
        pB0 = (u32x4){0u, 0u, 0u, 0u}; pB1 = pB0;
    }
#define FIN(S, i, PK, e) do { PK[e] = pack2(S[i], S[(i) + 1]); PIN(PK); } while (0)
#define EXP2(S, i) do { S[i] = EX2(S[i]); S[(i) + 1] = EX2(S[(i) + 1]); PIN(S); } while (0)
#define LSUM(PK) do { lsum = __builtin_amdgcn_mfma_f32_16x16x32_bf16(__builtin_bit_cast(bf16x8, onesA), __builtin_bit_cast(bf16x8, PK), lsum, 0, 0, 0); PIN(lsum); } while (0)
#define PERIOD(SC, SN, PP0, PP1, PC0, PC1, koff, voff, D0, D1) do { \
        vf[0] = VF(voff, 0); FIN(SN, 14, PP1, 3); SB(); SN = MFMA(kf[0], q[0], cm); PIN(SN); EXP2(SC, 0); SB(); \
        vf[2] = VF(voff, 2); SB(); SN = MFMA(kf[1], q[1], SN); PIN(SN); EXP2(SC, 2); FIN(SC, 0, PC0, 0); SB(); \
        vf[1] = VF(voff, 1); SB(); SN = MFMA(kf[2], q[2], SN); PIN(SN); EXP2(SC, 4); FIN(SC, 2, PC0, 1); SB(); D0; SB(); \
        vf[3] = VF(voff, 3); SB(); SN = MFMA(kf[3], q[3], SN); PIN(SN); EXP2(SC, 6); FIN(SC, 4, PC0, 2); SB(); \
        kf[0] = KF(koff, 0); SB(); o0 = MFMA(vf[0], PP0, o0); PIN(o0); EXP2(SC, 8); FIN(SC, 6, PC0, 3); SB(); LSUM(PP0); SB(); \
        kf[1] = KF(koff, 1); SB(); o1 = MFMA(vf[2], PP0, o1); PIN(o1); EXP2(SC, 10); FIN(SC, 8, PC1, 0); SB(); \
        kf[2] = KF(koff, 2); SB(); o0 = MFMA(vf[1], PP1, o0); PIN(o0); EXP2(SC, 12); FIN(SC, 10, PC1, 1); SB(); LSUM(PP1); SB(); D1; SB(); \
        kf[3] = KF(koff, 3); SB(); o1 = MFMA(vf[3], PP1, o1); PIN(o1); EXP2(SC, 14); FIN(SC, 12, PC1, 2); SB(); \
    } while (0)
    int tk0 = 0, tk1 = 0, tk2 = 0, acc_c = 0, acc_w = 0;
#define TICK(x) do {} while (0)
    int vprev = 0;
    for (int i = 0; i < 31; ++i) {
        const int scur = (i & 3) * 32768, snext = ((i + 1) & 3) * 32768;
        const int sk = i + 3 < 32 ? i + 3 : 31, sv = i + 2 < 32 ? i + 2 : 31;
        const u32x4* pk_ = sk < 31 ? srcw + (size_t)sk * 2048 : srcw31;
        const u32x4* pv_ = (sv < 31 ? srcw + (size_t)sv * 2048 : srcw31) + 256;
        const unsigned dk_ = dstw + (sk & 3) * 32768, dv_ = dstw + (sv & 3) * 32768 + 16384;
#define G2(p, d) do { if (!ABL_NODMA && g == 0) { glds16((p), (unsigned)__builtin_amdgcn_readfirstlane(d)); glds16((p) + 64, (unsigned)__builtin_amdgcn_readfirstlane((d) + 1024)); } } while (0)
        SB();
        PERIOD(sA, sB, pB0, pB1, pA0, pA1, snext, vprev, G2(pk_, dk_), G2(pk_ + 128, dk_ + 2048));
        PERIOD(sB, sA, pA0, pA1, pB0, pB1, snext + 4096, scur, G2(pv_, dv_), G2(pv_ + 128, dv_ + 2048));
        vprev = scur + 4096;
        TICK(tk1); if (!ABL_NOBAR) { if (g) WAIT_BAR(0); else WAIT_BAR(8); } TICK(tk2); acc_c += tk1 - tk0; acc_w += tk2 - tk1; tk0 = tk2;
    }
    {
        if (g == 1) {
#pragma unroll
            for (int r = 0; r < 16; ++r) sA[r] = -INFINITY;
        }
        PERIOD(sA, sB, pB0, pB1, pA0, pA1, 0, vprev, (void)0, (void)0);
#pragma unroll
        for (int r = 0; r < 16; ++r) sB[r] = -INFINITY;
        PERIOD(sB, sA, pA0, pA1, pB0, pB1, 0, 3 * 32768, (void)0, (void)0);
    }
    float l;
    { const float la = __shfl(lsum[0], lane & 15), lb = __shfl(lsum[1], lane & 15); l = 0.5f * ((lane & 16) ? lb : la); }
    WAIT_BAR(0);
#undef DMA_K
#undef DMA_V
#undef DMA2
#undef PERIOD
#undef FIN
#undef EXP2
#undef LSUM
#undef G2
#undef KF
#undef VF
    {
        bool bad = !(l < AT_BIG);
#pragma unroll
        for (int r = 0; r < 16; ++r) bad = bad || !(fabsf(o0[r]) < INFINITY) || !(fabsf(o1[r]) < INFINITY);
        if (__builtin_expect(__any(bad), 0)) {
            o0 = (f32x16){}; o1 = (f32x16){}; l = 0.f; float mrun = -INFINITY;
            for (int kt = 0; kt < TPB; ++kt) {
                if (((kt >> 1) & 1) != g) continue;
                const u32x4* kg = kv + (size_t)kt * 512 + lane;
                f32x16 sc = (f32x16){};
#pragma unroll
                for (int f = 0; f < 4; ++f) sc = MFMA(kg[f * 64], q[f], sc);
                float mx = sc[0];
#pragma unroll
                for (int r = 1; r < 16; ++r) mx = fmaxf(mx, sc[r]);
                mx = fmaxf(mx, xor32(mx));
                const float mn = fmaxf(mrun, mx), f = EX2(mrun - mn);
                mrun = mn; l *= f;
#pragma unroll
                for (int r = 0; r < 16; ++r) { o0[r] *= f; o1[r] *= f; sc[r] = EX2(sc[r] - mn); l += sc[r]; }
                const u32x4 p0 = pack8(sc, 0), p1 = pack8(sc, 1);
                o0 = MFMA(kg[256], p0, o0); o1 = MFMA(kg[256 + 128], p0, o1);
                o0 = MFMA(kg[256 + 64], p1, o0); o1 = MFMA(kg[256 + 192], p1, o1);
            }
            negm = -mrun;
        }
    }
    l += xor32(l);
    float* cf = (float*)lds;
    if (g == 1) {
        float* dst = cf + qi * 34 * 64 + lane;
#pragma unroll
        for (int r = 0; r < 16; ++r) { dst[r * 64] = o0[r]; dst[(16 + r) * 64] = o1[r]; }
        dst[32 * 64] = negm; dst[33 * 64] = l;
    }
    __syncthreads();
    if (g == 0) {
        const float* s = cf + qi * 34 * 64 + lane;
        const float negm1 = s[32 * 64], l1 = s[33 * 64];
        const float nm = fminf(negm, negm1);
        const float f0 = __builtin_amdgcn_exp2f(nm - negm), f1 = __builtin_amdgcn_exp2f(nm - negm1);
        const float inv = __builtin_amdgcn_rcpf(l * f0 + l1 * f1);
        const float c0 = f0 * inv, c1 = f1 * inv;
#pragma unroll
        for (int r = 0; r < 16; ++r) { o0[r] = o0[r] * c0 + s[r * 64] * c1; o1[r] = o1[r] * c0 + s[(16 + r) * 64] * c1; }
        if (qvalid) {
            const int b = bh >> 2, hd = bh & 3;
            u32x4* ao = ws + WS_AT + ((size_t)(b * TPB + qt) * 16 + 4 * hd) * 64 + lane;
            const u32x4 r0 = pack8(o0, 0), r1 = pack8(o0, 1), r2 = pack8(o1, 0), r3 = pack8(o1, 1);
            asm volatile("global_store_dwordx4 %0, %1, off sc1\n\tglobal_store_dwordx4 %0, %2, off offset:1024 sc1\n\t"
                         "global_store_dwordx4 %0, %3, off offset:2048 sc1\n\tglobal_store_dwordx4 %0, %4, off offset:3072 sc1\n\t"
                         "s_waitcnt vmcnt(0)" :: "v"(ao), "v"(r0), "v"(r1), "v"(r2), "v"(r3) : "memory");
            if (lane == 0) __hip_atomic_fetch_add((unsigned*)(ws + WS_FL) + (b * TPB + qt) * 16, 1u, __ATOMIC_RELAXED, __HIP_MEMORY_SCOPE_AGENT);
        }
    }
    STAMPR((ws + (10u<<20)), 15);
}

#define DBGC ((u32x4*)ws + (9u<<20))
DI void stageC_body(const u32x4* ws, const uint16_t* __restrict__ dw, uint16_t* __restrict__ out, u32x4* lds) {
    u32x4* fx = lds;
    u32x4* fy = lds + 16 * 64;
    u32x4* fh = lds + 32 * 64;
    float* st0 = (float*)(lds + 32 * 64 + 64 * 64);
    float* st1 = st0 + 512;
    float* st2 = st0 + 1024;
    u32x4* dwl = lds + 32 * 64 + 64 * 64 + 384;
    const int tile = blockIdx.x, tid = threadIdx.x, lane = tid & 63, h = lane >> 5, m = lane & 31;
    const int w = __builtin_amdgcn_readfirstlane(tid >> 6);
    STAMPR(DBGC, 14); STAMP(DBGC, 0);

    const int tt = tile % TPB;
    const int hm = m & 3;
    int toff = hm < 2 ? hm - 2 : 30 + hm;
    const int tl = tt * 32 + toff;
    const bool hvalid = (tl >= 0) && (tl < T_SEQ);
    if (!hvalid) toff = 0;
    const int gtok = tile * 32 + toff;
    const size_t hbase = (size_t)(gtok >> 5) * 16 * 64 + h * 32 + (gtok & 31);

    u32x4 in[8];
    {
        const u32x4* x1 = ws + WS_X1;
        const size_t mb = (size_t)tile * 16 * 64 + lane;
        in[4] = x1[mb + (2 * w) * 64]; in[5] = x1[mb + (2 * w + 1) * 64];
        in[6] = x1[hbase + (2 * w) * 64]; in[7] = x1[hbase + (2 * w + 1) * 64];
    }
    u32x4 dwr;
    if (tid < 160) dwr = ((const u32x4*)dw)[tid];
    RAWBAR();
    constexpr int NFX = NFC;
    const u32x4* wp = ws + WS_WC + (size_t)w * NFC * 64 + lane;
    u32x4 ring[32];
    LD8(0); LD8(8);
    if (w == 0) {
        const int tn = tt + lane - 1;
        const bool need = lane < 3 && tn >= 0 && tn < TPB;
        const unsigned* fl = (const unsigned*)(ws + WS_FL) + (need ? tile + lane - 1 : tile) * 16;
        for (unsigned spin = 0; spin < (1u << 24); ++spin) {
            const unsigned v = __hip_atomic_load(fl, __ATOMIC_RELAXED, __HIP_MEMORY_SCOPE_AGENT);
            if (__all(v >= 4u)) break;
            __builtin_amdgcn_s_sleep(1);
        }
    }
    __syncthreads();
    {
        const __amdgpu_buffer_rsrc_t rs = __builtin_amdgcn_make_buffer_rsrc((void*)(ws + WS_AT), 0, NTILE * 16 * 1024, 0x00020000);
        const int mo = (tile * 16 * 64 + lane + 2 * w * 64) * 16, ho = ((int)hbase + 2 * w * 64) * 16;
        in[0] = __builtin_bit_cast(u32x4, __builtin_amdgcn_raw_buffer_load_b128(rs, mo, 0, 16));
        in[1] = __builtin_bit_cast(u32x4, __builtin_amdgcn_raw_buffer_load_b128(rs, mo + 1024, 0, 16));
        in[2] = __builtin_bit_cast(u32x4, __builtin_amdgcn_raw_buffer_load_b128(rs, ho, 0, 16));
        in[3] = __builtin_bit_cast(u32x4, __builtin_amdgcn_raw_buffer_load_b128(rs, ho + 1024, 0, 16));
    }
    f32x16 xv, yv;
    fx[(2 * w) * 64 + lane] = in[0]; fx[(2 * w + 1) * 64 + lane] = in[1];
    fy[(2 * w) * 64 + lane] = in[2]; fy[(2 * w + 1) * 64 + lane] = in[3];
    if (tid < 160) dwl[tid] = dwr;
    unpack16(xv, in[4], in[5]);
    unpack16(yv, in[6], in[7]);
    __syncthreads();
    STAMP(DBGC, 1);
    {
        f32x16 a = (f32x16){}, b = (f32x16){};
        u32x4 bx[2], by[2];
        bx[0] = fx[lane]; by[0] = fy[lane]; bx[1] = fx[64 + lane]; by[1] = fy[64 + lane];
#pragma unroll
        for (int ks = 0; ks < 16; ++ks) {
            a = MFMA(ring[ks & 31], bx[ks & 1], a);
            b = MFMA(ring[ks & 31], by[ks & 1], b);
            if (ks + 2 < 16) { bx[ks & 1] = fx[(ks + 2) * 64 + lane]; by[ks & 1] = fy[(ks + 2) * 64 + lane]; }
            REFILL(ks, 0);
            SB();
        }
        STAMP(DBGC, 2);
#pragma unroll
        for (int r = 0; r < 16; ++r) { xv[r] = rbf(xv[r] + rbf(a[r])); yv[r] = rbf(yv[r] + rbf(b[r])); }
    }
    float sc, sh, sc2, sh2;
    ln_partial(xv, st0, w, lane);
    ln_partial(yv, st1, w, lane);
    __syncthreads();
    ln_finish(st0, lane, sc, sh);
    ln_finish(st1, lane, sc2, sh2);
    norm_store(xv, sc, sh, fx, w, lane);
    norm_store(yv, sc2, sh2, fy, w, lane);
    __syncthreads();
    STAMP(DBGC, 3);
    {
        f32x16 a = (f32x16){}, b = (f32x16){}, c = (f32x16){}, d = (f32x16){};
        u32x4 bxn = fx[lane], byn = fy[lane];
#pragma unroll
        for (int ks = 0; ks < 16; ++ks) {
            const int F = 16 + 2 * ks;
            const u32x4 bx = bxn, by = byn;
            if (ks + 1 < 16) { bxn = fx[(ks + 1) * 64 + lane]; byn = fy[(ks + 1) * 64 + lane]; }
            a = MFMA(ring[F & 31], bx, a); c = MFMA(ring[F & 31], by, c); REFILL(F, 0);
            b = MFMA(ring[(F + 1) & 31], bx, b); d = MFMA(ring[(F + 1) & 31], by, d); REFILL(F + 1, 0);
            SB();
        }
        STAMP(DBGC, 4);
#pragma unroll
        for (int r = 0; r < 16; ++r) {
            a[r] = a[r] * sigmoidf_(b[r]);
            c[r] = hvalid ? c[r] * sigmoidf_(d[r]) : 0.f;
        }
        f32x4* cs = (f32x4*)fh + w * (4 * 2 * 36);
#pragma unroll
        for (int g = 0; g < 4; ++g) {
            cs[(g * 2 + h) * 36 + m + 2] = (f32x4){a[4 * g], a[4 * g + 1], a[4 * g + 2], a[4 * g + 3]};
            if (m < 4) cs[(g * 2 + h) * 36 + (m < 2 ? m : 32 + m)] = (f32x4){c[4 * g], c[4 * g + 1], c[4 * g + 2], c[4 * g + 3]};
        }
        __syncthreads();
        const uint16_t* dp = (const uint16_t*)dwl + 32 * w + 4 * h;
#pragma unroll
        for (int g = 0; g < 4; ++g) {
            f32x4 acc = (f32x4){0.f, 0.f, 0.f, 0.f};
#pragma unroll
            for (int i = 0; i < 5; ++i) {
                const f32x4 hv = cs[(g * 2 + h) * 36 + m + i];
                const u32x2 t = *(const u32x2*)(dp + i * DM + 8 * g);
                acc[0] += hv[0] * bflo(t[0]); acc[1] += hv[1] * bfhi(t[0]); acc[2] += hv[2] * bflo(t[1]); acc[3] += hv[3] * bfhi(t[1]);
            }
#pragma unroll
            for (int i = 0; i < 4; ++i) a[4 * g + i] = siluf_(acc[i]);
        }
        fx[(2 * w) * 64 + lane] = pack8(a, 0);
        fx[(2 * w + 1) * 64 + lane] = pack8(a, 1);
    }
    __syncthreads();
    STAMP(DBGC, 5);
    {
        f32x16 a;
        GEMM16(a, 48, 64, fx, false);
        STAMP(DBGC, 6);
#pragma unroll
        for (int r = 0; r < 16; ++r) xv[r] = rbf(xv[r] + rbf(a[r]));
    }
    ln_partial(xv, st2, w, lane);
    __syncthreads();
    ln_finish(st2, lane, sc, sh);
    norm_store(xv, sc, sh, fx, w, lane);
    __syncthreads();
    STAMP(DBGC, 7);
    {
        f32x16 acc;
        FFN(acc, 64, DBGC, 8);
#pragma unroll
        for (int r = 0; r < 16; ++r) xv[r] = rbf(__builtin_fmaf(0.5f, rbf(acc[r]), xv[r]));
    }
    ln_partial(xv, st0, w, lane);
    __syncthreads();
    ln_finish(st0, lane, sc, sh);
    {
        uint16_t* op = out + (size_t)(tile * 32 + m) * DM + 32 * w + 4 * h;
#pragma unroll
        for (int g = 0; g < 4; ++g) {
            u32x2 t;
            t[0] = pack2(__builtin_fmaf(xv[4 * g], sc, sh), __builtin_fmaf(xv[4 * g + 1], sc, sh));
            t[1] = pack2(__builtin_fmaf(xv[4 * g + 2], sc, sh), __builtin_fmaf(xv[4 * g + 3], sc, sh));
            *(u32x2*)(op + 8 * g) = t;
        }
    }
    STAMP(DBGC, 11); STAMPR(DBGC, 15);
}

__global__ __launch_bounds__(512, 2) void k_attnC(u32x4* ws, const uint16_t* __restrict__ dw, uint16_t* __restrict__ out) {
    __shared__ u32x4 lds[4 * 2048];
    attn_body(ws, ws, lds);
    if (blockIdx.x >= NTILE) return;
    __syncthreads();
    stageC_body(ws, dw, out, lds);
}

extern "C" void kernel_launch(void* const* d_in, const int* in_sizes, int n_in,
                              void* d_out, int out_size, void* d_ws, size_t ws_size,
                              hipStream_t stream) {
    PackArgs p;
    p.w1a = (const uint16_t*)d_in[1]; p.w1b = (const uint16_t*)d_in[2]; p.wqkv = (const uint16_t*)d_in[3]; p.wo = (const uint16_t*)d_in[4];
    p.wp1 = (const uint16_t*)d_in[5]; p.wp2 = (const uint16_t*)d_in[7]; p.w2a = (const uint16_t*)d_in[8]; p.w2b = (const uint16_t*)d_in[9];
    p.ws = (u32x4*)d_ws;
    u32x4* ws = (u32x4*)d_ws;
    const int nchunk = 8 * (NFA + NFC) * 64;
    k_pack<<<(nchunk + 255) / 256, 256, 0, stream>>>(p);
    k_stageA<<<NTILE, 512, 0, stream>>>((const uint16_t*)d_in[0], ws);
    k_attnC<<<256, 512, 0, stream>>>(ws, (const uint16_t*)d_in[6], (uint16_t*)d_out);
}
```

```cpp
#include <hip/hip_runtime.h>
#include <hip/hip_bf16.h>
#include <stdint.h>

typedef short bf16x8 __attribute__((ext_vector_type(8)));
typedef float f32x16 __attribute__((ext_vector_type(16)));
typedef float f32x2 __attribute__((ext_vector_type(2)));
typedef float f32x4 __attribute__((ext_vector_type(4)));
typedef unsigned u32x4 __attribute__((ext_vector_type(4)));
typedef unsigned u32x2 __attribute__((ext_vector_type(2)));
typedef __bf16 bf16v2 __attribute__((ext_vector_type(2)));

#define DI __device__ __forceinline__
#define SB() __builtin_amdgcn_sched_barrier(0)
#define STAMPR(base, i) do {} while (0)
#define STAMP(base, i) do {} while (0)
#define MFMA(a, b, c) __builtin_amdgcn_mfma_f32_32x32x16_bf16(__builtin_bit_cast(bf16x8, (a)), __builtin_bit_cast(bf16x8, (b)), (c), 0, 0, 0)

constexpr int T_SEQ = 4000, DM = 256, NTOK = 8000, NTILE = 250, TPB = 125;
constexpr int NFA = 176;
constexpr int NFC = 192;

constexpr size_t WS_WA = 0;
constexpr size_t WS_WC = WS_WA + (size_t)8 * NFA * 64;
constexpr size_t WS_X1 = WS_WC + (size_t)8 * NFC * 64;
constexpr size_t WS_Q  = WS_X1 + (size_t)NTILE * 16 * 64;
constexpr size_t WS_KV = WS_Q + (size_t)8 * TPB * 4 * 64;
constexpr size_t WS_AT = WS_KV + (size_t)8 * TPB * 8 * 64 + 4096;
constexpr size_t WS_FL = WS_AT + (size_t)NTILE * 16 * 64;

DI unsigned pack2(float lo, float hi) {
    f32x2 v = {lo, hi};
    bf16v2 r = __builtin_convertvector(v, bf16v2);
    return __builtin_bit_cast(unsigned, r);
}
DI float bflo(unsigned u) { return __uint_as_float(u << 16); }
DI float bfhi(unsigned u) { return __uint_as_float(u & 0xffff0000u); }
DI u32x4 pack8(const f32x16& a, int s) {
    u32x4 r;
    r[0] = pack2(a[8 * s + 0], a[8 * s + 1]);
    r[1] = pack2(a[8 * s + 2], a[8 * s + 3]);
    r[2] = pack2(a[8 * s + 4], a[8 * s + 5]);
    r[3] = pack2(a[8 * s + 6], a[8 * s + 7]);
    return r;
}
DI void unpack16(f32x16& a, const u32x4& f0, const u32x4& f1) {
#pragma unroll
    for (int i = 0; i < 4; ++i) {
        a[2 * i] = bflo(f0[i]); a[2 * i + 1] = bfhi(f0[i]);
        a[8 + 2 * i] = bflo(f1[i]); a[8 + 2 * i + 1] = bfhi(f1[i]);
    }
}
DI float xor32(float v) { return __shfl_xor(v, 32); }
DI float sigmoidf_(float x) { return __builtin_amdgcn_rcpf(1.0f + __builtin_amdgcn_exp2f(-1.4426950408889634f * x)); }
DI float siluf_(float x) { return x * sigmoidf_(x); }

struct PackArgs {
    const uint16_t* w1a; const uint16_t* w1b; const uint16_t* wqkv; const uint16_t* wo;
    const uint16_t* wp1; const uint16_t* wp2; const uint16_t* w2a; const uint16_t* w2b;
    u32x4* ws;
};

__global__ __launch_bounds__(256) void k_pack(PackArgs p) {
    const int gid = blockIdx.x * 256 + threadIdx.x;
    const int lane = gid & 63, fg = gid >> 6;
    if (gid < (NTILE + 16) * 4) p.ws[WS_FL + gid] = (u32x4){0u, 0u, 0u, 0u};
    if (fg >= 8 * (NFA + NFC)) return;
    const uint16_t* W; int N, nt, ks;
    if (fg < 8 * NFA) {
        const int w = fg / NFA, f = fg % NFA;
        if (f < 64)       { W = p.w1a; N = 1024; ks = f & 15; nt = 4 * w + (f >> 4); }
        else if (f < 128) { W = p.w1b; N = 256; ks = f - 64; nt = w; }
        else              { const int g = f - 128; W = p.wqkv; N = 768; ks = g & 15; nt = w + 8 * (g >> 4); }
    } else {
        const int fg2 = fg - 8 * NFA, w = fg2 / NFC, f = fg2 % NFC;
        if (f < 16)       { W = p.wo; N = 256; ks = f; nt = w; }
        else if (f < 48)  { const int g = f - 16; W = p.wp1; N = 512; ks = g >> 1; nt = w + 8 * (g & 1); }
        else if (f < 64)  { W = p.wp2; N = 256; ks = f - 48; nt = w; }
        else if (f < 128) { const int g = f - 64; W = p.w2a; N = 1024; ks = g & 15; nt = 4 * w + (g >> 4); }
        else              { W = p.w2b; N = 256; ks = f - 128; nt = w; }
    }
    const int r = lane & 31, h = lane >> 5;
    const uint16_t* src = W + (size_t)(16 * ks + 4 * h) * N + 32 * nt + r;
    unsigned e[8];
#pragma unroll
    for (int j = 0; j < 8; ++j) e[j] = src[(size_t)(8 * (j >> 2) + (j & 3)) * N];
    u32x4 o;
#pragma unroll
    for (int i = 0; i < 4; ++i) o[i] = e[2 * i] | (e[2 * i + 1] << 16);
    p.ws[gid] = o;
}

DI void glds16(const void* g, unsigned lds_base) {
    unsigned sv;
    asm volatile("s_mov_b32 %0, m0\n\ts_mov_b32 m0, %2\n\ts_nop 0\n\tglobal_load_lds_dwordx4 %1, off\n\ts_mov_b32 m0, %0"
                 : "=&s"(sv) : "v"(g), "s"(lds_base) : "memory");
}
DI void st2_sc1(void* ptr, const u32x4& a, const u32x4& b) {
    asm volatile("global_store_dwordx4 %0, %1, off sc1\n\tglobal_store_dwordx4 %0, %2, off offset:1024 sc1\n\ts_nop 1" :: "v"(ptr), "v"(a), "v"(b) : "memory");
}
DI void glds16c(const void* g, unsigned lds_base) {
    unsigned sv;
    asm volatile("s_mov_b32 %0, m0\n\ts_mov_b32 m0, %2\n\ts_nop 0\n\tglobal_load_lds_dwordx4 %1, off sc1\n\ts_mov_b32 m0, %0"
                 : "=&s"(sv) : "v"(g), "s"(lds_base) : "memory");
}
template <int NI> DI void l2_warm(const u32x4* base, int total_chunks, int rank, int w, int lane, unsigned lds_scratch) {
#pragma unroll
    for (int i = 0; i < NI; ++i) {
        int c = ((rank * NI + i) * 8 + w) * 64;
        c = c + 64 <= total_chunks ? c : total_chunks - 64;
        glds16(base + c + lane, lds_scratch);
    }
}
#define WLOAD(F) ring[(F) & 31] = wp[(F) * 64]
#define REFILL(F, PE) do { if ((F) < 16) WLOAD((F) + 16); if ((F) + 32 < (NFX)) WLOAD((F) + 32); } while (0)
#define LD8(F) do { SB(); _Pragma("unroll") for (int i_ = 0; i_ < 8; ++i_) WLOAD((F) + i_); SB(); } while (0)
#define RAWBAR() do { SB(); __builtin_amdgcn_s_barrier(); SB(); } while (0)

DI void ln_partial(const f32x16& v, float* st, int w, int lane) {
    float s = 0.f, q = 0.f;
#pragma unroll
    for (int i = 0; i < 16; ++i) { s += v[i]; q = __builtin_fmaf(v[i], v[i], q); }
    auto r = __builtin_amdgcn_permlane32_swap(__float_as_uint(s), __float_as_uint(q), false, false);
    st[w * 64 + lane] = __uint_as_float(r[0]) + __uint_as_float(r[1]);
}
DI void ln_finish(const float* st, int lane, float& scale, float& shift) {
    const float* p = st + (lane & 31);
    float s = 0.f, q = 0.f;
#pragma unroll
    for (int i = 0; i < 8; ++i) { s += p[i * 64]; q += p[i * 64 + 32]; }
    const float m = s * (1.0f / 256.0f);
    const float var = fmaxf(q * (1.0f / 256.0f) - m * m, 0.f);
    scale = __builtin_amdgcn_rsqf(var + 1e-5f);
    shift = -m * scale;
}
DI void norm_store(const f32x16& v, float scale, float shift, u32x4* dst  , int w, int lane) {
    f32x16 n;
#pragma unroll
    for (int i = 0; i < 16; ++i) n[i] = __builtin_fmaf(v[i], scale, shift);
    dst[(2 * w) * 64 + lane] = pack8(n, 0);
    dst[(2 * w + 1) * 64 + lane] = pack8(n, 1);
}
DI float rbf(float x) { return bflo(pack2(x, 0.f) & 0xffffu); }

#define GEMM16(acc, F0, PE, fxp, SWAP) do { \
        acc = (f32x16){}; u32x4 bq_[4]; \
        _Pragma("unroll") for (int i_ = 0; i_ < 4; ++i_) bq_[i_] = (fxp)[i_ * 64 + lane]; \
        _Pragma("unroll") for (int ks_ = 0; ks_ < 16; ++ks_) { \
            if (SWAP) acc = MFMA(bq_[ks_ & 3], ring[((F0) + ks_) & 31], acc); else acc = MFMA(ring[((F0) + ks_) & 31], bq_[ks_ & 3], acc); \
            if (ks_ + 4 < 16) bq_[ks_ & 3] = (fxp)[(ks_ + 4) * 64 + lane]; \
            REFILL((F0) + ks_, PE); SB(); } } while (0)

#define FFN(acc, F0, DBG, ST) do { \
        _Pragma("unroll") for (int t_ = 0; t_ < 4; ++t_) { \
            f32x16 a_; GEMM16(a_, (F0) + 16 * t_, (F0) + 64, fx, false); \
            if (t_ == 3) { STAMP(DBG, ST); } \
            _Pragma("unroll") for (int r_ = 0; r_ < 8; ++r_) a_[r_] = siluf_(a_[r_]); \
            fh[((4 * w + t_) * 2 + 0) * 64 + lane] = pack8(a_, 0); \
            _Pragma("unroll") for (int r_ = 8; r_ < 16; ++r_) a_[r_] = siluf_(a_[r_]); \
            fh[((4 * w + t_) * 2 + 1) * 64 + lane] = pack8(a_, 1); } \
        __syncthreads(); \
        STAMP(DBG, ST + 1); \
        acc = (f32x16){}; u32x4 bq_[4]; \
        _Pragma("unroll") for (int i_ = 0; i_ < 4; ++i_) bq_[i_] = fh[i_ * 64 + lane]; \
        _Pragma("unroll") for (int ks_ = 0; ks_ < 64; ++ks_) { \
            acc = MFMA(ring[((F0) + 64 + ks_) & 31], bq_[ks_ & 3], acc); \
            if (ks_ + 4 < 64) bq_[ks_ & 3] = fh[(ks_ + 4) * 64 + lane]; \
            REFILL((F0) + 64 + ks_, (F0) + 128); SB(); } \
        STAMP(DBG, ST + 2); } while (0)

#define DBGA (ws + (8u<<20))
DI void stageA_body(const uint16_t* __restrict__ x, u32x4* ws, u32x4* lds) {
    u32x4* fx = lds;
    u32x4* fh = lds + 16 * 64;
    float* st0 = (float*)(lds + 16 * 64 + 64 * 64);
    float* st1 = st0 + 512;
    const int tile = blockIdx.x, tid = threadIdx.x, lane = tid & 63, h = lane >> 5, m = lane & 31;
    const int w = __builtin_amdgcn_readfirstlane(tid >> 6);
    STAMPR(DBGA, 14); STAMP(DBGA, 0);
    u32x2 xr[4];
    {
        const uint16_t* xp = x + (size_t)(tile * 32 + m) * DM + 32 * w + 4 * h;
#pragma unroll
        for (int g = 0; g < 4; ++g) xr[g] = *(const u32x2*)(xp + 8 * g);
    }
    RAWBAR();
    constexpr int NFX = NFA;
    const u32x4* wp = ws + WS_WA + (size_t)w * NFA * 64 + lane;
    u32x4 ring[32];
    LD8(0); LD8(8);
    f32x16 xv;
#pragma unroll
    for (int g = 0; g < 4; ++g) { xv[4 * g + 0] = bflo(xr[g][0]); xv[4 * g + 1] = bfhi(xr[g][0]); xv[4 * g + 2] = bflo(xr[g][1]); xv[4 * g + 3] = bfhi(xr[g][1]); }
    float sc, sh;
    ln_partial(xv, st0, w, lane);
    __syncthreads();
    ln_finish(st0, lane, sc, sh);
    norm_store(xv, sc, sh, fx, w, lane);
    __syncthreads();
    STAMP(DBGA, 1);
    {
        f32x16 acc;
        FFN(acc, 0, DBGA, 2);
#pragma unroll
        for (int r = 0; r < 16; ++r) xv[r] = rbf(__builtin_fmaf(0.5f, rbf(acc[r]), xv[r]));
    }
    ln_partial(xv, st1, w, lane);
    {
        u32x4* xo = ws + WS_X1 + (size_t)tile * 16 * 64;
        st2_sc1(xo + (2 * w) * 64 + lane, pack8(xv, 0), pack8(xv, 1));
    }
    __syncthreads();
    ln_finish(st1, lane, sc, sh);
    norm_store(xv, sc, sh, fx, w, lane);
    __syncthreads();
    STAMP(DBGA, 5);
    {
        const int b = tile / TPB, tt = tile % TPB, bh = b * 4 + (w >> 1), t2 = w & 1;
        u32x4* qo = ws + WS_Q + ((size_t)(bh * TPB + tt) * 4 + 2 * t2) * 64 + lane;
        u32x4* ko = ws + WS_KV + ((size_t)(bh * TPB + tt) * 8 + 2 * t2) * 64 + lane;
        f32x16 a;
        GEMM16(a, 128, NFA, fx, false);
        const float qs = 0.125f * 1.4426950408889634f;
#pragma unroll
        for (int r = 0; r < 16; ++r) a[r] *= qs;
        st2_sc1(qo, pack8(a, 0), pack8(a, 1));
        GEMM16(a, 144, NFA, fx, false);
        st2_sc1(ko, pack8(a, 0), pack8(a, 1));
        GEMM16(a, 160, NFA, fx, true);
        STAMP(DBGA, 6);
        st2_sc1(ko + 4 * 64, pack8(a, 0), pack8(a, 1));
        asm volatile("s_waitcnt vmcnt(0)" ::: "memory");
        __syncthreads();
        if (tid == 0) __hip_atomic_fetch_add((unsigned*)(ws + WS_FL) + (NTILE + 4 * b) * 16, 1u, __ATOMIC_RELAXED, __HIP_MEMORY_SCOPE_AGENT);
    }
    STAMP(DBGA, 7); STAMPR(DBGA, 15);
}

#define WAIT_BAR(N) asm volatile("s_waitcnt vmcnt(" #N ") lgkmcnt(0)\n\ts_barrier" ::: "memory")

constexpr int AT_SLOT = 1024;
constexpr float AT_BIG = 1.0e30f;
#define ABL_NODMA 0
#define ABL_NOBAR 0
#define ABL_NOLDS 0
#define ABL_NOEXP 0
#define ABL_NOADD 0
#define PIN(x) asm volatile("" : "+v"(x))
DI float vadd(float a, float b) { float r; asm("v_add_f32 %0, %1, %2" : "=v"(r) : "v"(a), "v"(b)); return r; }
#define EX2(x) __builtin_amdgcn_exp2f(x)

DI void attn_body(const u32x4* ws_in, u32x4* ws, u32x4* lds  ) {
    int tid = threadIdx.x; asm volatile("" : "+v"(tid));
    const int lane = tid & 63;
    const int w = __builtin_amdgcn_readfirstlane(tid >> 6);
    STAMP((ws + (10u<<20)), 0);
    const int bh = blockIdx.x & 7, qb = blockIdx.x >> 3, qi = w & 3, g = w >> 2;
    int qt = 4 * qb + qi; const bool qvalid = qt < TPB; if (!qvalid) qt = TPB - 1;
    const u32x4* kv = ws_in + WS_KV + (size_t)bh * TPB * 8 * 64;
    const unsigned lds0 = (unsigned)(uintptr_t)lds;
    const u32x4* src = kv + (w >> 1) * 512 + (w & 1) * 128 + lane;
    const u32x4* src31 = kv + 124 * 512 + (w & 1) * 128 + lane;
    const unsigned dstk = lds0 + (w >> 1) * 4096 + (w & 1) * 2048;
    const u32x4* srcw = kv + qi * 512 + lane;
    const u32x4* srcw31 = kv + 124 * 512 + lane;
    const unsigned dstw = lds0 + qi * 4096;
#define DMA2(sp, dst) do { glds16c((sp), (unsigned)__builtin_amdgcn_readfirstlane(dst)); glds16c((sp) + 64, (unsigned)__builtin_amdgcn_readfirstlane((dst) + 1024)); } while (0)
#define DMA_K(step) do { const int st_ = (step); const u32x4* s_ = st_ < 31 ? src + (size_t)st_ * 2048 : src31; DMA2(s_, dstk + (st_ & 3) * 32768); } while (0)
#define DMA_V(step) do { const int st_ = (step); const u32x4* s_ = st_ < 31 ? src + (size_t)st_ * 2048 : src31; DMA2(s_ + 256, dstk + (st_ & 3) * 32768 + 16384); } while (0)
    if (w == 0) {
        const unsigned* fl = (const unsigned*)(ws_in + WS_FL) + (NTILE + 4 * (bh >> 2)) * 16;
        for (unsigned spin = 0; spin < (1u << 24); ++spin) {
            if (__hip_atomic_load(fl, __ATOMIC_RELAXED, __HIP_MEMORY_SCOPE_AGENT) >= (unsigned)TPB) break;
            __builtin_amdgcn_s_sleep(1);
        }
    }
    __syncthreads();
    DMA_K(0); DMA_V(0); DMA_K(1);
    u32x4 q[4];
    {
        const __amdgpu_buffer_rsrc_t rs = __builtin_amdgcn_make_buffer_rsrc((void*)(ws_in + WS_Q), 0, 8 * TPB * 4 * 1024, 0x00020000);
        const int qoff = ((bh * TPB + qt) * 4 * 64 + lane) * 16;
#pragma unroll
        for (int f = 0; f < 4; ++f) q[f] = __builtin_bit_cast(u32x4, __builtin_amdgcn_raw_buffer_load_b128(rs, qoff + f * 1024, 0, 16));
    }
    DMA_K(2); DMA_V(1);
    const char* lbase = (const char*)lds + (2 * g) * 4096 + lane * 16;
#define KF(off, i) (*(const u32x4*)(lbase + (off) + (i) * 1024))
#define VF(off, i) (*(const u32x4*)(lbase + (off) + 16384 + (i) * 1024))
    f32x16 o0 = (f32x16){}, o1 = (f32x16){};
    float negm;
    f32x4 lsum = (f32x4){0.f, 0.f, 0.f, 0.f};
    u32x4 onesA;
    { const unsigned o_ = ((lane & 15) == ((lane >> 4) & 1)) ? 0x3f803f80u : 0u; onesA = (u32x4){o_, o_, o_, o_}; }
    f32x16 cm, sA, sB;
    u32x4 kf[4], vf[4], pA0, pA1, pB0, pB1;
    WAIT_BAR(4);
    {
#pragma unroll
        for (int i = 0; i < 4; ++i) kf[i] = KF(0, i);
        const f32x16 z = (f32x16){};
        sA = MFMA(kf[0], q[0], z); sA = MFMA(kf[1], q[1], sA); sA = MFMA(kf[2], q[2], sA); sA = MFMA(kf[3], q[3], sA);
#pragma unroll
        for (int i = 0; i < 4; ++i) kf[i] = KF(4096, i);
        float mx = sA[0];
#pragma unroll
        for (int r = 1; r < 16; ++r) mx = fmaxf(mx, sA[r]);
        mx = fmaxf(mx, xor32(mx));
        negm = -mx;
#pragma unroll
        for (int r = 0; r < 16; ++r) { sA[r] += negm; cm[r] = negm; sB[r] = 0.f; }
        pB0 = (u32x4){0u, 0u, 0u, 0u}; pB1 = pB0;
    }
#define FIN(S, i, PK, e) do { PK[e] = pack2(S[i], S[(i) + 1]); PIN(PK); } while (0)
#define EXP2(S, i) do { S[i] = EX2(S[i]); S[(i) + 1] = EX2(S[(i) + 1]); PIN(S); } while (0)
#define LSUM(PK) do { lsum = __builtin_amdgcn_mfma_f32_16x16x32_bf16(__builtin_bit_cast(bf16x8, onesA), __builtin_bit_cast(bf16x8, PK), lsum, 0, 0, 0); PIN(lsum); } while (0)
#define PERIOD(SC, SN, PP0, PP1, PC0, PC1, koff, voff, D0, D1) do { \
        vf[0] = VF(voff, 0); FIN(SN, 14, PP1, 3); SB(); SN = MFMA(kf[0], q[0], cm); PIN(SN); EXP2(SC, 0); SB(); \
        vf[2] = VF(voff, 2); SB(); SN = MFMA(kf[1], q[1], SN); PIN(SN); EXP2(SC, 2); FIN(SC, 0, PC0, 0); SB(); \
        vf[1] = VF(voff, 1); SB(); SN = MFMA(kf[2], q[2], SN); PIN(SN); EXP2(SC, 4); FIN(SC, 2, PC0, 1); SB(); D0; SB(); \
        vf[3] = VF(voff, 3); SB(); SN = MFMA(kf[3], q[3], SN); PIN(SN); EXP2(SC, 6); FIN(SC, 4, PC0, 2); SB(); \
        kf[0] = KF(koff, 0); SB(); o0 = MFMA(vf[0], PP0, o0); PIN(o0); EXP2(SC, 8); FIN(SC, 6, PC0, 3); SB(); LSUM(PP0); SB(); \
        kf[1] = KF(koff, 1); SB(); o1 = MFMA(vf[2], PP0, o1); PIN(o1); EXP2(SC, 10); FIN(SC, 8, PC1, 0); SB(); \
        kf[2] = KF(koff, 2); SB(); o0 = MFMA(vf[1], PP1, o0); PIN(o0); EXP2(SC, 12); FIN(SC, 10, PC1, 1); SB(); LSUM(PP1); SB(); D1; SB(); \
        kf[3] = KF(koff, 3); SB(); o1 = MFMA(vf[3], PP1, o1); PIN(o1); EXP2(SC, 14); FIN(SC, 12, PC1, 2); SB(); \
    } while (0)
    int tk0 = 0, tk1 = 0, tk2 = 0, acc_c = 0, acc_w = 0;
#define TICK(x) do {} while (0)
    int vprev = 0;
    for (int i = 0; i < 31; ++i) {
        const int scur = (i & 3) * 32768, snext = ((i + 1) & 3) * 32768;
        const int sk = i + 3 < 32 ? i + 3 : 31, sv = i + 2 < 32 ? i + 2 : 31;
        const u32x4* pk_ = sk < 31 ? srcw + (size_t)sk * 2048 : srcw31;
        const u32x4* pv_ = (sv < 31 ? srcw + (size_t)sv * 2048 : srcw31) + 256;
        const unsigned dk_ = dstw + (sk & 3) * 32768, dv_ = dstw + (sv & 3) * 32768 + 16384;
#define G2(p, d) do { if (!ABL_NODMA && g == 0) { glds16c((p), (unsigned)__builtin_amdgcn_readfirstlane(d)); glds16c((p) + 64, (unsigned)__builtin_amdgcn_readfirstlane((d) + 1024)); } } while (0)
        SB();
        PERIOD(sA, sB, pB0, pB1, pA0, pA1, snext, vprev, G2(pk_, dk_), G2(pk_ + 128, dk_ + 2048));
        PERIOD(sB, sA, pA0, pA1, pB0, pB1, snext + 4096, scur, G2(pv_, dv_), G2(pv_ + 128, dv_ + 2048));
        vprev = scur + 4096;
        TICK(tk1); if (!ABL_NOBAR) { if (g) WAIT_BAR(0); else WAIT_BAR(8); } TICK(tk2); acc_c += tk1 - tk0; acc_w += tk2 - tk1; tk0 = tk2;
    }
    {
        if (g == 1) {
#pragma unroll
            for (int r = 0; r < 16; ++r) sA[r] = -INFINITY;
        }
        PERIOD(sA, sB, pB0, pB1, pA0, pA1, 0, vprev, (void)0, (void)0);
#pragma unroll
        for (int r = 0; r < 16; ++r) sB[r] = -INFINITY;
        PERIOD(sB, sA, pA0, pA1, pB0, pB1, 0, 3 * 32768, (void)0, (void)0);
    }
    float l;
    { const float la = __shfl(lsum[0], lane & 15), lb = __shfl(lsum[1], lane & 15); l = 0.5f * ((lane & 16) ? lb : la); }
    WAIT_BAR(0);
#undef DMA_K
#undef DMA_V
#undef DMA2
#undef PERIOD
#undef FIN
#undef EXP2
#undef LSUM
#undef G2
#undef KF
#undef VF
    {
        bool bad = !(l < AT_BIG);
#pragma unroll
        for (int r = 0; r < 16; ++r) bad = bad || !(fabsf(o0[r]) < INFINITY) || !(fabsf(o1[r]) < INFINITY);
        if (__builtin_expect(__any(bad), 0)) {
            __builtin_amdgcn_fence(__ATOMIC_ACQUIRE, "agent");
            o0 = (f32x16){}; o1 = (f32x16){}; l = 0.f; float mrun = -INFINITY;
            for (int kt = 0; kt < TPB; ++kt) {
                if (((kt >> 1) & 1) != g) continue;
                const u32x4* kg = kv + (size_t)kt * 512 + lane;
                f32x16 sc = (f32x16){};
#pragma unroll
                for (int f = 0; f < 4; ++f) sc = MFMA(kg[f * 64], q[f], sc);
                float mx = sc[0];
#pragma unroll
                for (int r = 1; r < 16; ++r) mx = fmaxf(mx, sc[r]);
                mx = fmaxf(mx, xor32(mx));
                const float mn = fmaxf(mrun, mx), f = EX2(mrun - mn);
                mrun = mn; l *= f;
#pragma unroll
                for (int r = 0; r < 16; ++r) { o0[r] *= f; o1[r] *= f; sc[r] = EX2(sc[r] - mn); l += sc[r]; }
                const u32x4 p0 = pack8(sc, 0), p1 = pack8(sc, 1);
                o0 = MFMA(kg[256], p0, o0); o1 = MFMA(kg[256 + 128], p0, o1);
                o0 = MFMA(kg[256 + 64], p1, o0); o1 = MFMA(kg[256 + 192], p1, o1);
            }
            negm = -mrun;
        }
    }
    l += xor32(l);
    float* cf = (float*)lds;
    if (g == 1) {
        float* dst = cf + qi * 34 * 64 + lane;
#pragma unroll
        for (int r = 0; r < 16; ++r) { dst[r * 64] = o0[r]; dst[(16 + r) * 64] = o1[r]; }
        dst[32 * 64] = negm; dst[33 * 64] = l;
    }
    __syncthreads();
    if (g == 0) {
        const float* s = cf + qi * 34 * 64 + lane;
        const float negm1 = s[32 * 64], l1 = s[33 * 64];
        const float nm = fminf(negm, negm1);
        const float f0 = __builtin_amdgcn_exp2f(nm - negm), f1 = __builtin_amdgcn_exp2f(nm - negm1);
        const float inv = __builtin_amdgcn_rcpf(l * f0 + l1 * f1);
        const float c0 = f0 * inv, c1 = f1 * inv;
#pragma unroll
        for (int r = 0; r < 16; ++r) { o0[r] = o0[r] * c0 + s[r * 64] * c1; o1[r] = o1[r] * c0 + s[(16 + r) * 64] * c1; }
        if (qvalid) {
            const int b = bh >> 2, hd = bh & 3;
            u32x4* ao = ws + WS_AT + ((size_t)(b * TPB + qt) * 16 + 4 * hd) * 64 + lane;
            const u32x4 r0 = pack8(o0, 0), r1 = pack8(o0, 1), r2 = pack8(o1, 0), r3 = pack8(o1, 1);
            asm volatile("global_store_dwordx4 %0, %1, off sc1\n\tglobal_store_dwordx4 %0, %2, off offset:1024 sc1\n\t"
                         "global_store_dwordx4 %0, %3, off offset:2048 sc1\n\tglobal_store_dwordx4 %0, %4, off offset:3072 sc1\n\t"
                         "s_waitcnt vmcnt(0)" :: "v"(ao), "v"(r0), "v"(r1), "v"(r2), "v"(r3) : "memory");
            if (lane == 0) __hip_atomic_fetch_add((unsigned*)(ws + WS_FL) + (b * TPB + qt) * 16, 1u, __ATOMIC_RELAXED, __HIP_MEMORY_SCOPE_AGENT);
        }
    }
    STAMPR((ws + (10u<<20)), 15);
}

#define DBGC ((u32x4*)ws + (9u<<20))
DI void stageC_body(const u32x4* ws, const uint16_t* __restrict__ dw, uint16_t* __restrict__ out, u32x4* lds) {
    u32x4* fx = lds;
    u32x4* fy = lds + 16 * 64;
    u32x4* fh = lds + 32 * 64;
    float* st0 = (float*)(lds + 32 * 64 + 64 * 64);
    float* st1 = st0 + 512;
    float* st2 = st0 + 1024;
    u32x4* dwl = lds + 32 * 64 + 64 * 64 + 384;
    int tid = threadIdx.x; asm volatile("" : "+v"(tid));
    const int tile = blockIdx.x, lane = tid & 63, h = lane >> 5, m = lane & 31;
    const int w = __builtin_amdgcn_readfirstlane(tid >> 6);
    STAMPR(DBGC, 14); STAMP(DBGC, 0);

    const int tt = tile % TPB;
    const int hm = m & 3;
    int toff = hm < 2 ? hm - 2 : 30 + hm;
    const int tl = tt * 32 + toff;
    const bool hvalid = (tl >= 0) && (tl < T_SEQ);
    if (!hvalid) toff = 0;
    const int gtok = tile * 32 + toff;
    const size_t hbase = (size_t)(gtok >> 5) * 16 * 64 + h * 32 + (gtok & 31);

    u32x4 in[8];
    u32x4 dwr;
    if (tid < 160) dwr = ((const u32x4*)dw)[tid];
    RAWBAR();
    constexpr int NFX = NFC;
    const u32x4* wp = ws + WS_WC + (size_t)w * NFC * 64 + lane;
    u32x4 ring[32];
    LD8(0); LD8(8);
    if (w == 0) {
        const int tn = tt + lane - 1;
        const bool need = lane < 3 && tn >= 0 && tn < TPB;
        const unsigned* fl = (const unsigned*)(ws + WS_FL) + (need ? tile + lane - 1 : tile) * 16;
        for (unsigned spin = 0; spin < (1u << 24); ++spin) {
            const unsigned v = __hip_atomic_load(fl, __ATOMIC_RELAXED, __HIP_MEMORY_SCOPE_AGENT);
            if (__all(v >= 4u)) break;
            __builtin_amdgcn_s_sleep(1);
        }
    }
    __syncthreads();
    {
        const __amdgpu_buffer_rsrc_t rs = __builtin_amdgcn_make_buffer_rsrc((void*)(ws + WS_AT), 0, NTILE * 16 * 1024, 0x00020000);
        const int mo = (tile * 16 * 64 + lane + 2 * w * 64) * 16, ho = ((int)hbase + 2 * w * 64) * 16;
        in[0] = __builtin_bit_cast(u32x4, __builtin_amdgcn_raw_buffer_load_b128(rs, mo, 0, 16));
        in[1] = __builtin_bit_cast(u32x4, __builtin_amdgcn_raw_buffer_load_b128(rs, mo + 1024, 0, 16));
        in[2] = __builtin_bit_cast(u32x4, __builtin_amdgcn_raw_buffer_load_b128(rs, ho, 0, 16));
        in[3] = __builtin_bit_cast(u32x4, __builtin_amdgcn_raw_buffer_load_b128(rs, ho + 1024, 0, 16));
    }
    {
        const __amdgpu_buffer_rsrc_t rs = __builtin_amdgcn_make_buffer_rsrc((void*)(ws + WS_X1), 0, NTILE * 16 * 1024, 0x00020000);
        const int mo = (tile * 16 * 64 + lane + 2 * w * 64) * 16, ho = ((int)hbase + 2 * w * 64) * 16;
        in[4] = __builtin_bit_cast(u32x4, __builtin_amdgcn_raw_buffer_load_b128(rs, mo, 0, 16));
        in[5] = __builtin_bit_cast(u32x4, __builtin_amdgcn_raw_buffer_load_b128(rs, mo + 1024, 0, 16));
        in[6] = __builtin_bit_cast(u32x4, __builtin_amdgcn_raw_buffer_load_b128(rs, ho, 0, 16));
        in[7] = __builtin_bit_cast(u32x4, __builtin_amdgcn_raw_buffer_load_b128(rs, ho + 1024, 0, 16));
    }
    f32x16 xv, yv;
    fx[(2 * w) * 64 + lane] = in[0]; fx[(2 * w + 1) * 64 + lane] = in[1];
    fy[(2 * w) * 64 + lane] = in[2]; fy[(2 * w + 1) * 64 + lane] = in[3];
    if (tid < 160) dwl[tid] = dwr;
    unpack16(xv, in[4], in[5]);
    unpack16(yv, in[6], in[7]);
    __syncthreads();
    STAMP(DBGC, 1);
    {
        f32x16 a = (f32x16){}, b = (f32x16){};
        u32x4 bx[2], by[2];
        bx[0] = fx[lane]; by[0] = fy[lane]; bx[1] = fx[64 + lane]; by[1] = fy[64 + lane];
#pragma unroll
        for (int ks = 0; ks < 16; ++ks) {
            a = MFMA(ring[ks & 31], bx[ks & 1], a);
            b = MFMA(ring[ks & 31], by[ks & 1], b);
            if (ks + 2 < 16) { bx[ks & 1] = fx[(ks + 2) * 64 + lane]; by[ks & 1] = fy[(ks + 2) * 64 + lane]; }
            REFILL(ks, 0);
            SB();
        }
        STAMP(DBGC, 2);
#pragma unroll
        for (int r = 0; r < 16; ++r) { xv[r] = rbf(xv[r] + rbf(a[r])); yv[r] = rbf(yv[r] + rbf(b[r])); }
    }
    float sc, sh, sc2, sh2;
    ln_partial(xv, st0, w, lane);
    ln_partial(yv, st1, w, lane);
    __syncthreads();
    ln_finish(st0, lane, sc, sh);
    ln_finish(st1, lane, sc2, sh2);
    norm_store(xv, sc, sh, fx, w, lane);
    norm_store(yv, sc2, sh2, fy, w, lane);
    __syncthreads();
    STAMP(DBGC, 3);
    {
        f32x16 a = (f32x16){}, b = (f32x16){}, c = (f32x16){}, d = (f32x16){};
        u32x4 bxn = fx[lane], byn = fy[lane];
#pragma unroll
        for (int ks = 0; ks < 16; ++ks) {
            const int F = 16 + 2 * ks;
            const u32x4 bx = bxn, by = byn;
            if (ks + 1 < 16) { bxn = fx[(ks + 1) * 64 + lane]; byn = fy[(ks + 1) * 64 + lane]; }
            a = MFMA(ring[F & 31], bx, a); c = MFMA(ring[F & 31], by, c); REFILL(F, 0);
            b = MFMA(ring[(F + 1) & 31], bx, b); d = MFMA(ring[(F + 1) & 31], by, d); REFILL(F + 1, 0);
            SB();
        }
        STAMP(DBGC, 4);
#pragma unroll
        for (int r = 0; r < 16; ++r) {
            a[r] = a[r] * sigmoidf_(b[r]);
            c[r] = hvalid ? c[r] * sigmoidf_(d[r]) : 0.f;
        }
        f32x4* cs = (f32x4*)fh + w * (4 * 2 * 36);
#pragma unroll
        for (int g = 0; g < 4; ++g) {
            cs[(g * 2 + h) * 36 + m + 2] = (f32x4){a[4 * g], a[4 * g + 1], a[4 * g + 2], a[4 * g + 3]};
            if (m < 4) cs[(g * 2 + h) * 36 + (m < 2 ? m : 32 + m)] = (f32x4){c[4 * g], c[4 * g + 1], c[4 * g + 2], c[4 * g + 3]};
        }
        __syncthreads();
        const uint16_t* dp = (const uint16_t*)dwl + 32 * w + 4 * h;
#pragma unroll
        for (int g = 0; g < 4; ++g) {
            f32x4 acc = (f32x4){0.f, 0.f, 0.f, 0.f};
#pragma unroll
            for (int i = 0; i < 5; ++i) {
                const f32x4 hv = cs[(g * 2 + h) * 36 + m + i];
                const u32x2 t = *(const u32x2*)(dp + i * DM + 8 * g);
                acc[0] += hv[0] * bflo(t[0]); acc[1] += hv[1] * bfhi(t[0]); acc[2] += hv[2] * bflo(t[1]); acc[3] += hv[3] * bfhi(t[1]);
            }
#pragma unroll
            for (int i = 0; i < 4; ++i) a[4 * g + i] = siluf_(acc[i]);
        }
        fx[(2 * w) * 64 + lane] = pack8(a, 0);
        fx[(2 * w + 1) * 64 + lane] = pack8(a, 1);
    }
    __syncthreads();
    STAMP(DBGC, 5);
    {
        f32x16 a;
        GEMM16(a, 48, 64, fx, false);
        STAMP(DBGC, 6);
#pragma unroll
        for (int r = 0; r < 16; ++r) xv[r] = rbf(xv[r] + rbf(a[r]));
    }
    ln_partial(xv, st2, w, lane);
    __syncthreads();
    ln_finish(st2, lane, sc, sh);
    norm_store(xv, sc, sh, fx, w, lane);
    __syncthreads();
    STAMP(DBGC, 7);
    {
        f32x16 acc;
        FFN(acc, 64, DBGC, 8);
#pragma unroll
        for (int r = 0; r < 16; ++r) xv[r] = rbf(__builtin_fmaf(0.5f, rbf(acc[r]), xv[r]));
    }
    ln_partial(xv, st0, w, lane);
    __syncthreads();
    ln_finish(st0, lane, sc, sh);
    {
        uint16_t* op = out + (size_t)(tile * 32 + m) * DM + 32 * w + 4 * h;
#pragma unroll
        for (int g = 0; g < 4; ++g) {
            u32x2 t;
            t[0] = pack2(__builtin_fmaf(xv[4 * g], sc, sh), __builtin_fmaf(xv[4 * g + 1], sc, sh));
            t[1] = pack2(__builtin_fmaf(xv[4 * g + 2], sc, sh), __builtin_fmaf(xv[4 * g + 3], sc, sh));
            *(u32x2*)(op + 8 * g) = t;
        }
    }
    STAMP(DBGC, 11); STAMPR(DBGC, 15);
}

__global__ __launch_bounds__(512, 2) void k_main(const uint16_t* __restrict__ x, u32x4* ws, const uint16_t* __restrict__ dw, uint16_t* __restrict__ out) {
    __shared__ u32x4 lds[4 * 2048];
    if (blockIdx.x < NTILE) stageA_body(x, ws, lds);
    attn_body(ws, ws, lds);
    if (blockIdx.x >= NTILE) return;
    __syncthreads();
    stageC_body(ws, dw, out, lds);
}

extern "C" void kernel_launch(void* const* d_in, const int* in_sizes, int n_in,
                              void* d_out, int out_size, void* d_ws, size_t ws_size,
                              hipStream_t stream) {
    PackArgs p;
    p.w1a = (const uint16_t*)d_in[1]; p.w1b = (const uint16_t*)d_in[2]; p.wqkv = (const uint16_t*)d_in[3]; p.wo = (const uint16_t*)d_in[4];
    p.wp1 = (const uint16_t*)d_in[5]; p.wp2 = (const uint16_t*)d_in[7]; p.w2a = (const uint16_t*)d_in[8]; p.w2b = (const uint16_t*)d_in[9];
    p.ws = (u32x4*)d_ws;
    u32x4* ws = (u32x4*)d_ws;
    const int nchunk = 8 * (NFA + NFC) * 64;
    k_pack<<<(nchunk + 255) / 256, 256, 0, stream>>>(p);
    k_main<<<256, 512, 0, stream>>>((const uint16_t*)d_in[0], ws, (const uint16_t*)d_in[6], (uint16_t*)d_out);
}
```

```cpp
#include <hip/hip_runtime.h>
#include <hip/hip_bf16.h>
#include <stdint.h>

typedef short bf16x8 __attribute__((ext_vector_type(8)));
typedef float f32x16 __attribute__((ext_vector_type(16)));
typedef float f32x2 __attribute__((ext_vector_type(2)));
typedef float f32x4 __attribute__((ext_vector_type(4)));
typedef unsigned u32x4 __attribute__((ext_vector_type(4)));
typedef unsigned u32x2 __attribute__((ext_vector_type(2)));
typedef __bf16 bf16v2 __attribute__((ext_vector_type(2)));

#define DI __device__ __forceinline__
#define SB() __builtin_amdgcn_sched_barrier(0)
#define STAMPR(base, i) do {} while (0)
#define STAMP(base, i) do {} while (0)
#define MFMA(a, b, c) __builtin_amdgcn_mfma_f32_32x32x16_bf16(__builtin_bit_cast(bf16x8, (a)), __builtin_bit_cast(bf16x8, (b)), (c), 0, 0, 0)

constexpr int T_SEQ = 4000, DM = 256, NTOK = 8000, NTILE = 250, TPB = 125;
constexpr int NFA = 176;
constexpr int NFC = 192;

constexpr size_t WS_WA = 0;
constexpr size_t WS_WC = WS_WA + (size_t)8 * NFA * 64;
constexpr size_t WS_X1 = WS_WC + (size_t)8 * NFC * 64;
constexpr size_t WS_Q  = WS_X1 + (size_t)NTILE * 16 * 64;
constexpr size_t WS_KV = WS_Q + (size_t)8 * TPB * 4 * 64;
constexpr size_t WS_AT = WS_KV + (size_t)8 * TPB * 8 * 64 + 4096;
constexpr size_t WS_FL = WS_AT + (size_t)NTILE * 16 * 64;

DI unsigned pack2(float lo, float hi) {
    f32x2 v = {lo, hi};
    bf16v2 r = __builtin_convertvector(v, bf16v2);
    return __builtin_bit_cast(unsigned, r);
}
DI float bflo(unsigned u) { return __uint_as_float(u << 16); }
DI float bfhi(unsigned u) { return __uint_as_float(u & 0xffff0000u); }
DI u32x4 pack8(const f32x16& a, int s) {
    u32x4 r;
    r[0] = pack2(a[8 * s + 0], a[8 * s + 1]);
    r[1] = pack2(a[8 * s + 2], a[8 * s + 3]);
    r[2] = pack2(a[8 * s + 4], a[8 * s + 5]);
    r[3] = pack2(a[8 * s + 6], a[8 * s + 7]);
    return r;
}
DI void unpack16(f32x16& a, const u32x4& f0, const u32x4& f1) {
#pragma unroll
    for (int i = 0; i < 4; ++i) {
        a[2 * i] = bflo(f0[i]); a[2 * i + 1] = bfhi(f0[i]);
        a[8 + 2 * i] = bflo(f1[i]); a[8 + 2 * i + 1] = bfhi(f1[i]);
    }
}
DI float xor32(float v) { return __shfl_xor(v, 32); }
DI float sigmoidf_(float x) { return __builtin_amdgcn_rcpf(1.0f + __builtin_amdgcn_exp2f(-1.4426950408889634f * x)); }
DI float siluf_(float x) { return x * sigmoidf_(x); }

struct PackArgs {
    const uint16_t* w1a; const uint16_t* w1b; const uint16_t* wqkv; const uint16_t* wo;
    const uint16_t* wp1; const uint16_t* wp2; const uint16_t* w2a; const uint16_t* w2b;
    u32x4* ws;
};

__global__ __launch_bounds__(256) void k_pack(PackArgs p) {
    const int gid = blockIdx.x * 256 + threadIdx.x;
    const int lane = gid & 63, fg = gid >> 6;
    if (gid < NTILE * 4) p.ws[WS_FL + gid] = (u32x4){0u, 0u, 0u, 0u};
    if (fg >= 8 * (NFA + NFC)) return;
    const uint16_t* W; int N, nt, ks;
    if (fg < 8 * NFA) {
        const int w = fg / NFA, f = fg % NFA;
        if (f < 64)       { W = p.w1a; N = 1024; ks = f & 15; nt = 4 * w + (f >> 4); }
        else if (f < 128) { W = p.w1b; N = 256; ks = f - 64; nt = w; }
        else              { const int g = f - 128; W = p.wqkv; N = 768; ks = g & 15; nt = w + 8 * (g >> 4); }
    } else {
        const int fg2 = fg - 8 * NFA, w = fg2 / NFC, f = fg2 % NFC;
        if (f < 16)       { W = p.wo; N = 256; ks = f; nt = w; }
        else if (f < 48)  { const int g = f - 16; W = p.wp1; N = 512; ks = g >> 1; nt = w + 8 * (g & 1); }
        else if (f < 64)  { W = p.wp2; N = 256; ks = f - 48; nt = w; }
        else if (f < 128) { const int g = f - 64; W = p.w2a; N = 1024; ks = g & 15; nt = 4 * w + (g >> 4); }
        else              { W = p.w2b; N = 256; ks = f - 128; nt = w; }
    }
    const int r = lane & 31, h = lane >> 5;
    const uint16_t* src = W + (size_t)(16 * ks + 4 * h) * N + 32 * nt + r;
    unsigned e[8];
#pragma unroll
    for (int j = 0; j < 8; ++j) e[j] = src[(size_t)(8 * (j >> 2) + (j & 3)) * N];
    u32x4 o;
#pragma unroll
    for (int i = 0; i < 4; ++i) o[i] = e[2 * i] | (e[2 * i + 1] << 16);
    p.ws[gid] = o;
}

DI void glds16(const void* g, unsigned lds_base) {
    unsigned sv;
    asm volatile("s_mov_b32 %0, m0\n\ts_mov_b32 m0, %2\n\ts_nop 0\n\tglobal_load_lds_dwordx4 %1, off\n\ts_mov_b32 m0, %0"
                 : "=&s"(sv) : "v"(g), "s"(lds_base) : "memory");
}
template <int NI> DI void l2_warm(const u32x4* base, int total_chunks, int rank, int w, int lane, unsigned lds_scratch) {
#pragma unroll
    for (int i = 0; i < NI; ++i) {
        int c = ((rank * NI + i) * 8 + w) * 64;
        c = c + 64 <= total_chunks ? c : total_chunks - 64;
        glds16(base + c + lane, lds_scratch);
    }
}
#define WLOAD(F) ring[(F) & 31] = wp[(F) * 64]
#define REFILL(F, PE) do { if ((F) < 16 && NFX == NFA) WLOAD((F) + 16); if ((F) + 32 < (NFX)) WLOAD((F) + 32); } while (0)
#define LD8(F) do { SB(); _Pragma("unroll") for (int i_ = 0; i_ < 8; ++i_) WLOAD((F) + i_); SB(); } while (0)
#define RAWBAR() do { SB(); __builtin_amdgcn_s_barrier(); SB(); } while (0)

DI void ln_partial(const f32x16& v, float* st, int w, int lane) {
    float s = 0.f, q = 0.f;
#pragma unroll
    for (int i = 0; i < 16; ++i) { s += v[i]; q = __builtin_fmaf(v[i], v[i], q); }
    auto r = __builtin_amdgcn_permlane32_swap(__float_as_uint(s), __float_as_uint(q), false, false);
    st[w * 64 + lane] = __uint_as_float(r[0]) + __uint_as_float(r[1]);
}
DI void ln_finish(const float* st, int lane, float& scale, float& shift) {
    const float* p = st + (lane & 31);
    float s = 0.f, q = 0.f;
#pragma unroll
    for (int i = 0; i < 8; ++i) { s += p[i * 64]; q += p[i * 64 + 32]; }
    const float m = s * (1.0f / 256.0f);
    const float var = fmaxf(q * (1.0f / 256.0f) - m * m, 0.f);
    scale = __builtin_amdgcn_rsqf(var + 1e-5f);
    shift = -m * scale;
}
DI void norm_store(const f32x16& v, float scale, float shift, u32x4* dst  , int w, int lane) {
    f32x16 n;
#pragma unroll
    for (int i = 0; i < 16; ++i) n[i] = __builtin_fmaf(v[i], scale, shift);
    dst[(2 * w) * 64 + lane] = pack8(n, 0);
    dst[(2 * w + 1) * 64 + lane] = pack8(n, 1);
}
DI float rbf(float x) { return bflo(pack2(x, 0.f) & 0xffffu); }

#define GEMM16(acc, F0, PE, fxp, SWAP) do { \
        acc = (f32x16){}; u32x4 bq_[4]; \
        _Pragma("unroll") for (int i_ = 0; i_ < 4; ++i_) bq_[i_] = (fxp)[i_ * 64 + lane]; \
        _Pragma("unroll") for (int ks_ = 0; ks_ < 16; ++ks_) { \
            if (SWAP) acc = MFMA(bq_[ks_ & 3], ring[((F0) + ks_) & 31], acc); else acc = MFMA(ring[((F0) + ks_) & 31], bq_[ks_ & 3], acc); \
            if (ks_ + 4 < 16) bq_[ks_ & 3] = (fxp)[(ks_ + 4) * 64 + lane]; \
            REFILL((F0) + ks_, PE); SB(); } } while (0)

#define FFN(acc, F0, DBG, ST) do { \
        _Pragma("unroll") for (int t_ = 0; t_ < 4; ++t_) { \
            f32x16 a_; GEMM16(a_, (F0) + 16 * t_, (F0) + 64, fx, false); \
            if (t_ == 3) { STAMP(DBG, ST); } \
            _Pragma("unroll") for (int r_ = 0; r_ < 8; ++r_) a_[r_] = siluf_(a_[r_]); \
            fh[((4 * w + t_) * 2 + 0) * 64 + lane] = pack8(a_, 0); \
            _Pragma("unroll") for (int r_ = 8; r_ < 16; ++r_) a_[r_] = siluf_(a_[r_]); \
            fh[((4 * w + t_) * 2 + 1) * 64 + lane] = pack8(a_, 1); } \
        __syncthreads(); \
        STAMP(DBG, ST + 1); \
        acc = (f32x16){}; u32x4 bq_[4]; \
        _Pragma("unroll") for (int i_ = 0; i_ < 4; ++i_) bq_[i_] = fh[i_ * 64 + lane]; \
        _Pragma("unroll") for (int ks_ = 0; ks_ < 64; ++ks_) { \
            acc = MFMA(ring[((F0) + 64 + ks_) & 31], bq_[ks_ & 3], acc); \
            if (ks_ + 4 < 64) bq_[ks_ & 3] = fh[(ks_ + 4) * 64 + lane]; \
            REFILL((F0) + 64 + ks_, (F0) + 128); SB(); } \
        STAMP(DBG, ST + 2); } while (0)

#define DBGA (ws + (8u<<20))
__global__ __launch_bounds__(512, 2) void k_stageA(const uint16_t* __restrict__ x, u32x4* __restrict__ ws) {
    __shared__ u32x4 lds[16 * 64 + 64 * 64 + 256 + 512];
    u32x4* fx = lds;
    u32x4* fh = lds + 16 * 64;
    float* st0 = (float*)(lds + 16 * 64 + 64 * 64);
    float* st1 = st0 + 512;
    const int tile = blockIdx.x, tid = threadIdx.x, lane = tid & 63, h = lane >> 5, m = lane & 31;
    const int w = __builtin_amdgcn_readfirstlane(tid >> 6);
    STAMPR(DBGA, 14); STAMP(DBGA, 0);
    u32x2 xr[4];
    {
        const uint16_t* xp = x + (size_t)(tile * 32 + m) * DM + 32 * w + 4 * h;
#pragma unroll
        for (int g = 0; g < 4; ++g) xr[g] = *(const u32x2*)(xp + 8 * g);
    }
    RAWBAR();
    constexpr int NFX = NFA;
    const u32x4* wp = ws + WS_WA + (size_t)w * NFA * 64 + lane;
    u32x4 ring[32];
    LD8(0); LD8(8);
    f32x16 xv;
#pragma unroll
    for (int g = 0; g < 4; ++g) { xv[4 * g + 0] = bflo(xr[g][0]); xv[4 * g + 1] = bfhi(xr[g][0]); xv[4 * g + 2] = bflo(xr[g][1]); xv[4 * g + 3] = bfhi(xr[g][1]); }
    float sc, sh;
    ln_partial(xv, st0, w, lane);
    __syncthreads();
    ln_finish(st0, lane, sc, sh);
    norm_store(xv, sc, sh, fx, w, lane);
    __syncthreads();
    STAMP(DBGA, 1);
    {
        f32x16 acc;
        FFN(acc, 0, DBGA, 2);
#pragma unroll
        for (int r = 0; r < 16; ++r) xv[r] = rbf(__builtin_fmaf(0.5f, rbf(acc[r]), xv[r]));
    }
    ln_partial(xv, st1, w, lane);
    {
        u32x4* xo = ws + WS_X1 + (size_t)tile * 16 * 64;
        xo[(2 * w) * 64 + lane] = pack8(xv, 0);
        xo[(2 * w + 1) * 64 + lane] = pack8(xv, 1);
    }
    __syncthreads();
    ln_finish(st1, lane, sc, sh);
    norm_store(xv, sc, sh, fx, w, lane);
    __syncthreads();
    STAMP(DBGA, 5);
    {
        const int b = tile / TPB, tt = tile % TPB, bh = b * 4 + (w >> 1), t2 = w & 1;
        u32x4* qo = ws + WS_Q + ((size_t)(bh * TPB + tt) * 4 + 2 * t2) * 64 + lane;
        u32x4* ko = ws + WS_KV + ((size_t)(bh * TPB + tt) * 8 + 2 * t2) * 64 + lane;
        f32x16 a;
        GEMM16(a, 128, NFA, fx, false);
        const float qs = 0.125f * 1.4426950408889634f;
#pragma unroll
        for (int r = 0; r < 16; ++r) a[r] *= qs;
        qo[0] = pack8(a, 0); qo[64] = pack8(a, 1);
        GEMM16(a, 144, NFA, fx, false);
        ko[0] = pack8(a, 0); ko[64] = pack8(a, 1);
        GEMM16(a, 160, NFA, fx, true);
        STAMP(DBGA, 6);
        ko[4 * 64] = pack8(a, 0); ko[5 * 64] = pack8(a, 1);
    }
    STAMP(DBGA, 7); STAMPR(DBGA, 15);
}

#define WAIT_BAR(N) asm volatile("s_waitcnt vmcnt(" #N ") lgkmcnt(0)\n\ts_barrier" ::: "memory")

constexpr int AT_SLOT = 1024;
constexpr float AT_BIG = 1.0e30f;
#define ABL_NODMA 0
#define ABL_NOBAR 0
#define ABL_NOLDS 0
#define ABL_NOEXP 0
#define ABL_NOADD 0
#define PIN(x) asm volatile("" : "+v"(x))
DI float vadd(float a, float b) { float r; asm("v_add_f32 %0, %1, %2" : "=v"(r) : "v"(a), "v"(b)); return r; }
#define EX2(x) __builtin_amdgcn_exp2f(x)

DI void attn_body(const u32x4* ws_in, u32x4* ws, u32x4 (&ring)[32], u32x4* lds  ) {
    const int tid = threadIdx.x, lane = tid & 63;
    const int w = __builtin_amdgcn_readfirstlane(tid >> 6);
    STAMP((ws + (10u<<20)), 0);
    const int bh = blockIdx.x & 7, qb = blockIdx.x >> 3, qi = w & 3, g = w >> 2;
    int qt = 4 * qb + qi; const bool qvalid = qt < TPB; if (!qvalid) qt = TPB - 1;
    const u32x4* kv = ws_in + WS_KV + (size_t)bh * TPB * 8 * 64;
    const unsigned lds0 = (unsigned)(uintptr_t)lds;
    const u32x4* src = kv + (w >> 1) * 512 + (w & 1) * 128 + lane;
    const u32x4* src31 = kv + 124 * 512 + (w & 1) * 128 + lane;
    const unsigned dstk = lds0 + (w >> 1) * 4096 + (w & 1) * 2048;
    const u32x4* srcw = kv + qi * 512 + lane;
    const u32x4* srcw31 = kv + 124 * 512 + lane;
    const unsigned dstw = lds0 + qi * 4096;
#define DMA2(sp, dst) do { glds16((sp), (unsigned)__builtin_amdgcn_readfirstlane(dst)); glds16((sp) + 64, (unsigned)__builtin_amdgcn_readfirstlane((dst) + 1024)); } while (0)
#define DMA_K(step) do { const int st_ = (step); const u32x4* s_ = st_ < 31 ? src + (size_t)st_ * 2048 : src31; DMA2(s_, dstk + (st_ & 3) * 32768); } while (0)
#define DMA_V(step) do { const int st_ = (step); const u32x4* s_ = st_ < 31 ? src + (size_t)st_ * 2048 : src31; DMA2(s_ + 256, dstk + (st_ & 3) * 32768 + 16384); } while (0)
    DMA_K(0); DMA_V(0); DMA_K(1);
    u32x4 q[4];
    {
        const u32x4* qp = ws_in + WS_Q + (size_t)(bh * TPB + qt) * 4 * 64 + lane;
#pragma unroll
        for (int f = 0; f < 4; ++f) q[f] = qp[f * 64];
    }
    DMA_K(2); DMA_V(1);
    const char* lbase = (const char*)lds + (2 * g) * 4096 + lane * 16;
#define KF(off, i) (*(const u32x4*)(lbase + (off) + (i) * 1024))
#define VF(off, i) (*(const u32x4*)(lbase + (off) + 16384 + (i) * 1024))
    f32x16 o0 = (f32x16){}, o1 = (f32x16){};
    float negm;
    f32x4 lsum = (f32x4){0.f, 0.f, 0.f, 0.f};
    u32x4 onesA;
    { const unsigned o_ = ((lane & 15) == ((lane >> 4) & 1)) ? 0x3f803f80u : 0u; onesA = (u32x4){o_, o_, o_, o_}; }
    f32x16 cm, sA, sB;
    u32x4 kf[4], vf[4], pA0, pA1, pB0, pB1;
    WAIT_BAR(4);
    {
#pragma unroll
        for (int i = 0; i < 4; ++i) kf[i] = KF(0, i);
        const f32x16 z = (f32x16){};
        sA = MFMA(kf[0], q[0], z); sA = MFMA(kf[1], q[1], sA); sA = MFMA(kf[2], q[2], sA); sA = MFMA(kf[3], q[3], sA);
#pragma unroll
        for (int i = 0; i < 4; ++i) kf[i] = KF(4096, i);
        float mx = sA[0];
#pragma unroll
        for (int r = 1; r < 16; ++r) mx = fmaxf(mx, sA[r]);
        mx = fmaxf(mx, xor32(mx));
        negm = -mx;
#pragma unroll
        for (int r = 0; r < 16; ++r) { sA[r] += negm; cm[r] = negm; sB[r] = 0.f; }
        pB0 = (u32x4){0u, 0u, 0u, 0u}; pB1 = pB0;
    }
#define FIN(S, i, PK, e) do { PK[e] = pack2(S[i], S[(i) + 1]); PIN(PK); } while (0)
#define EXP2(S, i) do { S[i] = EX2(S[i]); S[(i) + 1] = EX2(S[(i) + 1]); PIN(S); } while (0)
#define LSUM(PK) do { lsum = __builtin_amdgcn_mfma_f32_16x16x32_bf16(__builtin_bit_cast(bf16x8, onesA), __builtin_bit_cast(bf16x8, PK), lsum, 0, 0, 0); PIN(lsum); } while (0)
#define PERIOD(SC, SN, PP0, PP1, PC0, PC1, koff, voff, D0, D1) do { \
        vf[0] = VF(voff, 0); FIN(SN, 14, PP1, 3); SB(); SN = MFMA(kf[0], q[0], cm); PIN(SN); EXP2(SC, 0); SB(); \
        vf[2] = VF(voff, 2); SB(); SN = MFMA(kf[1], q[1], SN); PIN(SN); EXP2(SC, 2); FIN(SC, 0, PC0, 0); SB(); \
        vf[1] = VF(voff, 1); SB(); SN = MFMA(kf[2], q[2], SN); PIN(SN); EXP2(SC, 4); FIN(SC, 2, PC0, 1); SB(); D0; SB(); \
        vf[3] = VF(voff, 3); SB(); SN = MFMA(kf[3], q[3], SN); PIN(SN); EXP2(SC, 6); FIN(SC, 4, PC0, 2); SB(); \
        kf[0] = KF(koff, 0); SB(); o0 = MFMA(vf[0], PP0, o0); PIN(o0); EXP2(SC, 8); FIN(SC, 6, PC0, 3); SB(); LSUM(PP0); SB(); \
        kf[1] = KF(koff, 1); SB(); o1 = MFMA(vf[2], PP0, o1); PIN(o1); EXP2(SC, 10); FIN(SC, 8, PC1, 0); SB(); \
        kf[2] = KF(koff, 2); SB(); o0 = MFMA(vf[1], PP1, o0); PIN(o0); EXP2(SC, 12); FIN(SC, 10, PC1, 1); SB(); LSUM(PP1); SB(); D1; SB(); \
        kf[3] = KF(koff, 3); SB(); o1 = MFMA(vf[3], PP1, o1); PIN(o1); EXP2(SC, 14); FIN(SC, 12, PC1, 2); SB(); \
    } while (0)
    int tk0 = 0, tk1 = 0, tk2 = 0, acc_c = 0, acc_w = 0;
#define TICK(x) do {} while (0)
    int vprev = 0;
    for (int i = 0; i < 31; ++i) {
        const int scur = (i & 3) * 32768, snext = ((i + 1) & 3) * 32768;
        const int sk = i + 3 < 32 ? i + 3 : 31, sv = i + 2 < 32 ? i + 2 : 31;
        const u32x4* pk_ = sk < 31 ? srcw + (size_t)sk * 2048 : srcw31;
        const u32x4* pv_ = (sv < 31 ? srcw + (size_t)sv * 2048 : srcw31) + 256;
        const unsigned dk_ = dstw + (sk & 3) * 32768, dv_ = dstw + (sv & 3) * 32768 + 16384;
#define G2(p, d) do { if (!ABL_NODMA && g == 0) { glds16((p), (unsigned)__builtin_amdgcn_readfirstlane(d)); glds16((p) + 64, (unsigned)__builtin_amdgcn_readfirstlane((d) + 1024)); } } while (0)
        SB();
        PERIOD(sA, sB, pB0, pB1, pA0, pA1, snext, vprev, G2(pk_, dk_), G2(pk_ + 128, dk_ + 2048));
        PERIOD(sB, sA, pA0, pA1, pB0, pB1, snext + 4096, scur, G2(pv_, dv_), G2(pv_ + 128, dv_ + 2048));
        vprev = scur + 4096;
        TICK(tk1); if (!ABL_NOBAR) { if (g) WAIT_BAR(0); else WAIT_BAR(8); } TICK(tk2); acc_c += tk1 - tk0; acc_w += tk2 - tk1; tk0 = tk2;
    }
    {
        if (g == 1) {
#pragma unroll
            for (int r = 0; r < 16; ++r) sA[r] = -INFINITY;
        }
        PERIOD(sA, sB, pB0, pB1, pA0, pA1, 0, vprev, (void)0, (void)0);
#pragma unroll
        for (int r = 0; r < 16; ++r) sB[r] = -INFINITY;
        PERIOD(sB, sA, pA0, pA1, pB0, pB1, 0, 3 * 32768, (void)0, (void)0);
    }
    float l;
    { const float la = __shfl(lsum[0], lane & 15), lb = __shfl(lsum[1], lane & 15); l = 0.5f * ((lane & 16) ? lb : la); }
    WAIT_BAR(0);
#undef DMA_K
#undef DMA_V
#undef DMA2
#undef PERIOD
#undef FIN
#undef EXP2
#undef LSUM
#undef G2
#undef KF
#undef VF
    {
        bool bad = !(l < AT_BIG);
#pragma unroll
        for (int r = 0; r < 16; ++r) bad = bad || !(fabsf(o0[r]) < INFINITY) || !(fabsf(o1[r]) < INFINITY);
        if (__builtin_expect(__any(bad), 0)) {
            o0 = (f32x16){}; o1 = (f32x16){}; l = 0.f; float mrun = -INFINITY;
            for (int kt = 0; kt < TPB; ++kt) {
                if (((kt >> 1) & 1) != g) continue;
                const u32x4* kg = kv + (size_t)kt * 512 + lane;
                f32x16 sc = (f32x16){};
#pragma unroll
                for (int f = 0; f < 4; ++f) sc = MFMA(kg[f * 64], q[f], sc);
                float mx = sc[0];
#pragma unroll
                for (int r = 1; r < 16; ++r) mx = fmaxf(mx, sc[r]);
                mx = fmaxf(mx, xor32(mx));
                const float mn = fmaxf(mrun, mx), f = EX2(mrun - mn);
                mrun = mn; l *= f;
#pragma unroll
                for (int r = 0; r < 16; ++r) { o0[r] *= f; o1[r] *= f; sc[r] = EX2(sc[r] - mn); l += sc[r]; }
                const u32x4 p0 = pack8(sc, 0), p1 = pack8(sc, 1);
                o0 = MFMA(kg[256], p0, o0); o1 = MFMA(kg[256 + 128], p0, o1);
                o0 = MFMA(kg[256 + 64], p1, o0); o1 = MFMA(kg[256 + 192], p1, o1);
            }
            negm = -mrun;
        }
    }
    l += xor32(l);
    float* cf = (float*)lds;
    if (g == 1) {
        float* dst = cf + qi * 34 * 64 + lane;
#pragma unroll
        for (int r = 0; r < 16; ++r) { dst[r * 64] = o0[r]; dst[(16 + r) * 64] = o1[r]; }
        dst[32 * 64] = negm; dst[33 * 64] = l;
    }
    __syncthreads();
    if (g == 0) {
        const float* s = cf + qi * 34 * 64 + lane;
        const float negm1 = s[32 * 64], l1 = s[33 * 64];
        const float nm = fminf(negm, negm1);
        const float f0 = __builtin_amdgcn_exp2f(nm - negm), f1 = __builtin_amdgcn_exp2f(nm - negm1);
        const float inv = __builtin_amdgcn_rcpf(l * f0 + l1 * f1);
        const float c0 = f0 * inv, c1 = f1 * inv;
#pragma unroll
        for (int r = 0; r < 16; ++r) { o0[r] = o0[r] * c0 + s[r * 64] * c1; o1[r] = o1[r] * c0 + s[(16 + r) * 64] * c1; }
        if (qvalid) {
            const int b = bh >> 2, hd = bh & 3;
            u32x4* ao = ws + WS_AT + ((size_t)(b * TPB + qt) * 16 + 4 * hd) * 64 + lane;
            const u32x4 r0 = pack8(o0, 0), r1 = pack8(o0, 1), r2 = pack8(o1, 0), r3 = pack8(o1, 1);
            asm volatile("global_store_dwordx4 %0, %1, off sc1\n\tglobal_store_dwordx4 %0, %2, off offset:1024 sc1\n\t"
                         "global_store_dwordx4 %0, %3, off offset:2048 sc1\n\tglobal_store_dwordx4 %0, %4, off offset:3072 sc1\n\t"
                         "s_nop 1" :: "v"(ao), "v"(r0), "v"(r1), "v"(r2), "v"(r3) : "memory");
        }
    }
    {
        const u32x4* wp = ws_in + WS_WC + (size_t)w * NFC * 64 + lane;
        LD8(0); LD8(8); LD8(16); LD8(24);
    }
    if (g == 0 && qvalid) {
        asm volatile("s_waitcnt vmcnt(32)" ::: "memory");
        if (lane == 0) __hip_atomic_fetch_add((unsigned*)(ws + WS_FL) + ((bh >> 2) * TPB + qt) * 16, 1u, __ATOMIC_RELAXED, __HIP_MEMORY_SCOPE_AGENT);
    }
    STAMPR((ws + (10u<<20)), 15);
}

#define DBGC ((u32x4*)ws + (9u<<20))
DI void stageC_body(const u32x4* ws, const uint16_t* __restrict__ dw, uint16_t* __restrict__ out, u32x4 (&ring)[32], u32x4* lds) {
    u32x4* fx = lds;
    u32x4* fy = lds + 16 * 64;
    u32x4* fh = lds + 32 * 64;
    float* st0 = (float*)(lds + 32 * 64 + 64 * 64);
    float* st1 = st0 + 512;
    float* st2 = st0 + 1024;
    u32x4* dwl = lds + 32 * 64 + 64 * 64 + 384;
    const int tile = blockIdx.x, tid = threadIdx.x, lane = tid & 63, h = lane >> 5, m = lane & 31;
    const int w = __builtin_amdgcn_readfirstlane(tid >> 6);
    STAMPR(DBGC, 14); STAMP(DBGC, 0);

#define MFMA16(a, b, c) __builtin_amdgcn_mfma_f32_16x16x32_bf16(__builtin_bit_cast(bf16x8, (a)), __builtin_bit_cast(bf16x8, (b)), (c), 0, 0, 0)
    const int tt = tile % TPB;
    const int c16 = lane & 15, g4 = lane >> 4, hm = lane & 3;
    int toff = hm < 2 ? hm - 2 : 30 + hm;
    const int tl = tt * 32 + toff;
    const bool hvalid = (tl >= 0) && (tl < T_SEQ);
    if (!hvalid) toff = 0;
    const int gtok = tile * 32 + toff;
    u32x4* hal = fy;
    const u32x4* hrd = (c16 < 8 && (c16 >> 2) == (g4 & 1)) ? hal + hm * 32 + (g4 >> 1) : hal + 128;

    u32x4 in[4]; u32x2 xh;
    {
        const u32x4* x1 = ws + WS_X1;
        const size_t mb = (size_t)tile * 16 * 64 + lane;
        in[2] = x1[mb + (2 * w) * 64]; in[3] = x1[mb + (2 * w + 1) * 64];
        xh = *(const u32x2*)((const char*)(x1 + ((size_t)(gtok >> 5) * 16 + 2 * w + ((c16 >> 2) & 1)) * 64 + (g4 & 1) * 32 + (gtok & 31)) + 8 * (g4 >> 1));
    }
    u32x4 dwr;
    if (tid < 160) dwr = ((const u32x4*)dw)[tid];
    constexpr int NFX = NFC;
    const u32x4* wp = ws + WS_WC + (size_t)w * NFC * 64 + lane;
    if (w == 0) {
        const int tn = tt + lane - 1;
        const bool need = lane < 3 && tn >= 0 && tn < TPB;
        const unsigned* fl = (const unsigned*)(ws + WS_FL) + (need ? tile + lane - 1 : tile) * 16;
        for (unsigned spin = 0; spin < (1u << 24); ++spin) {
            const unsigned v = __hip_atomic_load(fl, __ATOMIC_RELAXED, __HIP_MEMORY_SCOPE_AGENT);
            if (__all(v >= 4u)) break;
            __builtin_amdgcn_s_sleep(1);
        }
    }
    __syncthreads();
    u32x4 hin;
    {
        const __amdgpu_buffer_rsrc_t rs = __builtin_amdgcn_make_buffer_rsrc((void*)(ws + WS_AT), 0, NTILE * 16 * 1024, 0x00020000);
        const int mo = (tile * 16 * 64 + lane + 2 * w * 64) * 16;
        in[0] = __builtin_bit_cast(u32x4, __builtin_amdgcn_raw_buffer_load_b128(rs, mo, 0, 16));
        in[1] = __builtin_bit_cast(u32x4, __builtin_amdgcn_raw_buffer_load_b128(rs, mo + 1024, 0, 16));
        const int ho = (((gtok >> 5) * 16 + 2 * w + ((lane >> 3) & 1)) * 64 + ((lane >> 2) & 1) * 32 + (gtok & 31)) * 16;
        hin = __builtin_bit_cast(u32x4, __builtin_amdgcn_raw_buffer_load_b128(rs, ho, 0, 16));
    }
    f32x16 xv; f32x4 yv;
    fx[(2 * w) * 64 + lane] = in[0]; fx[(2 * w + 1) * 64 + lane] = in[1];
    if (lane < 16) hal[hm * 32 + (2 * w + ((lane >> 3) & 1)) * 2 + ((lane >> 2) & 1)] = hin;
    if (tid >= 480) hal[128 + tid - 480] = (u32x4){0u, 0u, 0u, 0u};
    if (tid < 160) dwl[tid] = dwr;
    unpack16(xv, in[2], in[3]);
    yv = (f32x4){bflo(xh[0]), bfhi(xh[0]), bflo(xh[1]), bfhi(xh[1])};
    __syncthreads();
    STAMP(DBGC, 1);
    {
        f32x16 a = (f32x16){}; f32x4 b = (f32x4){0.f, 0.f, 0.f, 0.f};
        u32x4 bx[2], by[2];
        bx[0] = fx[lane]; by[0] = hrd[0]; bx[1] = fx[64 + lane]; by[1] = hrd[2];
#pragma unroll
        for (int ks = 0; ks < 16; ++ks) {
            a = MFMA(ring[ks & 31], bx[ks & 1], a);
            b = MFMA16(ring[ks & 31], by[ks & 1], b);
            if (ks + 2 < 16) { bx[ks & 1] = fx[(ks + 2) * 64 + lane]; by[ks & 1] = hrd[2 * (ks + 2)]; }
            REFILL(ks, 0);
            SB();
        }
        STAMP(DBGC, 2);
#pragma unroll
        for (int r = 0; r < 16; ++r) xv[r] = rbf(xv[r] + rbf(a[r]));
#pragma unroll
        for (int r = 0; r < 4; ++r) yv[r] = rbf(yv[r] + rbf(b[r]));
    }
    float sc, sh;
    ln_partial(xv, st0, w, lane);
    {
        float s_ = (yv[0] + yv[1]) + (yv[2] + yv[3]), q_ = (yv[0] * yv[0] + yv[1] * yv[1]) + (yv[2] * yv[2] + yv[3] * yv[3]);
        s_ += __shfl_xor(s_, 4); q_ += __shfl_xor(q_, 4);
        s_ += __shfl_xor(s_, 16); q_ += __shfl_xor(q_, 16);
        s_ += __shfl_xor(s_, 32); q_ += __shfl_xor(q_, 32);
        if (lane < 4) { st1[w * 8 + lane] = s_; st1[w * 8 + 4 + lane] = q_; }
    }
    __syncthreads();
    ln_finish(st0, lane, sc, sh);
    norm_store(xv, sc, sh, fx, w, lane);
    {
        float s_ = 0.f, q_ = 0.f;
#pragma unroll
        for (int i = 0; i < 8; ++i) { s_ += st1[i * 8 + hm]; q_ += st1[i * 8 + 4 + hm]; }
        const float m_ = s_ * (1.0f / 256.0f);
        const float sc2 = __builtin_amdgcn_rsqf(fmaxf(q_ * (1.0f / 256.0f) - m_ * m_, 0.f) + 1e-5f), sh2 = -m_ * sc2;
        const u32x2 o_ = {pack2(__builtin_fmaf(yv[0], sc2, sh2), __builtin_fmaf(yv[1], sc2, sh2)), pack2(__builtin_fmaf(yv[2], sc2, sh2), __builtin_fmaf(yv[3], sc2, sh2))};
        if (c16 < 8) *(u32x2*)((char*)(hal + hm * 32 + (2 * w + (c16 >> 2)) * 2 + (g4 & 1)) + 8 * (g4 >> 1)) = o_;
    }
    __syncthreads();
    STAMP(DBGC, 3);
    {
        f32x16 a = (f32x16){}, b = (f32x16){}; f32x4 c = (f32x4){0.f, 0.f, 0.f, 0.f}, d = c;
        u32x4 bxn = fx[lane], byn = hrd[0];
#pragma unroll
        for (int ks = 0; ks < 16; ++ks) {
            const int F = 16 + 2 * ks;
            const u32x4 bx = bxn, by = byn;
            if (ks + 1 < 16) { bxn = fx[(ks + 1) * 64 + lane]; byn = hrd[2 * (ks + 1)]; }
            a = MFMA(ring[F & 31], bx, a); c = MFMA16(ring[F & 31], by, c); REFILL(F, 0);
            b = MFMA(ring[(F + 1) & 31], bx, b); d = MFMA16(ring[(F + 1) & 31], by, d); REFILL(F + 1, 0);
            SB();
        }
        STAMP(DBGC, 4);
#pragma unroll
        for (int r = 0; r < 16; ++r) a[r] = a[r] * sigmoidf_(b[r]);
#pragma unroll
        for (int r = 0; r < 4; ++r) c[r] = hvalid ? c[r] * sigmoidf_(d[r]) : 0.f;
        f32x4* cs = (f32x4*)fh + w * (4 * 2 * 36);
#pragma unroll
        for (int g = 0; g < 4; ++g) cs[(g * 2 + h) * 36 + m + 2] = (f32x4){a[4 * g], a[4 * g + 1], a[4 * g + 2], a[4 * g + 3]};
        if (c16 < 8) cs[((2 * (c16 >> 2) + (g4 >> 1)) * 2 + (g4 & 1)) * 36 + (hm < 2 ? hm : 32 + hm)] = c;
        __syncthreads();
        const uint16_t* dp = (const uint16_t*)dwl + 32 * w + 4 * h;
#pragma unroll
        for (int g = 0; g < 4; ++g) {
            f32x4 acc = (f32x4){0.f, 0.f, 0.f, 0.f};
#pragma unroll
            for (int i = 0; i < 5; ++i) {
                const f32x4 hv = cs[(g * 2 + h) * 36 + m + i];
                const u32x2 t = *(const u32x2*)(dp + i * DM + 8 * g);
                acc[0] += hv[0] * bflo(t[0]); acc[1] += hv[1] * bfhi(t[0]); acc[2] += hv[2] * bflo(t[1]); acc[3] += hv[3] * bfhi(t[1]);
            }
#pragma unroll
            for (int i = 0; i < 4; ++i) a[4 * g + i] = siluf_(acc[i]);
        }
        fx[(2 * w) * 64 + lane] = pack8(a, 0);
        fx[(2 * w + 1) * 64 + lane] = pack8(a, 1);
    }
    __syncthreads();
    STAMP(DBGC, 5);
    {
        f32x16 a;
        GEMM16(a, 48, 64, fx, false);
        STAMP(DBGC, 6);
#pragma unroll
        for (int r = 0; r < 16; ++r) xv[r] = rbf(xv[r] + rbf(a[r]));
    }
    ln_partial(xv, st2, w, lane);
    __syncthreads();
    ln_finish(st2, lane, sc, sh);
    norm_store(xv, sc, sh, fx, w, lane);
    __syncthreads();
    STAMP(DBGC, 7);
    {
        f32x16 acc;
        FFN(acc, 64, DBGC, 8);
#pragma unroll
        for (int r = 0; r < 16; ++r) xv[r] = rbf(__builtin_fmaf(0.5f, rbf(acc[r]), xv[r]));
    }
    ln_partial(xv, st0, w, lane);
    __syncthreads();
    ln_finish(st0, lane, sc, sh);
    {
        uint16_t* op = out + (size_t)(tile * 32 + m) * DM + 32 * w + 4 * h;
#pragma unroll
        for (int g = 0; g < 4; ++g) {
            u32x2 t;
            t[0] = pack2(__builtin_fmaf(xv[4 * g], sc, sh), __builtin_fmaf(xv[4 * g + 1], sc, sh));
            t[1] = pack2(__builtin_fmaf(xv[4 * g + 2], sc, sh), __builtin_fmaf(xv[4 * g + 3], sc, sh));
            *(u32x2*)(op + 8 * g) = t;
        }
    }
    STAMP(DBGC, 11); STAMPR(DBGC, 15);
}

__global__ __launch_bounds__(512, 2) void k_attnC(u32x4* ws, const uint16_t* __restrict__ dw, uint16_t* __restrict__ out) {
    __shared__ u32x4 lds[4 * 2048];
    u32x4 ring[32];
    attn_body(ws, ws, ring, lds);
    if (blockIdx.x >= NTILE) return;
    __syncthreads();
    stageC_body(ws, dw, out, ring, lds);
}

extern "C" void kernel_launch(void* const* d_in, const int* in_sizes, int n_in,
                              void* d_out, int out_size, void* d_ws, size_t ws_size,
                              hipStream_t stream) {
    PackArgs p;
    p.w1a = (const uint16_t*)d_in[1]; p.w1b = (const uint16_t*)d_in[2]; p.wqkv = (const uint16_t*)d_in[3]; p.wo = (const uint16_t*)d_in[4];
    p.wp1 = (const uint16_t*)d_in[5]; p.wp2 = (const uint16_t*)d_in[7]; p.w2a = (const uint16_t*)d_in[8]; p.w2b = (const uint16_t*)d_in[9];
    p.ws = (u32x4*)d_ws;
    u32x4* ws = (u32x4*)d_ws;
    const int nchunk = 8 * (NFA + NFC) * 64;
    k_pack<<<(nchunk + 255) / 256, 256, 0, stream>>>(p);
    k_stageA<<<NTILE, 512, 0, stream>>>((const uint16_t*)d_in[0], ws);
    k_attnC<<<256, 512, 0, stream>>>(ws, (const uint16_t*)d_in[6], (uint16_t*)d_out);
}
```

```cpp
#include <hip/hip_runtime.h>
#include <hip/hip_bf16.h>
#include <stdint.h>

typedef short bf16x8 __attribute__((ext_vector_type(8)));
typedef float f32x16 __attribute__((ext_vector_type(16)));
typedef float f32x2 __attribute__((ext_vector_type(2)));
typedef float f32x4 __attribute__((ext_vector_type(4)));
typedef unsigned u32x4 __attribute__((ext_vector_type(4)));
typedef unsigned u32x2 __attribute__((ext_vector_type(2)));
typedef __bf16 bf16v2 __attribute__((ext_vector_type(2)));

#define DI __device__ __forceinline__
#define SB() __builtin_amdgcn_sched_barrier(0)
#define STAMPR(base, i) do {} while (0)
#define STAMP(base, i) do {} while (0)
#define MFMA(a, b, c) __builtin_amdgcn_mfma_f32_32x32x16_bf16(__builtin_bit_cast(bf16x8, (a)), __builtin_bit_cast(bf16x8, (b)), (c), 0, 0, 0)

constexpr int T_SEQ = 4000, DM = 256, NTOK = 8000, NTILE = 250, TPB = 125;
constexpr int NFA = 176;
constexpr int NFC = 192;

constexpr size_t WS_WA = 0;
constexpr size_t WS_WC = WS_WA + (size_t)8 * NFA * 64;
constexpr size_t WS_X1 = WS_WC + (size_t)8 * NFC * 64;
constexpr size_t WS_Q  = WS_X1 + (size_t)NTILE * 16 * 64;
constexpr size_t WS_KV = WS_Q + (size_t)8 * TPB * 4 * 64;
constexpr size_t WS_AT = WS_KV + (size_t)8 * TPB * 8 * 64 + 4096;
constexpr size_t WS_FL = WS_AT + (size_t)NTILE * 16 * 64;

DI unsigned pack2(float lo, float hi) {
    f32x2 v = {lo, hi};
    bf16v2 r = __builtin_convertvector(v, bf16v2);
    return __builtin_bit_cast(unsigned, r);
}
DI float bflo(unsigned u) { return __uint_as_float(u << 16); }
DI float bfhi(unsigned u) { return __uint_as_float(u & 0xffff0000u); }
DI u32x4 pack8(const f32x16& a, int s) {
    u32x4 r;
    r[0] = pack2(a[8 * s + 0], a[8 * s + 1]);
    r[1] = pack2(a[8 * s + 2], a[8 * s + 3]);
    r[2] = pack2(a[8 * s + 4], a[8 * s + 5]);
    r[3] = pack2(a[8 * s + 6], a[8 * s + 7]);
    return r;
}
DI void unpack16(f32x16& a, const u32x4& f0, const u32x4& f1) {
#pragma unroll
    for (int i = 0; i < 4; ++i) {
        a[2 * i] = bflo(f0[i]); a[2 * i + 1] = bfhi(f0[i]);
        a[8 + 2 * i] = bflo(f1[i]); a[8 + 2 * i + 1] = bfhi(f1[i]);
    }
}
DI float xor32(float v) { return __shfl_xor(v, 32); }
DI float sigmoidf_(float x) { return __builtin_amdgcn_rcpf(1.0f + __builtin_amdgcn_exp2f(-1.4426950408889634f * x)); }
DI float siluf_(float x) { return x * sigmoidf_(x); }

struct PackArgs {
    const uint16_t* w1a; const uint16_t* w1b; const uint16_t* wqkv; const uint16_t* wo;
    const uint16_t* wp1; const uint16_t* wp2; const uint16_t* w2a; const uint16_t* w2b;
    u32x4* ws;
};

__global__ __launch_bounds__(256) void k_pack(PackArgs p) {
    const int gid = blockIdx.x * 256 + threadIdx.x;
    const int lane = gid & 63, fg = gid >> 6;
    if (gid < NTILE * 4) p.ws[WS_FL + gid] = (u32x4){0u, 0u, 0u, 0u};
    if (fg >= 8 * (NFA + NFC)) return;
    const uint16_t* W; int N, nt, ks;
    if (fg < 8 * NFA) {
        const int w = fg / NFA, f = fg % NFA;
        if (f < 64)       { W = p.w1a; N = 1024; ks = f & 15; nt = 4 * w + (f >> 4); }
        else if (f < 128) { W = p.w1b; N = 256; ks = f - 64; nt = w; }
        else              { const int g = f - 128; W = p.wqkv; N = 768; ks = g & 15; nt = w + 8 * (g >> 4); }
    } else {
        const int fg2 = fg - 8 * NFA, w = fg2 / NFC, f = fg2 % NFC;
        if (f < 16)       { W = p.wo; N = 256; ks = f; nt = w; }
        else if (f < 48)  { const int g = f - 16; W = p.wp1; N = 512; ks = g >> 1; nt = w + 8 * (g & 1); }
        else if (f < 64)  { W = p.wp2; N = 256; ks = f - 48; nt = w; }
        else if (f < 128) { const int g = f - 64; W = p.w2a; N = 1024; ks = g & 15; nt = 4 * w + (g >> 4); }
        else              { W = p.w2b; N = 256; ks = f - 128; nt = w; }
    }
    const int r = lane & 31, h = lane >> 5;
    const uint16_t* src = W + (size_t)(16 * ks + 4 * h) * N + 32 * nt + r;
    unsigned e[8];
#pragma unroll
    for (int j = 0; j < 8; ++j) e[j] = src[(size_t)(8 * (j >> 2) + (j & 3)) * N];
    u32x4 o;
#pragma unroll
    for (int i = 0; i < 4; ++i) o[i] = e[2 * i] | (e[2 * i + 1] << 16);
    p.ws[gid] = o;
}

DI void glds16(const void* g, unsigned lds_base) {
    unsigned sv;
    asm volatile("s_mov_b32 %0, m0\n\ts_mov_b32 m0, %2\n\ts_nop 0\n\tglobal_load_lds_dwordx4 %1, off\n\ts_mov_b32 m0, %0"
                 : "=&s"(sv) : "v"(g), "s"(lds_base) : "memory");
}
template <int NI> DI void l2_warm(const u32x4* base, int total_chunks, int rank, int w, int lane, unsigned lds_scratch) {
#pragma unroll
    for (int i = 0; i < NI; ++i) {
        int c = ((rank * NI + i) * 8 + w) * 64;
        c = c + 64 <= total_chunks ? c : total_chunks - 64;
        glds16(base + c + lane, lds_scratch);
    }
}
#define WLOAD(F) ring[(F) & 31] = wp[(F) * 64]
#define REFILL(F, PE) do { if ((F) < 16 && NFX == NFA) WLOAD((F) + 16); if ((F) + 32 < (NFX)) WLOAD((F) + 32); } while (0)
#define LD8(F) do { SB(); _Pragma("unroll") for (int i_ = 0; i_ < 8; ++i_) WLOAD((F) + i_); SB(); } while (0)
#define RAWBAR() do { SB(); __builtin_amdgcn_s_barrier(); SB(); } while (0)

DI void ln_partial(const f32x16& v, float* st, int w, int lane) {
    float s = 0.f, q = 0.f;
#pragma unroll
    for (int i = 0; i < 16; ++i) { s += v[i]; q = __builtin_fmaf(v[i], v[i], q); }
    auto r = __builtin_amdgcn_permlane32_swap(__float_as_uint(s), __float_as_uint(q), false, false);
    st[w * 64 + lane] = __uint_as_float(r[0]) + __uint_as_float(r[1]);
}
DI void ln_finish(const float* st, int lane, float& scale, float& shift) {
    const float* p = st + (lane & 31);
    float s = 0.f, q = 0.f;
#pragma unroll
    for (int i = 0; i < 8; ++i) { s += p[i * 64]; q += p[i * 64 + 32]; }
    const float m = s * (1.0f / 256.0f);
    const float var = fmaxf(q * (1.0f / 256.0f) - m * m, 0.f);
    scale = __builtin_amdgcn_rsqf(var + 1e-5f);
    shift = -m * scale;
}
DI void norm_store(const f32x16& v, float scale, float shift, u32x4* dst  , int w, int lane) {
    f32x16 n;
#pragma unroll
    for (int i = 0; i < 16; ++i) n[i] = __builtin_fmaf(v[i], scale, shift);
    dst[(2 * w) * 64 + lane] = pack8(n, 0);
    dst[(2 * w + 1) * 64 + lane] = pack8(n, 1);
}
DI float rbf(float x) { return bflo(pack2(x, 0.f) & 0xffffu); }

#define GEMM16(acc, F0, PE, fxp, SWAP) do { \
        acc = (f32x16){}; u32x4 bq_[4]; \
        _Pragma("unroll") for (int i_ = 0; i_ < 4; ++i_) bq_[i_] = (fxp)[i_ * 64 + lane]; \
        _Pragma("unroll") for (int ks_ = 0; ks_ < 16; ++ks_) { \
            if (SWAP) acc = MFMA(bq_[ks_ & 3], ring[((F0) + ks_) & 31], acc); else acc = MFMA(ring[((F0) + ks_) & 31], bq_[ks_ & 3], acc); \
            if (ks_ + 4 < 16) bq_[ks_ & 3] = (fxp)[(ks_ + 4) * 64 + lane]; \
            REFILL((F0) + ks_, PE); SB(); } } while (0)

#define FFN(acc, F0, DBG, ST) do { \
        _Pragma("unroll") for (int t_ = 0; t_ < 4; ++t_) { \
            f32x16 a_; GEMM16(a_, (F0) + 16 * t_, (F0) + 64, fx, false); \
            if (t_ == 3) { STAMP(DBG, ST); } \
            _Pragma("unroll") for (int r_ = 0; r_ < 8; ++r_) a_[r_] = siluf_(a_[r_]); \
            fh[((4 * w + t_) * 2 + 0) * 64 + lane] = pack8(a_, 0); \
            _Pragma("unroll") for (int r_ = 8; r_ < 16; ++r_) a_[r_] = siluf_(a_[r_]); \
            fh[((4 * w + t_) * 2 + 1) * 64 + lane] = pack8(a_, 1); } \
        __syncthreads(); \
        STAMP(DBG, ST + 1); \
        acc = (f32x16){}; u32x4 bq_[4]; \
        _Pragma("unroll") for (int i_ = 0; i_ < 4; ++i_) bq_[i_] = fh[i_ * 64 + lane]; \
        _Pragma("unroll") for (int ks_ = 0; ks_ < 64; ++ks_) { \
            acc = MFMA(ring[((F0) + 64 + ks_) & 31], bq_[ks_ & 3], acc); \
            if (ks_ + 4 < 64) bq_[ks_ & 3] = fh[(ks_ + 4) * 64 + lane]; \
            REFILL((F0) + 64 + ks_, (F0) + 128); SB(); } \
        STAMP(DBG, ST + 2); } while (0)

#define DBGA (ws + (8u<<20))
__global__ __launch_bounds__(512, 2) void k_stageA(const uint16_t* __restrict__ x, u32x4* __restrict__ ws) {
    __shared__ u32x4 lds[16 * 64 + 64 * 64 + 256 + 512];
    u32x4* fx = lds;
    u32x4* fh = lds + 16 * 64;
    float* st0 = (float*)(lds + 16 * 64 + 64 * 64);
    float* st1 = st0 + 512;
    const int tile = blockIdx.x, tid = threadIdx.x, lane = tid & 63, h = lane >> 5, m = lane & 31;
    const int w = __builtin_amdgcn_readfirstlane(tid >> 6);
    STAMPR(DBGA, 14); STAMP(DBGA, 0);
    u32x2 xr[4];
    {
        const uint16_t* xp = x + (size_t)(tile * 32 + m) * DM + 32 * w + 4 * h;
#pragma unroll
        for (int g = 0; g < 4; ++g) xr[g] = *(const u32x2*)(xp + 8 * g);
    }
    RAWBAR();
    constexpr int NFX = NFA;
    const u32x4* wp = ws + WS_WA + (size_t)w * NFA * 64 + lane;
    u32x4 ring[32];
    LD8(0); LD8(8);
    f32x16 xv;
#pragma unroll
    for (int g = 0; g < 4; ++g) { xv[4 * g + 0] = bflo(xr[g][0]); xv[4 * g + 1] = bfhi(xr[g][0]); xv[4 * g + 2] = bflo(xr[g][1]); xv[4 * g + 3] = bfhi(xr[g][1]); }
    float sc, sh;
    ln_partial(xv, st0, w, lane);
    __syncthreads();
    ln_finish(st0, lane, sc, sh);
    norm_store(xv, sc, sh, fx, w, lane);
    __syncthreads();
    STAMP(DBGA, 1);
    {
        f32x16 acc;
        FFN(acc, 0, DBGA, 2);
#pragma unroll
        for (int r = 0; r < 16; ++r) xv[r] = rbf(__builtin_fmaf(0.5f, rbf(acc[r]), xv[r]));
    }
    ln_partial(xv, st1, w, lane);
    {
        u32x4* xo = ws + WS_X1 + (size_t)tile * 16 * 64;
        xo[(2 * w) * 64 + lane] = pack8(xv, 0);
        xo[(2 * w + 1) * 64 + lane] = pack8(xv, 1);
    }
    __syncthreads();
    ln_finish(st1, lane, sc, sh);
    norm_store(xv, sc, sh, fx, w, lane);
    __syncthreads();
    STAMP(DBGA, 5);
    {
        const int b = tile / TPB, tt = tile % TPB, bh = b * 4 + (w >> 1), t2 = w & 1;
        u32x4* qo = ws + WS_Q + ((size_t)(bh * TPB + tt) * 4 + 2 * t2) * 64 + lane;
        u32x4* ko = ws + WS_KV + ((size_t)(bh * TPB + tt) * 8 + 2 * t2) * 64 + lane;
        f32x16 a;
        GEMM16(a, 128, NFA, fx, false);
        const float qs = 0.125f * 1.4426950408889634f;
#pragma unroll
        for (int r = 0; r < 16; ++r) a[r] *= qs;
        qo[0] = pack8(a, 0); qo[64] = pack8(a, 1);
        GEMM16(a, 144, NFA, fx, false);
        ko[0] = pack8(a, 0); ko[64] = pack8(a, 1);
        GEMM16(a, 160, NFA, fx, true);
        STAMP(DBGA, 6);
        ko[4 * 64] = pack8(a, 0); ko[5 * 64] = pack8(a, 1);
    }
    STAMP(DBGA, 7); STAMPR(DBGA, 15);
}

#define WAIT_BAR(N) asm volatile("s_waitcnt vmcnt(" #N ") lgkmcnt(0)\n\ts_barrier" ::: "memory")

constexpr int AT_SLOT = 1024;
constexpr float AT_BIG = 1.0e30f;
#define ABL_NODMA 0
#define ABL_NOBAR 0
#define ABL_NOLDS 0
#define ABL_NOEXP 0
#define ABL_NOADD 0
#define PIN(x) asm volatile("" : "+v"(x))
DI float vadd(float a, float b) { float r; asm("v_add_f32 %0, %1, %2" : "=v"(r) : "v"(a), "v"(b)); return r; }
#define EX2(x) __builtin_amdgcn_exp2f(x)

DI void attn_body(const u32x4* ws_in, u32x4* ws, u32x4 (&ring)[32], u32x4* lds  ) {
    const int tid = threadIdx.x, lane = tid & 63;
    const int w = __builtin_amdgcn_readfirstlane(tid >> 6);
    STAMP((ws + (10u<<20)), 0);
    const int bh = blockIdx.x & 7, qb = blockIdx.x >> 3, qi = w & 3, g = w >> 2;
    int qt = 4 * qb + qi; const bool qvalid = qt < TPB; if (!qvalid) qt = TPB - 1;
    const u32x4* kv = ws_in + WS_KV + (size_t)bh * TPB * 8 * 64;
    const unsigned lds0 = (unsigned)(uintptr_t)lds;
    const u32x4* src = kv + (w >> 1) * 512 + (w & 1) * 128 + lane;
    const u32x4* src31 = kv + 124 * 512 + (w & 1) * 128 + lane;
    const unsigned dstk = lds0 + (w >> 1) * 4096 + (w & 1) * 2048;
    const u32x4* srcw = kv + qi * 512 + lane;
    const u32x4* srcw31 = kv + 124 * 512 + lane;
    const unsigned dstw = lds0 + qi * 4096;
#define DMA2(sp, dst) do { glds16((sp), (unsigned)__builtin_amdgcn_readfirstlane(dst)); glds16((sp) + 64, (unsigned)__builtin_amdgcn_readfirstlane((dst) + 1024)); } while (0)
#define DMA_K(step) do { const int st_ = (step); const u32x4* s_ = st_ < 31 ? src + (size_t)st_ * 2048 : src31; DMA2(s_, dstk + (st_ & 3) * 16384); } while (0)
#define DMA_V(step) do { const int st_ = (step); const u32x4* s_ = st_ < 31 ? src + (size_t)st_ * 2048 : src31; DMA2(s_ + 256, dstk + (st_ % 5) * 16384 + 65536); } while (0)
    DMA_K(0); DMA_V(0); DMA_K(1);
    u32x4 q[4];
    {
        const u32x4* qp = ws_in + WS_Q + (size_t)(bh * TPB + qt) * 4 * 64 + lane;
#pragma unroll
        for (int f = 0; f < 4; ++f) q[f] = qp[f * 64];
    }
    DMA_K(2); DMA_V(1);
    const char* lbase = (const char*)lds + (2 * g) * 4096 + lane * 16;
#define KF(off, i) (*(const u32x4*)(lbase + (off) + (i) * 1024))
#define VF(off, i) (*(const u32x4*)(lbase + (off) + 65536 + (i) * 1024))
    f32x16 o0 = (f32x16){}, o1 = (f32x16){};
    float negm;
    f32x4 lsum = (f32x4){0.f, 0.f, 0.f, 0.f};
    u32x4 onesA;
    { const unsigned o_ = ((lane & 15) == ((lane >> 4) & 1)) ? 0x3f803f80u : 0u; onesA = (u32x4){o_, o_, o_, o_}; }
    f32x16 cm, sA, sB;
    u32x4 kf[4], vf[4], pA0, pA1, pB0, pB1;
    WAIT_BAR(4);
    {
#pragma unroll
        for (int i = 0; i < 4; ++i) kf[i] = KF(0, i);
        const f32x16 z = (f32x16){};
        sA = MFMA(kf[0], q[0], z); sA = MFMA(kf[1], q[1], sA); sA = MFMA(kf[2], q[2], sA); sA = MFMA(kf[3], q[3], sA);
#pragma unroll
        for (int i = 0; i < 4; ++i) kf[i] = KF(4096, i);
        float mx = sA[0];
#pragma unroll
        for (int r = 1; r < 16; ++r) mx = fmaxf(mx, sA[r]);
        mx = fmaxf(mx, xor32(mx));
        negm = -mx;
#pragma unroll
        for (int r = 0; r < 16; ++r) { sA[r] += negm; cm[r] = negm; sB[r] = 0.f; }
        pB0 = (u32x4){0u, 0u, 0u, 0u}; pB1 = pB0;
    }
    WAIT_BAR(0);
#define FIN(S, i, PK, e) do { PK[e] = pack2(S[i], S[(i) + 1]); PIN(PK); } while (0)
#define EXP2(S, i) do { S[i] = EX2(S[i]); S[(i) + 1] = EX2(S[(i) + 1]); PIN(S); } while (0)
#define LSUM(PK) do { lsum = __builtin_amdgcn_mfma_f32_16x16x32_bf16(__builtin_bit_cast(bf16x8, onesA), __builtin_bit_cast(bf16x8, PK), lsum, 0, 0, 0); PIN(lsum); } while (0)
#define PERIOD(SC, SN, PP0, PP1, PC0, PC1, koff, voff, D0, D1) do { \
        vf[0] = VF(voff, 0); FIN(SN, 14, PP1, 3); SB(); SN = MFMA(kf[0], q[0], cm); PIN(SN); EXP2(SC, 0); SB(); \
        vf[2] = VF(voff, 2); SB(); SN = MFMA(kf[1], q[1], SN); PIN(SN); EXP2(SC, 2); FIN(SC, 0, PC0, 0); SB(); \
        vf[1] = VF(voff, 1); SB(); SN = MFMA(kf[2], q[2], SN); PIN(SN); EXP2(SC, 4); FIN(SC, 2, PC0, 1); SB(); D0; SB(); \
        vf[3] = VF(voff, 3); SB(); SN = MFMA(kf[3], q[3], SN); PIN(SN); EXP2(SC, 6); FIN(SC, 4, PC0, 2); SB(); \
        kf[0] = KF(koff, 0); SB(); o0 = MFMA(vf[0], PP0, o0); PIN(o0); EXP2(SC, 8); FIN(SC, 6, PC0, 3); SB(); LSUM(PP0); SB(); \
        kf[1] = KF(koff, 1); SB(); o1 = MFMA(vf[2], PP0, o1); PIN(o1); EXP2(SC, 10); FIN(SC, 8, PC1, 0); SB(); \
        kf[2] = KF(koff, 2); SB(); o0 = MFMA(vf[1], PP1, o0); PIN(o0); EXP2(SC, 12); FIN(SC, 10, PC1, 1); SB(); LSUM(PP1); SB(); D1; SB(); \
        kf[3] = KF(koff, 3); SB(); o1 = MFMA(vf[3], PP1, o1); PIN(o1); EXP2(SC, 14); FIN(SC, 12, PC1, 2); SB(); \
    } while (0)
    int tk0 = 0, tk1 = 0, tk2 = 0, acc_c = 0, acc_w = 0;
#define TICK(x) do {} while (0)
    int vm = 4096, v0 = 0, v1 = 16384;
#define G1(p, d, n) do { if (!ABL_NODMA && g == 0) glds16((p) + (n) * 64, (unsigned)__builtin_amdgcn_readfirstlane((d) + (n) * 1024)); } while (0)
#define NEXT5(v) ((v) >= 4 * 16384 ? 0 : (v) + 16384)
    for (int s0 = 0; s0 < 30; s0 += 2) {
        const int k1 = ((s0 + 1) & 3) * 16384, k2 = ((s0 + 2) & 3) * 16384;
        const int v2 = NEXT5(v1), v3 = NEXT5(v2);
        const int sk4 = s0 + 4 < 32 ? s0 + 4 : 31;
        const u32x4* pa_ = s0 + 3 < 31 ? srcw + (size_t)(s0 + 3) * 2048 : srcw31;
        const u32x4* pb_ = srcw + (size_t)(s0 + 2) * 2048 + 256;
        const u32x4* pc_ = sk4 < 31 ? srcw + (size_t)sk4 * 2048 : srcw31;
        const u32x4* pd_ = pa_ + 256;
        const unsigned da_ = dstw + ((s0 + 3) & 3) * 16384, db_ = dstw + 65536 + v2, dc_ = dstw + (sk4 & 3) * 16384, dd_ = dstw + 65536 + v3;
        SB();
        PERIOD(sA, sB, pB0, pB1, pA0, pA1, k1, vm, { G1(pa_, da_, 0); G1(pa_, da_, 1); G1(pa_, da_, 2); }, { G1(pa_, da_, 3); G1(pb_, db_, 0); G1(pb_, db_, 1); });
        PERIOD(sB, sA, pA0, pA1, pB0, pB1, k1 + 4096, v0, { G1(pb_, db_, 2); G1(pb_, db_, 3); G1(pc_, dc_, 0); }, { G1(pc_, dc_, 1); G1(pc_, dc_, 2); G1(pc_, dc_, 3); });
        PERIOD(sA, sB, pB0, pB1, pA0, pA1, k2, v0 + 4096, { G1(pd_, dd_, 0); G1(pd_, dd_, 1); }, { G1(pd_, dd_, 2); G1(pd_, dd_, 3); });
        PERIOD(sB, sA, pA0, pA1, pB0, pB1, k2 + 4096, v1, (void)0, (void)0);
        vm = v1 + 4096; v0 = v2; v1 = v3;
        TICK(tk1); if (!ABL_NOBAR) { WAIT_BAR(0); } TICK(tk2); acc_c += tk1 - tk0; acc_w += tk2 - tk1; tk0 = tk2;
    }
    {
        PERIOD(sA, sB, pB0, pB1, pA0, pA1, 3 * 16384, vm, (void)0, (void)0);
        PERIOD(sB, sA, pA0, pA1, pB0, pB1, 3 * 16384 + 4096, v0, (void)0, (void)0);
        if (g == 1) {
#pragma unroll
            for (int r = 0; r < 16; ++r) sA[r] = -INFINITY;
        }
        PERIOD(sA, sB, pB0, pB1, pA0, pA1, 0, v0 + 4096, (void)0, (void)0);
#pragma unroll
        for (int r = 0; r < 16; ++r) sB[r] = -INFINITY;
        PERIOD(sB, sA, pA0, pA1, pB0, pB1, 0, v1, (void)0, (void)0);
    }
    float l;
    { const float la = __shfl(lsum[0], lane & 15), lb = __shfl(lsum[1], lane & 15); l = 0.5f * ((lane & 16) ? lb : la); }
    WAIT_BAR(0);
#undef DMA_K
#undef DMA_V
#undef DMA2
#undef PERIOD
#undef FIN
#undef EXP2
#undef LSUM
#undef G1
#undef NEXT5
#undef KF
#undef VF
    {
        bool bad = !(l < AT_BIG);
#pragma unroll
        for (int r = 0; r < 16; ++r) bad = bad || !(fabsf(o0[r]) < INFINITY) || !(fabsf(o1[r]) < INFINITY);
        if (__builtin_expect(__any(bad), 0)) {
            o0 = (f32x16){}; o1 = (f32x16){}; l = 0.f; float mrun = -INFINITY;
            for (int kt = 0; kt < TPB; ++kt) {
                if (((kt >> 1) & 1) != g) continue;
                const u32x4* kg = kv + (size_t)kt * 512 + lane;
                f32x16 sc = (f32x16){};
#pragma unroll
                for (int f = 0; f < 4; ++f) sc = MFMA(kg[f * 64], q[f], sc);
                float mx = sc[0];
#pragma unroll
                for (int r = 1; r < 16; ++r) mx = fmaxf(mx, sc[r]);
                mx = fmaxf(mx, xor32(mx));
                const float mn = fmaxf(mrun, mx), f = EX2(mrun - mn);
                mrun = mn; l *= f;
#pragma unroll
                for (int r = 0; r < 16; ++r) { o0[r] *= f; o1[r] *= f; sc[r] = EX2(sc[r] - mn); l += sc[r]; }
                const u32x4 p0 = pack8(sc, 0), p1 = pack8(sc, 1);
                o0 = MFMA(kg[256], p0, o0); o1 = MFMA(kg[256 + 128], p0, o1);
                o0 = MFMA(kg[256 + 64], p1, o0); o1 = MFMA(kg[256 + 192], p1, o1);
            }
            negm = -mrun;
        }
    }
    l += xor32(l);
    float* cf = (float*)lds;
    if (g == 1) {
        float* dst = cf + qi * 34 * 64 + lane;
#pragma unroll
        for (int r = 0; r < 16; ++r) { dst[r * 64] = o0[r]; dst[(16 + r) * 64] = o1[r]; }
        dst[32 * 64] = negm; dst[33 * 64] = l;
    }
    __syncthreads();
    if (g == 0) {
        const float* s = cf + qi * 34 * 64 + lane;
        const float negm1 = s[32 * 64], l1 = s[33 * 64];
        const float nm = fminf(negm, negm1);
        const float f0 = __builtin_amdgcn_exp2f(nm - negm), f1 = __builtin_amdgcn_exp2f(nm - negm1);
        const float inv = __builtin_amdgcn_rcpf(l * f0 + l1 * f1);
        const float c0 = f0 * inv, c1 = f1 * inv;
#pragma unroll
        for (int r = 0; r < 16; ++r) { o0[r] = o0[r] * c0 + s[r * 64] * c1; o1[r] = o1[r] * c0 + s[(16 + r) * 64] * c1; }
        if (qvalid) {
            const int b = bh >> 2, hd = bh & 3;
            u32x4* ao = ws + WS_AT + ((size_t)(b * TPB + qt) * 16 + 4 * hd) * 64 + lane;
            const u32x4 r0 = pack8(o0, 0), r1 = pack8(o0, 1), r2 = pack8(o1, 0), r3 = pack8(o1, 1);
            asm volatile("global_store_dwordx4 %0, %1, off sc1\n\tglobal_store_dwordx4 %0, %2, off offset:1024 sc1\n\t"
                         "global_store_dwordx4 %0, %3, off offset:2048 sc1\n\tglobal_store_dwordx4 %0, %4, off offset:3072 sc1\n\t"
                         "s_nop 1" :: "v"(ao), "v"(r0), "v"(r1), "v"(r2), "v"(r3) : "memory");
        }
    }
    {
        const u32x4* wp = ws_in + WS_WC + (size_t)w * NFC * 64 + lane;
        LD8(0); LD8(8); LD8(16); LD8(24);
    }
    if (g == 0 && qvalid) {
        asm volatile("s_waitcnt vmcnt(32)" ::: "memory");
        if (lane == 0) __hip_atomic_fetch_add((unsigned*)(ws + WS_FL) + ((bh >> 2) * TPB + qt) * 16, 1u, __ATOMIC_RELAXED, __HIP_MEMORY_SCOPE_AGENT);
    }
    STAMPR((ws + (10u<<20)), 15);
}

#define DBGC ((u32x4*)ws + (9u<<20))
DI void stageC_body(const u32x4* ws, const uint16_t* __restrict__ dw, uint16_t* __restrict__ out, u32x4 (&ring)[32], u32x4* lds) {
    u32x4* fx = lds;
    u32x4* fy = lds + 16 * 64;
    u32x4* fh = lds + 32 * 64;
    float* st0 = (float*)(lds + 32 * 64 + 64 * 64);
    float* st1 = st0 + 512;
    float* st2 = st0 + 1024;
    u32x4* dwl = lds + 32 * 64 + 64 * 64 + 384;
    const int tile = blockIdx.x, tid = threadIdx.x, lane = tid & 63, h = lane >> 5, m = lane & 31;
    const int w = __builtin_amdgcn_readfirstlane(tid >> 6);
    STAMPR(DBGC, 14); STAMP(DBGC, 0);

#define MFMA16(a, b, c) __builtin_amdgcn_mfma_f32_16x16x32_bf16(__builtin_bit_cast(bf16x8, (a)), __builtin_bit_cast(bf16x8, (b)), (c), 0, 0, 0)
    const int tt = tile % TPB;
    const int c16 = lane & 15, g4 = lane >> 4, hm = lane & 3;
    int toff = hm < 2 ? hm - 2 : 30 + hm;
    const int tl = tt * 32 + toff;
    const bool hvalid = (tl >= 0) && (tl < T_SEQ);
    if (!hvalid) toff = 0;
    const int gtok = tile * 32 + toff;
    u32x4* hal = fy;
    const u32x4* hrd = (c16 < 8 && (c16 >> 2) == (g4 & 1)) ? hal + hm * 32 + (g4 >> 1) : hal + 128;

    u32x4 in[4]; u32x2 xh;
    {
        const u32x4* x1 = ws + WS_X1;
        const size_t mb = (size_t)tile * 16 * 64 + lane;
        in[2] = x1[mb + (2 * w) * 64]; in[3] = x1[mb + (2 * w + 1) * 64];
        xh = *(const u32x2*)((const char*)(x1 + ((size_t)(gtok >> 5) * 16 + 2 * w + ((c16 >> 2) & 1)) * 64 + (g4 & 1) * 32 + (gtok & 31)) + 8 * (g4 >> 1));
    }
    u32x4 dwr;
    if (tid < 160) dwr = ((const u32x4*)dw)[tid];
    constexpr int NFX = NFC;
    const u32x4* wp = ws + WS_WC + (size_t)w * NFC * 64 + lane;
    if (w == 0) {
        const int tn = tt + lane - 1;
        const bool need = lane < 3 && tn >= 0 && tn < TPB;
        const unsigned* fl = (const unsigned*)(ws + WS_FL) + (need ? tile + lane - 1 : tile) * 16;
        for (unsigned spin = 0; spin < (1u << 24); ++spin) {
            const unsigned v = __hip_atomic_load(fl, __ATOMIC_RELAXED, __HIP_MEMORY_SCOPE_AGENT);
            if (__all(v >= 4u)) break;
            __builtin_amdgcn_s_sleep(1);
        }
    }
    __syncthreads();
    u32x4 hin;
    {
        const __amdgpu_buffer_rsrc_t rs = __builtin_amdgcn_make_buffer_rsrc((void*)(ws + WS_AT), 0, NTILE * 16 * 1024, 0x00020000);
        const int mo = (tile * 16 * 64 + lane + 2 * w * 64) * 16;
        in[0] = __builtin_bit_cast(u32x4, __builtin_amdgcn_raw_buffer_load_b128(rs, mo, 0, 16));
        in[1] = __builtin_bit_cast(u32x4, __builtin_amdgcn_raw_buffer_load_b128(rs, mo + 1024, 0, 16));
        const int ho = (((gtok >> 5) * 16 + 2 * w + ((lane >> 3) & 1)) * 64 + ((lane >> 2) & 1) * 32 + (gtok & 31)) * 16;
        hin = __builtin_bit_cast(u32x4, __builtin_amdgcn_raw_buffer_load_b128(rs, ho, 0, 16));
    }
    f32x16 xv; f32x4 yv;
    fx[(2 * w) * 64 + lane] = in[0]; fx[(2 * w + 1) * 64 + lane] = in[1];
    if (lane < 16) hal[hm * 32 + (2 * w + ((lane >> 3) & 1)) * 2 + ((lane >> 2) & 1)] = hin;
    if (tid >= 480) hal[128 + tid - 480] = (u32x4){0u, 0u, 0u, 0u};
    if (tid < 160) dwl[tid] = dwr;
    unpack16(xv, in[2], in[3]);
    yv = (f32x4){bflo(xh[0]), bfhi(xh[0]), bflo(xh[1]), bfhi(xh[1])};
    __syncthreads();
    STAMP(DBGC, 1);
    {
        f32x16 a = (f32x16){}; f32x4 b = (f32x4){0.f, 0.f, 0.f, 0.f};
        u32x4 bx[2], by[2];
        bx[0] = fx[lane]; by[0] = hrd[0]; bx[1] = fx[64 + lane]; by[1] = hrd[2];
#pragma unroll
        for (int ks = 0; ks < 16; ++ks) {
            a = MFMA(ring[ks & 31], bx[ks & 1], a);
            b = MFMA16(ring[ks & 31], by[ks & 1], b);
            if (ks + 2 < 16) { bx[ks & 1] = fx[(ks + 2) * 64 + lane]; by[ks & 1] = hrd[2 * (ks + 2)]; }
            REFILL(ks, 0);
            SB();
        }
        STAMP(DBGC, 2);
#pragma unroll
        for (int r = 0; r < 16; ++r) xv[r] = rbf(xv[r] + rbf(a[r]));
#pragma unroll
        for (int r = 0; r < 4; ++r) yv[r] = rbf(yv[r] + rbf(b[r]));
    }
    float sc, sh;
    ln_partial(xv, st0, w, lane);
    {
        float s_ = (yv[0] + yv[1]) + (yv[2] + yv[3]), q_ = (yv[0] * yv[0] + yv[1] * yv[1]) + (yv[2] * yv[2] + yv[3] * yv[3]);
        s_ += __shfl_xor(s_, 4); q_ += __shfl_xor(q_, 4);
        s_ += __shfl_xor(s_, 16); q_ += __shfl_xor(q_, 16);
        s_ += __shfl_xor(s_, 32); q_ += __shfl_xor(q_, 32);
        if (lane < 4) { st1[w * 8 + lane] = s_; st1[w * 8 + 4 + lane] = q_; }
    }
    __syncthreads();
    ln_finish(st0, lane, sc, sh);
    norm_store(xv, sc, sh, fx, w, lane);
    {
        float s_ = 0.f, q_ = 0.f;
#pragma unroll
        for (int i = 0; i < 8; ++i) { s_ += st1[i * 8 + hm]; q_ += st1[i * 8 + 4 + hm]; }
        const float m_ = s_ * (1.0f / 256.0f);
        const float sc2 = __builtin_amdgcn_rsqf(fmaxf(q_ * (1.0f / 256.0f) - m_ * m_, 0.f) + 1e-5f), sh2 = -m_ * sc2;
        const u32x2 o_ = {pack2(__builtin_fmaf(yv[0], sc2, sh2), __builtin_fmaf(yv[1], sc2, sh2)), pack2(__builtin_fmaf(yv[2], sc2, sh2), __builtin_fmaf(yv[3], sc2, sh2))};
        if (c16 < 8) *(u32x2*)((char*)(hal + hm * 32 + (2 * w + (c16 >> 2)) * 2 + (g4 & 1)) + 8 * (g4 >> 1)) = o_;
    }
    __syncthreads();
    STAMP(DBGC, 3);
    {
        f32x16 a = (f32x16){}, b = (f32x16){}; f32x4 c = (f32x4){0.f, 0.f, 0.f, 0.f}, d = c;
        u32x4 bxn = fx[lane], byn = hrd[0];
#pragma unroll
        for (int ks = 0; ks < 16; ++ks) {
            const int F = 16 + 2 * ks;
            const u32x4 bx = bxn, by = byn;
            if (ks + 1 < 16) { bxn = fx[(ks + 1) * 64 + lane]; byn = hrd[2 * (ks + 1)]; }
            a = MFMA(ring[F & 31], bx, a); c = MFMA16(ring[F & 31], by, c); REFILL(F, 0);
            b = MFMA(ring[(F + 1) & 31], bx, b); d = MFMA16(ring[(F + 1) & 31], by, d); REFILL(F + 1, 0);
            SB();
        }
        STAMP(DBGC, 4);
#pragma unroll
        for (int r = 0; r < 16; ++r) a[r] = a[r] * sigmoidf_(b[r]);
#pragma unroll
        for (int r = 0; r < 4; ++r) c[r] = hvalid ? c[r] * sigmoidf_(d[r]) : 0.f;
        f32x4* cs = (f32x4*)fh + w * (4 * 2 * 36);
#pragma unroll
        for (int g = 0; g < 4; ++g) cs[(g * 2 + h) * 36 + m + 2] = (f32x4){a[4 * g], a[4 * g + 1], a[4 * g + 2], a[4 * g + 3]};
        if (c16 < 8) cs[((2 * (c16 >> 2) + (g4 >> 1)) * 2 + (g4 & 1)) * 36 + (hm < 2 ? hm : 32 + hm)] = c;
        __syncthreads();
        const uint16_t* dp = (const uint16_t*)dwl + 32 * w + 4 * h;
#pragma unroll
        for (int g = 0; g < 4; ++g) {
            f32x4 acc = (f32x4){0.f, 0.f, 0.f, 0.f};
#pragma unroll
            for (int i = 0; i < 5; ++i) {
                const f32x4 hv = cs[(g * 2 + h) * 36 + m + i];
                const u32x2 t = *(const u32x2*)(dp + i * DM + 8 * g);
                acc[0] += hv[0] * bflo(t[0]); acc[1] += hv[1] * bfhi(t[0]); acc[2] += hv[2] * bflo(t[1]); acc[3] += hv[3] * bfhi(t[1]);
            }
#pragma unroll
            for (int i = 0; i < 4; ++i) a[4 * g + i] = siluf_(acc[i]);
        }
        fx[(2 * w) * 64 + lane] = pack8(a, 0);
        fx[(2 * w + 1) * 64 + lane] = pack8(a, 1);
    }
    __syncthreads();
    STAMP(DBGC, 5);
    {
        f32x16 a;
        GEMM16(a, 48, 64, fx, false);
        STAMP(DBGC, 6);
#pragma unroll
        for (int r = 0; r < 16; ++r) xv[r] = rbf(xv[r] + rbf(a[r]));
    }
    ln_partial(xv, st2, w, lane);
    __syncthreads();
    ln_finish(st2, lane, sc, sh);
    norm_store(xv, sc, sh, fx, w, lane);
    __syncthreads();
    STAMP(DBGC, 7);
    {
        f32x16 acc;
        FFN(acc, 64, DBGC, 8);
#pragma unroll
        for (int r = 0; r < 16; ++r) xv[r] = rbf(__builtin_fmaf(0.5f, rbf(acc[r]), xv[r]));
    }
    ln_partial(xv, st0, w, lane);
    __syncthreads();
    ln_finish(st0, lane, sc, sh);
    {
        uint16_t* op = out + (size_t)(tile * 32 + m) * DM + 32 * w + 4 * h;
#pragma unroll
        for (int g = 0; g < 4; ++g) {
            u32x2 t;
            t[0] = pack2(__builtin_fmaf(xv[4 * g], sc, sh), __builtin_fmaf(xv[4 * g + 1], sc, sh));
            t[1] = pack2(__builtin_fmaf(xv[4 * g + 2], sc, sh), __builtin_fmaf(xv[4 * g + 3], sc, sh));
            *(u32x2*)(op + 8 * g) = t;
        }
    }
    STAMP(DBGC, 11); STAMPR(DBGC, 15);
}

__global__ __launch_bounds__(512, 2) void k_attnC(u32x4* ws, const uint16_t* __restrict__ dw, uint16_t* __restrict__ out) {
    __shared__ u32x4 lds[9 * 1024];
    u32x4 ring[32];
    attn_body(ws, ws, ring, lds);
    if (blockIdx.x >= NTILE) return;
    __syncthreads();
    stageC_body(ws, dw, out, ring, lds);
}

extern "C" void kernel_launch(void* const* d_in, const int* in_sizes, int n_in,
                              void* d_out, int out_size, void* d_ws, size_t ws_size,
                              hipStream_t stream) {
    PackArgs p;
    p.w1a = (const uint16_t*)d_in[1]; p.w1b = (const uint16_t*)d_in[2]; p.wqkv = (const uint16_t*)d_in[3]; p.wo = (const uint16_t*)d_in[4];
    p.wp1 = (const uint16_t*)d_in[5]; p.wp2 = (const uint16_t*)d_in[7]; p.w2a = (const uint16_t*)d_in[8]; p.w2b = (const uint16_t*)d_in[9];
    p.ws = (u32x4*)d_ws;
    u32x4* ws = (u32x4*)d_ws;
    const int nchunk = 8 * (NFA + NFC) * 64;
    k_pack<<<(nchunk + 255) / 256, 256, 0, stream>>>(p);
    k_stageA<<<NTILE, 512, 0, stream>>>((const uint16_t*)d_in[0], ws);
    k_attnC<<<256, 512, 0, stream>>>(ws, (const uint16_t*)d_in[6], (uint16_t*)d_out);
}
```

```cpp
#include <hip/hip_runtime.h>
#include <hip/hip_bf16.h>
#include <stdint.h>

typedef short bf16x8 __attribute__((ext_vector_type(8)));
typedef float f32x16 __attribute__((ext_vector_type(16)));
typedef float f32x2 __attribute__((ext_vector_type(2)));
typedef float f32x4 __attribute__((ext_vector_type(4)));
typedef unsigned u32x4 __attribute__((ext_vector_type(4)));
typedef unsigned u32x2 __attribute__((ext_vector_type(2)));
typedef __bf16 bf16v2 __attribute__((ext_vector_type(2)));

#define DI __device__ __forceinline__
#define SB() __builtin_amdgcn_sched_barrier(0)
#define STAMPR(base, i) do {} while (0)
#define STAMP(base, i) do {} while (0)
#define MFMA(a, b, c) __builtin_amdgcn_mfma_f32_32x32x16_bf16(__builtin_bit_cast(bf16x8, (a)), __builtin_bit_cast(bf16x8, (b)), (c), 0, 0, 0)

constexpr int T_SEQ = 4000, DM = 256, NTOK = 8000, NTILE = 250, TPB = 125;
constexpr int NFA = 176;
constexpr int NFC = 192;

constexpr size_t WS_WA = 0;
constexpr size_t WS_WC = WS_WA + (size_t)8 * NFA * 64;
constexpr size_t WS_X1 = WS_WC + (size_t)8 * NFC * 64;
constexpr size_t WS_Q  = WS_X1 + (size_t)NTILE * 16 * 64;
constexpr size_t WS_KV = WS_Q + (size_t)8 * TPB * 4 * 64;
constexpr size_t WS_AT = WS_KV + (size_t)8 * TPB * 8 * 64 + 4096;
constexpr size_t WS_FL = WS_AT + (size_t)NTILE * 16 * 64;
constexpr size_t WS_CS = WS_FL + 2048;

DI unsigned pack2(float lo, float hi) {
    f32x2 v = {lo, hi};
    bf16v2 r = __builtin_convertvector(v, bf16v2);
    return __builtin_bit_cast(unsigned, r);
}
DI float bflo(unsigned u) { return __uint_as_float(u << 16); }
DI float bfhi(unsigned u) { return __uint_as_float(u & 0xffff0000u); }
DI u32x4 pack8(const f32x16& a, int s) {
    u32x4 r;
    r[0] = pack2(a[8 * s + 0], a[8 * s + 1]);
    r[1] = pack2(a[8 * s + 2], a[8 * s + 3]);
    r[2] = pack2(a[8 * s + 4], a[8 * s + 5]);
    r[3] = pack2(a[8 * s + 6], a[8 * s + 7]);
    return r;
}
DI void unpack16(f32x16& a, const u32x4& f0, const u32x4& f1) {
#pragma unroll
    for (int i = 0; i < 4; ++i) {
        a[2 * i] = bflo(f0[i]); a[2 * i + 1] = bfhi(f0[i]);
        a[8 + 2 * i] = bflo(f1[i]); a[8 + 2 * i + 1] = bfhi(f1[i]);
    }
}
DI float xor32(float v) { return __shfl_xor(v, 32); }
DI float sigmoidf_(float x) { return __builtin_amdgcn_rcpf(1.0f + __builtin_amdgcn_exp2f(-1.4426950408889634f * x)); }
DI float siluf_(float x) { return x * sigmoidf_(x); }

struct PackArgs {
    const uint16_t* w1a; const uint16_t* w1b; const uint16_t* wqkv; const uint16_t* wo;
    const uint16_t* wp1; const uint16_t* wp2; const uint16_t* w2a; const uint16_t* w2b;
    u32x4* ws;
};

__global__ __launch_bounds__(256) void k_pack(PackArgs p) {
    const int gid = blockIdx.x * 256 + threadIdx.x;
    const int lane = gid & 63, fg = gid >> 6;
    if (gid < NTILE * 4) p.ws[WS_FL + gid] = (u32x4){0u, 0u, 0u, 0u};
    if (fg >= 8 * (NFA + NFC)) {
        const int t = gid - 8 * (NFA + NFC) * 64, col = (t >> 8) * 64 + (t & 63), part = (t >> 6) & 3;
        if (col >= 3328) return;
        const uint16_t* W; int N, c;
        if (col < 1024) { W = p.w1a; N = 1024; c = col; } else if (col < 1792) { W = p.wqkv; N = 768; c = col - 1024; }
        else if (col < 2304) { W = p.wp1; N = 512; c = col - 1792; } else { W = p.w2a; N = 1024; c = col - 2304; }
        float sum = 0.f;
#pragma unroll 16
        for (int k = 0; k < 64; ++k) sum += __uint_as_float((unsigned)W[(size_t)(part * 64 + k) * N + c] << 16);
        __shared__ float red[256];
        red[threadIdx.x] = sum;
        __syncthreads();
        if (part == 0) ((float*)(p.ws + WS_CS))[col] = (red[threadIdx.x] + red[threadIdx.x + 64]) + (red[threadIdx.x + 128] + red[threadIdx.x + 192]);
        return;
    }
    const uint16_t* W; int N, nt, ks;
    if (fg < 8 * NFA) {
        const int w = fg / NFA, f = fg % NFA;
        if (f < 64)       { W = p.w1a; N = 1024; ks = f & 15; nt = 4 * w + (f >> 4); }
        else if (f < 128) { W = p.w1b; N = 256; ks = f - 64; nt = w; }
        else              { const int g = f - 128; W = p.wqkv; N = 768; ks = g & 15; nt = w + 8 * (g >> 4); }
    } else {
        const int fg2 = fg - 8 * NFA, w = fg2 / NFC, f = fg2 % NFC;
        if (f < 16)       { W = p.wo; N = 256; ks = f; nt = w; }
        else if (f < 48)  { const int g = f - 16; W = p.wp1; N = 512; ks = g >> 1; nt = w + 8 * (g & 1); }
        else if (f < 64)  { W = p.wp2; N = 256; ks = f - 48; nt = w; }
        else if (f < 128) { const int g = f - 64; W = p.w2a; N = 1024; ks = g & 15; nt = 4 * w + (g >> 4); }
        else              { W = p.w2b; N = 256; ks = f - 128; nt = w; }
    }
    const int r = lane & 31, h = lane >> 5;
    const uint16_t* src = W + (size_t)(16 * ks + 4 * h) * N + 32 * nt + r;
    unsigned e[8];
#pragma unroll
    for (int j = 0; j < 8; ++j) e[j] = src[(size_t)(8 * (j >> 2) + (j & 3)) * N];
    u32x4 o;
#pragma unroll
    for (int i = 0; i < 4; ++i) o[i] = e[2 * i] | (e[2 * i + 1] << 16);
    p.ws[gid] = o;
}

DI void glds16(const void* g, unsigned lds_base) {
    unsigned sv;
    asm volatile("s_mov_b32 %0, m0\n\ts_mov_b32 m0, %2\n\ts_nop 0\n\tglobal_load_lds_dwordx4 %1, off\n\ts_mov_b32 m0, %0"
                 : "=&s"(sv) : "v"(g), "s"(lds_base) : "memory");
}
template <int NI> DI void l2_warm(const u32x4* base, int total_chunks, int rank, int w, int lane, unsigned lds_scratch) {
#pragma unroll
    for (int i = 0; i < NI; ++i) {
        int c = ((rank * NI + i) * 8 + w) * 64;
        c = c + 64 <= total_chunks ? c : total_chunks - 64;
        glds16(base + c + lane, lds_scratch);
    }
}
#define WLOAD(F) ring[(F) & 31] = wp[(F) * 64]
#define REFILL(F, PE) do { if ((F) < 16 && NFX == NFA) WLOAD((F) + 16); if ((F) + 32 < (NFX)) WLOAD((F) + 32); } while (0)
#define LD8(F) do { SB(); _Pragma("unroll") for (int i_ = 0; i_ < 8; ++i_) WLOAD((F) + i_); SB(); } while (0)
#define RAWBAR() do { SB(); __builtin_amdgcn_s_barrier(); SB(); } while (0)

DI void ln_partial(const f32x16& v, float* st, int w, int lane) {
    float s = 0.f, q = 0.f;
#pragma unroll
    for (int i = 0; i < 16; ++i) { s += v[i]; q = __builtin_fmaf(v[i], v[i], q); }
    auto r = __builtin_amdgcn_permlane32_swap(__float_as_uint(s), __float_as_uint(q), false, false);
    st[w * 64 + lane] = __uint_as_float(r[0]) + __uint_as_float(r[1]);
}
DI void ln_finish(const float* st, int lane, float& scale, float& shift) {
    const float* p = st + (lane & 31);
    float s = 0.f, q = 0.f;
#pragma unroll
    for (int i = 0; i < 8; ++i) { s += p[i * 64]; q += p[i * 64 + 32]; }
    const float m = s * (1.0f / 256.0f);
    const float var = fmaxf(q * (1.0f / 256.0f) - m * m, 0.f);
    scale = __builtin_amdgcn_rsqf(var + 1e-5f);
    shift = -m * scale;
}
DI void norm_store(const f32x16& v, float scale, float shift, u32x4* dst  , int w, int lane) {
    f32x16 n;
#pragma unroll
    for (int i = 0; i < 16; ++i) n[i] = __builtin_fmaf(v[i], scale, shift);
    dst[(2 * w) * 64 + lane] = pack8(n, 0);
    dst[(2 * w + 1) * 64 + lane] = pack8(n, 1);
}
DI void raw_store(const f32x16& v, u32x4* dst, int w, int lane) {
    dst[(2 * w) * 64 + lane] = pack8(v, 0);
    dst[(2 * w + 1) * 64 + lane] = pack8(v, 1);
}
DI void ln_apply(f32x16& a, float scale, float shift, const float* cs, int h) {
#pragma unroll
    for (int g = 0; g < 4; ++g) {
        const f32x4 c = *(const f32x4*)(cs + 8 * g + 4 * h);
#pragma unroll
        for (int i = 0; i < 4; ++i) a[4 * g + i] = __builtin_fmaf(a[4 * g + i], scale, shift * c[i]);
    }
}
DI float rbf(float x) { return bflo(pack2(x, 0.f) & 0xffffu); }

#define GEMM16(acc, F0, PE, fxp, SWAP) do { \
        acc = (f32x16){}; u32x4 bq_[4]; \
        _Pragma("unroll") for (int i_ = 0; i_ < 4; ++i_) bq_[i_] = (fxp)[i_ * 64 + lane]; \
        _Pragma("unroll") for (int ks_ = 0; ks_ < 16; ++ks_) { \
            if (SWAP) acc = MFMA(bq_[ks_ & 3], ring[((F0) + ks_) & 31], acc); else acc = MFMA(ring[((F0) + ks_) & 31], bq_[ks_ & 3], acc); \
            if (ks_ + 4 < 16) bq_[ks_ & 3] = (fxp)[(ks_ + 4) * 64 + lane]; \
            REFILL((F0) + ks_, PE); SB(); } } while (0)

#define FFN(acc, F0, fx, stp, csp, DBG, ST) do { \
        float sc_, sh_; \
        _Pragma("unroll") for (int t_ = 0; t_ < 4; ++t_) { \
            f32x16 a_; GEMM16(a_, (F0) + 16 * t_, (F0) + 64, fx, false); \
            if (t_ == 3) { STAMP(DBG, ST); } \
            if (t_ == 0) ln_finish(stp, lane, sc_, sh_); \
            ln_apply(a_, sc_, sh_, (csp) + (4 * w + t_) * 32, h); \
            _Pragma("unroll") for (int r_ = 0; r_ < 8; ++r_) a_[r_] = siluf_(a_[r_]); \
            fh[((4 * w + t_) * 2 + 0) * 64 + lane] = pack8(a_, 0); \
            _Pragma("unroll") for (int r_ = 8; r_ < 16; ++r_) a_[r_] = siluf_(a_[r_]); \
            fh[((4 * w + t_) * 2 + 1) * 64 + lane] = pack8(a_, 1); } \
        __syncthreads(); \
        STAMP(DBG, ST + 1); \
        acc = (f32x16){}; u32x4 bq_[4]; \
        _Pragma("unroll") for (int i_ = 0; i_ < 4; ++i_) bq_[i_] = fh[i_ * 64 + lane]; \
        _Pragma("unroll") for (int ks_ = 0; ks_ < 64; ++ks_) { \
            acc = MFMA(ring[((F0) + 64 + ks_) & 31], bq_[ks_ & 3], acc); \
            if (ks_ + 4 < 64) bq_[ks_ & 3] = fh[(ks_ + 4) * 64 + lane]; \
            REFILL((F0) + 64 + ks_, (F0) + 128); SB(); } \
        STAMP(DBG, ST + 2); } while (0)

#define DBGA (ws + (8u<<20))
__global__ __launch_bounds__(512, 2) void k_stageA(const uint16_t* __restrict__ x, u32x4* __restrict__ ws) {
    __shared__ u32x4 lds[32 * 64 + 64 * 64 + 256 + 448 + 128];
    u32x4* fx = lds;
    u32x4* fx1 = lds + 16 * 64;
    u32x4* fh = lds + 32 * 64;
    float* st0 = (float*)(lds + 32 * 64 + 64 * 64);
    float* st1 = st0 + 512;
    float* csl = (float*)(lds + 32 * 64 + 64 * 64 + 256);
    f32x2* scsh = (f32x2*)(lds + 32 * 64 + 64 * 64 + 256 + 448);
    const int tile = blockIdx.x, tid = threadIdx.x, lane = tid & 63, h = lane >> 5, m = lane & 31;
    const int w = __builtin_amdgcn_readfirstlane(tid >> 6);
    STAMPR(DBGA, 14); STAMP(DBGA, 0);
    u32x2 xr[4];
    {
        const uint16_t* xp = x + (size_t)(tile * 32 + m) * DM + 32 * w + 4 * h;
#pragma unroll
        for (int g = 0; g < 4; ++g) xr[g] = *(const u32x2*)(xp + 8 * g);
    }
    u32x4 csr;
    if (tid < 448) csr = ws[WS_CS + tid];
    RAWBAR();
    constexpr int NFX = NFA;
    const u32x4* wp = ws + WS_WA + (size_t)w * NFA * 64 + lane;
    u32x4 ring[32];
    LD8(0); LD8(8);
    f32x16 xv;
#pragma unroll
    for (int g = 0; g < 4; ++g) { xv[4 * g + 0] = bflo(xr[g][0]); xv[4 * g + 1] = bfhi(xr[g][0]); xv[4 * g + 2] = bflo(xr[g][1]); xv[4 * g + 3] = bfhi(xr[g][1]); }
    float sc, sh;
    ln_partial(xv, st0, w, lane);
    raw_store(xv, fx, w, lane);
    if (tid < 448) ((u32x4*)csl)[tid] = csr;
    __syncthreads();
    STAMP(DBGA, 1);
    {
        f32x16 acc;
        FFN(acc, 0, fx, st0, csl, DBGA, 2);
#pragma unroll
        for (int r = 0; r < 16; ++r) xv[r] = rbf(__builtin_fmaf(0.5f, rbf(acc[r]), xv[r]));
    }
    ln_partial(xv, st1, w, lane);
    {
        u32x4* xo = ws + WS_X1 + (size_t)tile * 16 * 64;
        xo[(2 * w) * 64 + lane] = pack8(xv, 0);
        xo[(2 * w + 1) * 64 + lane] = pack8(xv, 1);
    }
    raw_store(xv, fx1, w, lane);
    __syncthreads();
    STAMP(DBGA, 5);
    {
        const int b = tile / TPB, tt = tile % TPB, bh = b * 4 + (w >> 1), t2 = w & 1;
        u32x4* qo = ws + WS_Q + ((size_t)(bh * TPB + tt) * 4 + 2 * t2) * 64 + lane;
        u32x4* ko = ws + WS_KV + ((size_t)(bh * TPB + tt) * 8 + 2 * t2) * 64 + lane;
        f32x16 a;
        GEMM16(a, 128, NFA, fx1, false);
        ln_finish(st1, lane, sc, sh);
        if (lane < 32) scsh[w * 32 + lane] = (f32x2){sc, sh};
        const float qs = 0.125f * 1.4426950408889634f;
        ln_apply(a, sc * qs, sh * qs, csl + 1024 + w * 32, h);
        qo[0] = pack8(a, 0); qo[64] = pack8(a, 1);
        GEMM16(a, 144, NFA, fx1, false);
        ln_apply(a, sc, sh, csl + 1024 + 256 + w * 32, h);
        ko[0] = pack8(a, 0); ko[64] = pack8(a, 1);
        GEMM16(a, 160, NFA, fx1, true);
        STAMP(DBGA, 6);
        {
            const float cv = csl[1024 + 512 + w * 32 + m];
#pragma unroll
            for (int g = 0; g < 4; ++g) {
                const f32x4 p0 = *(const f32x4*)(scsh + w * 32 + 8 * g + 4 * h), p1 = *(const f32x4*)(scsh + w * 32 + 8 * g + 4 * h + 2);
                a[4 * g + 0] = __builtin_fmaf(a[4 * g + 0], p0[0], p0[1] * cv); a[4 * g + 1] = __builtin_fmaf(a[4 * g + 1], p0[2], p0[3] * cv);
                a[4 * g + 2] = __builtin_fmaf(a[4 * g + 2], p1[0], p1[1] * cv); a[4 * g + 3] = __builtin_fmaf(a[4 * g + 3], p1[2], p1[3] * cv);
            }
        }
        ko[4 * 64] = pack8(a, 0); ko[5 * 64] = pack8(a, 1);
    }
    STAMP(DBGA, 7); STAMPR(DBGA, 15);
}

#define WAIT_BAR(N) asm volatile("s_waitcnt vmcnt(" #N ") lgkmcnt(0)\n\ts_barrier" ::: "memory")

constexpr int AT_SLOT = 1024;
constexpr float AT_BIG = 1.0e30f;
#define ABL_NODMA 0
#define ABL_NOBAR 0
#define ABL_NOLDS 0
#define ABL_NOEXP 0
#define ABL_NOADD 0
#define PIN(x) asm volatile("" : "+v"(x))
DI float vadd(float a, float b) { float r; asm("v_add_f32 %0, %1, %2" : "=v"(r) : "v"(a), "v"(b)); return r; }
#define EX2(x) __builtin_amdgcn_exp2f(x)

DI void attn_body(const u32x4* ws_in, u32x4* ws, u32x4 (&ring)[32], u32x4* lds  ) {
    const int tid = threadIdx.x, lane = tid & 63;
    const int w = __builtin_amdgcn_readfirstlane(tid >> 6);
    STAMP((ws + (10u<<20)), 0);
    const int bh = blockIdx.x & 7, qb = blockIdx.x >> 3, qi = w & 3, g = w >> 2;
    int qt = 4 * qb + qi; const bool qvalid = qt < TPB; if (!qvalid) qt = TPB - 1;
    const u32x4* kv = ws_in + WS_KV + (size_t)bh * TPB * 8 * 64;
    const unsigned lds0 = (unsigned)(uintptr_t)lds;
    const u32x4* src = kv + (w >> 1) * 512 + (w & 1) * 128 + lane;
    const u32x4* src31 = kv + 124 * 512 + (w & 1) * 128 + lane;
    const unsigned dstk = lds0 + (w >> 1) * 4096 + (w & 1) * 2048;
    const u32x4* srcw = kv + qi * 512 + lane;
    const u32x4* srcw31 = kv + 124 * 512 + lane;
    const unsigned dstw = lds0 + qi * 4096;
#define DMA2(sp, dst) do { glds16((sp), (unsigned)__builtin_amdgcn_readfirstlane(dst)); glds16((sp) + 64, (unsigned)__builtin_amdgcn_readfirstlane((dst) + 1024)); } while (0)
#define DMA_K(step) do { const int st_ = (step); const u32x4* s_ = st_ < 31 ? src + (size_t)st_ * 2048 : src31; DMA2(s_, dstk + (st_ & 3) * 16384); } while (0)
#define DMA_V(step) do { const int st_ = (step); const u32x4* s_ = st_ < 31 ? src + (size_t)st_ * 2048 : src31; DMA2(s_ + 256, dstk + (st_ % 5) * 16384 + 65536); } while (0)
    DMA_K(0); DMA_V(0); DMA_K(1);
    u32x4 q[4];
    {
        const u32x4* qp = ws_in + WS_Q + (size_t)(bh * TPB + qt) * 4 * 64 + lane;
#pragma unroll
        for (int f = 0; f < 4; ++f) q[f] = qp[f * 64];
    }
    DMA_K(2); DMA_V(1);
    const char* lbase = (const char*)lds + (2 * g) * 4096 + lane * 16;
#define KF(off, i) (*(const u32x4*)(lbase + (off) + (i) * 1024))
#define VF(off, i) (*(const u32x4*)(lbase + (off) + 65536 + (i) * 1024))
    f32x16 o0 = (f32x16){}, o1 = (f32x16){};
    float negm;
    f32x4 lsum = (f32x4){0.f, 0.f, 0.f, 0.f};
    u32x4 onesA;
    { const unsigned o_ = ((lane & 15) == ((lane >> 4) & 1)) ? 0x3f803f80u : 0u; onesA = (u32x4){o_, o_, o_, o_}; }
    f32x16 cm, sA, sB;
    u32x4 kf[4], vf[4], pA0, pA1, pB0, pB1;
    WAIT_BAR(4);
    {
#pragma unroll
        for (int i = 0; i < 4; ++i) kf[i] = KF(0, i);
        const f32x16 z = (f32x16){};
        sA = MFMA(kf[0], q[0], z); sA = MFMA(kf[1], q[1], sA); sA = MFMA(kf[2], q[2], sA); sA = MFMA(kf[3], q[3], sA);
#pragma unroll
        for (int i = 0; i < 4; ++i) kf[i] = KF(4096, i);
        float mx = sA[0];
#pragma unroll
        for (int r = 1; r < 16; ++r) mx = fmaxf(mx, sA[r]);
        mx = fmaxf(mx, xor32(mx));
        negm = -mx;
#pragma unroll
        for (int r = 0; r < 16; ++r) { sA[r] += negm; cm[r] = negm; sB[r] = 0.f; }
        pB0 = (u32x4){0u, 0u, 0u, 0u}; pB1 = pB0;
    }
    WAIT_BAR(0);
#define FIN(S, i, PK, e) do { PK[e] = pack2(S[i], S[(i) + 1]); PIN(PK); } while (0)
#define EXP2(S, i) do { S[i] = EX2(S[i]); S[(i) + 1] = EX2(S[(i) + 1]); PIN(S); } while (0)
#define LSUM(PK) do { lsum = __builtin_amdgcn_mfma_f32_16x16x32_bf16(__builtin_bit_cast(bf16x8, onesA), __builtin_bit_cast(bf16x8, PK), lsum, 0, 0, 0); PIN(lsum); } while (0)
#define PERIOD(SC, SN, PP0, PP1, PC0, PC1, koff, voff, D0, D1) do { \
        vf[0] = VF(voff, 0); FIN(SN, 14, PP1, 3); SB(); SN = MFMA(kf[0], q[0], cm); PIN(SN); EXP2(SC, 0); SB(); \
        vf[2] = VF(voff, 2); SB(); SN = MFMA(kf[1], q[1], SN); PIN(SN); EXP2(SC, 2); FIN(SC, 0, PC0, 0); SB(); \
        vf[1] = VF(voff, 1); SB(); SN = MFMA(kf[2], q[2], SN); PIN(SN); EXP2(SC, 4); FIN(SC, 2, PC0, 1); SB(); D0; SB(); \
        vf[3] = VF(voff, 3); SB(); SN = MFMA(kf[3], q[3], SN); PIN(SN); EXP2(SC, 6); FIN(SC, 4, PC0, 2); SB(); \
        kf[0] = KF(koff, 0); SB(); o0 = MFMA(vf[0], PP0, o0); PIN(o0); EXP2(SC, 8); FIN(SC, 6, PC0, 3); SB(); LSUM(PP0); SB(); \
        kf[1] = KF(koff, 1); SB(); o1 = MFMA(vf[2], PP0, o1); PIN(o1); EXP2(SC, 10); FIN(SC, 8, PC1, 0); SB(); \
        kf[2] = KF(koff, 2); SB(); o0 = MFMA(vf[1], PP1, o0); PIN(o0); EXP2(SC, 12); FIN(SC, 10, PC1, 1); SB(); LSUM(PP1); SB(); D1; SB(); \
        kf[3] = KF(koff, 3); SB(); o1 = MFMA(vf[3], PP1, o1); PIN(o1); EXP2(SC, 14); FIN(SC, 12, PC1, 2); SB(); \
    } while (0)
    int tk0 = 0, tk1 = 0, tk2 = 0, acc_c = 0, acc_w = 0;
#define TICK(x) do {} while (0)
    int vm = 4096, v0 = 0, v1 = 16384;
#define G1(p, d, n) do { if (!ABL_NODMA && g == 0) glds16((p) + (n) * 64, (unsigned)__builtin_amdgcn_readfirstlane((d) + (n) * 1024)); } while (0)
#define NEXT5(v) ((v) >= 4 * 16384 ? 0 : (v) + 16384)
    for (int s0 = 0; s0 < 30; s0 += 2) {
        const int k1 = ((s0 + 1) & 3) * 16384, k2 = ((s0 + 2) & 3) * 16384;
        const int v2 = NEXT5(v1), v3 = NEXT5(v2);
        const int sk4 = s0 + 4 < 32 ? s0 + 4 : 31;
        const u32x4* pa_ = s0 + 3 < 31 ? srcw + (size_t)(s0 + 3) * 2048 : srcw31;
        const u32x4* pb_ = srcw + (size_t)(s0 + 2) * 2048 + 256;
        const u32x4* pc_ = sk4 < 31 ? srcw + (size_t)sk4 * 2048 : srcw31;
        const u32x4* pd_ = pa_ + 256;
        const unsigned da_ = dstw + ((s0 + 3) & 3) * 16384, db_ = dstw + 65536 + v2, dc_ = dstw + (sk4 & 3) * 16384, dd_ = dstw + 65536 + v3;
        SB();
        PERIOD(sA, sB, pB0, pB1, pA0, pA1, k1, vm, { G1(pa_, da_, 0); G1(pa_, da_, 1); G1(pa_, da_, 2); }, { G1(pa_, da_, 3); G1(pb_, db_, 0); G1(pb_, db_, 1); });
        PERIOD(sB, sA, pA0, pA1, pB0, pB1, k1 + 4096, v0, { G1(pb_, db_, 2); G1(pb_, db_, 3); G1(pc_, dc_, 0); }, { G1(pc_, dc_, 1); G1(pc_, dc_, 2); G1(pc_, dc_, 3); });
        PERIOD(sA, sB, pB0, pB1, pA0, pA1, k2, v0 + 4096, { G1(pd_, dd_, 0); G1(pd_, dd_, 1); }, { G1(pd_, dd_, 2); G1(pd_, dd_, 3); });
        PERIOD(sB, sA, pA0, pA1, pB0, pB1, k2 + 4096, v1, (void)0, (void)0);
        vm = v1 + 4096; v0 = v2; v1 = v3;
        TICK(tk1); if (!ABL_NOBAR) { WAIT_BAR(0); } TICK(tk2); acc_c += tk1 - tk0; acc_w += tk2 - tk1; tk0 = tk2;
    }
    {
        PERIOD(sA, sB, pB0, pB1, pA0, pA1, 3 * 16384, vm, (void)0, (void)0);
        PERIOD(sB, sA, pA0, pA1, pB0, pB1, 3 * 16384 + 4096, v0, (void)0, (void)0);
        if (g == 1) {
#pragma unroll
            for (int r = 0; r < 16; ++r) sA[r] = -INFINITY;
        }
        PERIOD(sA, sB, pB0, pB1, pA0, pA1, 0, v0 + 4096, (void)0, (void)0);
#pragma unroll
        for (int r = 0; r < 16; ++r) sB[r] = -INFINITY;
        PERIOD(sB, sA, pA0, pA1, pB0, pB1, 0, v1, (void)0, (void)0);
    }
    float l;
    { const float la = __shfl(lsum[0], lane & 15), lb = __shfl(lsum[1], lane & 15); l = 0.5f * ((lane & 16) ? lb : la); }
    WAIT_BAR(0);
#undef DMA_K
#undef DMA_V
#undef DMA2
#undef PERIOD
#undef FIN
#undef EXP2
#undef LSUM
#undef G1
#undef NEXT5
#undef KF
#undef VF
    {
        bool bad = !(l < AT_BIG);
#pragma unroll
        for (int r = 0; r < 16; ++r) bad = bad || !(fabsf(o0[r]) < INFINITY) || !(fabsf(o1[r]) < INFINITY);
        if (__builtin_expect(__any(bad), 0)) {
            o0 = (f32x16){}; o1 = (f32x16){}; l = 0.f; float mrun = -INFINITY;
            for (int kt = 0; kt < TPB; ++kt) {
                if (((kt >> 1) & 1) != g) continue;
                const u32x4* kg = kv + (size_t)kt * 512 + lane;
                f32x16 sc = (f32x16){};
#pragma unroll
                for (int f = 0; f < 4; ++f) sc = MFMA(kg[f * 64], q[f], sc);
                float mx = sc[0];
#pragma unroll
                for (int r = 1; r < 16; ++r) mx = fmaxf(mx, sc[r]);
                mx = fmaxf(mx, xor32(mx));
                const float mn = fmaxf(mrun, mx), f = EX2(mrun - mn);
                mrun = mn; l *= f;
#pragma unroll
                for (int r = 0; r < 16; ++r) { o0[r] *= f; o1[r] *= f; sc[r] = EX2(sc[r] - mn); l += sc[r]; }
                const u32x4 p0 = pack8(sc, 0), p1 = pack8(sc, 1);
                o0 = MFMA(kg[256], p0, o0); o1 = MFMA(kg[256 + 128], p0, o1);
                o0 = MFMA(kg[256 + 64], p1, o0); o1 = MFMA(kg[256 + 192], p1, o1);
            }
            negm = -mrun;
        }
    }
    l += xor32(l);
    float* cf = (float*)lds;
    if (g == 1) {
        float* dst = cf + qi * 34 * 64 + lane;
#pragma unroll
        for (int r = 0; r < 16; ++r) { dst[r * 64] = o0[r]; dst[(16 + r) * 64] = o1[r]; }
        dst[32 * 64] = negm; dst[33 * 64] = l;
    }
    __syncthreads();
    if (g == 0) {
        const float* s = cf + qi * 34 * 64 + lane;
        const float negm1 = s[32 * 64], l1 = s[33 * 64];
        const float nm = fminf(negm, negm1);
        const float f0 = __builtin_amdgcn_exp2f(nm - negm), f1 = __builtin_amdgcn_exp2f(nm - negm1);
        const float inv = __builtin_amdgcn_rcpf(l * f0 + l1 * f1);
        const float c0 = f0 * inv, c1 = f1 * inv;
#pragma unroll
        for (int r = 0; r < 16; ++r) { o0[r] = o0[r] * c0 + s[r * 64] * c1; o1[r] = o1[r] * c0 + s[(16 + r) * 64] * c1; }
        if (qvalid) {
            const int b = bh >> 2, hd = bh & 3;
            u32x4* ao = ws + WS_AT + ((size_t)(b * TPB + qt) * 16 + 4 * hd) * 64 + lane;
            const u32x4 r0 = pack8(o0, 0), r1 = pack8(o0, 1), r2 = pack8(o1, 0), r3 = pack8(o1, 1);
            asm volatile("global_store_dwordx4 %0, %1, off sc1\n\tglobal_store_dwordx4 %0, %2, off offset:1024 sc1\n\t"
                         "global_store_dwordx4 %0, %3, off offset:2048 sc1\n\tglobal_store_dwordx4 %0, %4, off offset:3072 sc1\n\t"
                         "s_nop 1" :: "v"(ao), "v"(r0), "v"(r1), "v"(r2), "v"(r3) : "memory");
        }
    }
    {
        const u32x4* wp = ws_in + WS_WC + (size_t)w * NFC * 64 + lane;
        LD8(0); LD8(8); LD8(16); LD8(24);
    }
    if (g == 0 && qvalid) {
        asm volatile("s_waitcnt vmcnt(32)" ::: "memory");
        if (lane == 0) __hip_atomic_fetch_add((unsigned*)(ws + WS_FL) + ((bh >> 2) * TPB + qt) * 16, 1u, __ATOMIC_RELAXED, __HIP_MEMORY_SCOPE_AGENT);
    }
    STAMPR((ws + (10u<<20)), 15);
}

#define DBGC ((u32x4*)ws + (9u<<20))
DI void stageC_body(const u32x4* ws, const uint16_t* __restrict__ dw, uint16_t* __restrict__ out, u32x4 (&ring)[32], u32x4* lds) {
    u32x4* fx = lds;
    u32x4* fx1 = lds + 16 * 64;
    u32x4* fh = lds + 32 * 64;
    float* st0 = (float*)(lds + 32 * 64 + 64 * 64);
    float* st1 = st0 + 512;
    float* st2 = st0 + 1024;
    u32x4* dwl = lds + 32 * 64 + 64 * 64 + 384;
    float* csl = (float*)(lds + 32 * 64 + 64 * 64 + 384 + 160);
    u32x4* hal = lds + 32 * 64 + 64 * 64 + 384 + 160 + 384;
    const int tile = blockIdx.x, tid = threadIdx.x, lane = tid & 63, h = lane >> 5, m = lane & 31;
    const int w = __builtin_amdgcn_readfirstlane(tid >> 6);
    STAMPR(DBGC, 14); STAMP(DBGC, 0);

#define MFMA16(a, b, c) __builtin_amdgcn_mfma_f32_16x16x32_bf16(__builtin_bit_cast(bf16x8, (a)), __builtin_bit_cast(bf16x8, (b)), (c), 0, 0, 0)
    const int tt = tile % TPB;
    const int c16 = lane & 15, g4 = lane >> 4, hm = lane & 3;
    int toff = hm < 2 ? hm - 2 : 30 + hm;
    const int tl = tt * 32 + toff;
    const bool hvalid = (tl >= 0) && (tl < T_SEQ);
    if (!hvalid) toff = 0;
    const int gtok = tile * 32 + toff;
    const bool hact = c16 < 8 && (c16 >> 2) == (g4 & 1);
    const u32x4* hrd = hact ? hal + hm * 32 + (g4 >> 1) : hal + 256;
    const u32x4* hrd1 = hact ? hal + 128 + hm * 32 + (g4 >> 1) : hal + 256;

    u32x4 in[4]; u32x2 xh;
    {
        const u32x4* x1 = ws + WS_X1;
        const size_t mb = (size_t)tile * 16 * 64 + lane;
        in[2] = x1[mb + (2 * w) * 64]; in[3] = x1[mb + (2 * w + 1) * 64];
        xh = *(const u32x2*)((const char*)(x1 + ((size_t)(gtok >> 5) * 16 + 2 * w + ((c16 >> 2) & 1)) * 64 + (g4 & 1) * 32 + (gtok & 31)) + 8 * (g4 >> 1));
    }
    if (tid >= 64 && tid < 224) dwl[tid - 64] = ((const u32x4*)dw)[tid - 64];
    if (tid >= 128) ((u32x4*)csl)[tid - 128] = ws[WS_CS + 448 + tid - 128];
    constexpr int NFX = NFC;
    const u32x4* wp = ws + WS_WC + (size_t)w * NFC * 64 + lane;
    if (w == 0) {
        const int tn = tt + lane - 1;
        const bool need = lane < 3 && tn >= 0 && tn < TPB;
        const unsigned* fl = (const unsigned*)(ws + WS_FL) + (need ? tile + lane - 1 : tile) * 16;
        for (unsigned spin = 0; spin < (1u << 24); ++spin) {
            const unsigned v = __hip_atomic_load(fl, __ATOMIC_RELAXED, __HIP_MEMORY_SCOPE_AGENT);
            if (__all(v >= 4u)) break;
            __builtin_amdgcn_s_sleep(1);
        }
    }
    __syncthreads();
    u32x4 hin;
    {
        const __amdgpu_buffer_rsrc_t rs = __builtin_amdgcn_make_buffer_rsrc((void*)(ws + WS_AT), 0, NTILE * 16 * 1024, 0x00020000);
        const int mo = (tile * 16 * 64 + lane + 2 * w * 64) * 16;
        in[0] = __builtin_bit_cast(u32x4, __builtin_amdgcn_raw_buffer_load_b128(rs, mo, 0, 16));
        in[1] = __builtin_bit_cast(u32x4, __builtin_amdgcn_raw_buffer_load_b128(rs, mo + 1024, 0, 16));
        const int ho = (((gtok >> 5) * 16 + 2 * w + ((lane >> 3) & 1)) * 64 + ((lane >> 2) & 1) * 32 + (gtok & 31)) * 16;
        hin = __builtin_bit_cast(u32x4, __builtin_amdgcn_raw_buffer_load_b128(rs, ho, 0, 16));
    }
    f32x16 xv; f32x4 yv;
    fx[(2 * w) * 64 + lane] = in[0]; fx[(2 * w + 1) * 64 + lane] = in[1];
    if (lane < 16) hal[hm * 32 + (2 * w + ((lane >> 3) & 1)) * 2 + ((lane >> 2) & 1)] = hin;
    if (tid >= 480) hal[256 + tid - 480] = (u32x4){0u, 0u, 0u, 0u};

    unpack16(xv, in[2], in[3]);
    yv = (f32x4){bflo(xh[0]), bfhi(xh[0]), bflo(xh[1]), bfhi(xh[1])};
    __syncthreads();
    STAMP(DBGC, 1);
    {
        f32x16 a = (f32x16){}; f32x4 b = (f32x4){0.f, 0.f, 0.f, 0.f};
        u32x4 bx[2], by[2];
        bx[0] = fx[lane]; by[0] = hrd[0]; bx[1] = fx[64 + lane]; by[1] = hrd[2];
#pragma unroll
        for (int ks = 0; ks < 16; ++ks) {
            a = MFMA(ring[ks & 31], bx[ks & 1], a);
            b = MFMA16(ring[ks & 31], by[ks & 1], b);
            if (ks + 2 < 16) { bx[ks & 1] = fx[(ks + 2) * 64 + lane]; by[ks & 1] = hrd[2 * (ks + 2)]; }
            REFILL(ks, 0);
            SB();
        }
        STAMP(DBGC, 2);
#pragma unroll
        for (int r = 0; r < 16; ++r) xv[r] = rbf(xv[r] + rbf(a[r]));
#pragma unroll
        for (int r = 0; r < 4; ++r) yv[r] = rbf(yv[r] + rbf(b[r]));
    }
    float sc, sh;
    ln_partial(xv, st0, w, lane);
    {
        float s_ = (yv[0] + yv[1]) + (yv[2] + yv[3]), q_ = (yv[0] * yv[0] + yv[1] * yv[1]) + (yv[2] * yv[2] + yv[3] * yv[3]);
        s_ += __shfl_xor(s_, 4); q_ += __shfl_xor(q_, 4);
        s_ += __shfl_xor(s_, 16); q_ += __shfl_xor(q_, 16);
        s_ += __shfl_xor(s_, 32); q_ += __shfl_xor(q_, 32);
        if (lane < 4) { st1[w * 8 + lane] = s_; st1[w * 8 + 4 + lane] = q_; }
    }
    raw_store(xv, fx1, w, lane);
    {
        const u32x2 o_ = {pack2(yv[0], yv[1]), pack2(yv[2], yv[3])};
        if (c16 < 8) *(u32x2*)((char*)(hal + 128 + hm * 32 + (2 * w + (c16 >> 2)) * 2 + (g4 & 1)) + 8 * (g4 >> 1)) = o_;
    }
    __syncthreads();
    STAMP(DBGC, 3);
    {
        f32x16 a = (f32x16){}, b = (f32x16){}; f32x4 c = (f32x4){0.f, 0.f, 0.f, 0.f}, d = c;
        u32x4 bxn = fx1[lane], byn = hrd1[0];
#pragma unroll
        for (int ks = 0; ks < 16; ++ks) {
            const int F = 16 + 2 * ks;
            const u32x4 bx = bxn, by = byn;
            if (ks + 1 < 16) { bxn = fx1[(ks + 1) * 64 + lane]; byn = hrd1[2 * (ks + 1)]; }
            a = MFMA(ring[F & 31], bx, a); c = MFMA16(ring[F & 31], by, c); if (F < 40) REFILL(F, 0);
            b = MFMA(ring[(F + 1) & 31], bx, b); d = MFMA16(ring[(F + 1) & 31], by, d); if (F < 40) REFILL(F + 1, 0);
            SB();
        }
        STAMP(DBGC, 4);
        ln_finish(st0, lane, sc, sh);
        ln_apply(a, sc, sh, csl + w * 32, h);
        ln_apply(b, sc, sh, csl + 256 + w * 32, h);
        {
            float s_ = 0.f, q_ = 0.f;
#pragma unroll
            for (int i = 0; i < 8; ++i) { s_ += st1[i * 8 + hm]; q_ += st1[i * 8 + 4 + hm]; }
            const float m_ = s_ * (1.0f / 256.0f);
            const float sc2 = __builtin_amdgcn_rsqf(fmaxf(q_ * (1.0f / 256.0f) - m_ * m_, 0.f) + 1e-5f), sh2 = -m_ * sc2;
            const f32x4 ca = *(const f32x4*)(csl + w * 32 + 16 * ((c16 >> 2) & 1) + 4 * g4), cb = *(const f32x4*)(csl + 256 + w * 32 + 16 * ((c16 >> 2) & 1) + 4 * g4);
#pragma unroll
            for (int r = 0; r < 4; ++r) { c[r] = __builtin_fmaf(c[r], sc2, sh2 * ca[r]); d[r] = __builtin_fmaf(d[r], sc2, sh2 * cb[r]); }
        }
#pragma unroll
        for (int r = 0; r < 16; ++r) a[r] = a[r] * sigmoidf_(b[r]);
#pragma unroll
        for (int r = 0; r < 4; ++r) c[r] = hvalid ? c[r] * sigmoidf_(d[r]) : 0.f;
        LD8(72);
        f32x4* cs = (f32x4*)fh + w * (4 * 2 * 36);
#pragma unroll
        for (int g = 0; g < 4; ++g) cs[(g * 2 + h) * 36 + m + 2] = (f32x4){a[4 * g], a[4 * g + 1], a[4 * g + 2], a[4 * g + 3]};
        if (c16 < 8) cs[((2 * (c16 >> 2) + (g4 >> 1)) * 2 + (g4 & 1)) * 36 + (hm < 2 ? hm : 32 + hm)] = c;
        __syncthreads();
        const uint16_t* dp = (const uint16_t*)dwl + 32 * w + 4 * h;
#pragma unroll
        for (int g = 0; g < 4; ++g) {
            f32x4 acc = (f32x4){0.f, 0.f, 0.f, 0.f};
#pragma unroll
            for (int i = 0; i < 5; ++i) {
                const f32x4 hv = cs[(g * 2 + h) * 36 + m + i];
                const u32x2 t = *(const u32x2*)(dp + i * DM + 8 * g);
                acc[0] += hv[0] * bflo(t[0]); acc[1] += hv[1] * bfhi(t[0]); acc[2] += hv[2] * bflo(t[1]); acc[3] += hv[3] * bfhi(t[1]);
            }
#pragma unroll
            for (int i = 0; i < 4; ++i) a[4 * g + i] = siluf_(acc[i]);
        }
        fx[(2 * w) * 64 + lane] = pack8(a, 0);
        fx[(2 * w + 1) * 64 + lane] = pack8(a, 1);
    }
    __syncthreads();
    STAMP(DBGC, 5);
    {
        f32x16 a;
        GEMM16(a, 48, 64, fx, false);
        STAMP(DBGC, 6);
#pragma unroll
        for (int r = 0; r < 16; ++r) xv[r] = rbf(xv[r] + rbf(a[r]));
    }
    ln_partial(xv, st2, w, lane);
    raw_store(xv, fx1, w, lane);
    __syncthreads();
    STAMP(DBGC, 7);
    {
        f32x16 acc;
        FFN(acc, 64, fx1, st2, csl + 512, DBGC, 8);
#pragma unroll
        for (int r = 0; r < 16; ++r) xv[r] = rbf(__builtin_fmaf(0.5f, rbf(acc[r]), xv[r]));
    }
    ln_partial(xv, st0, w, lane);
    __syncthreads();
    ln_finish(st0, lane, sc, sh);
    {
        uint16_t* op = out + (size_t)(tile * 32 + m) * DM + 32 * w + 4 * h;
#pragma unroll
        for (int g = 0; g < 4; ++g) {
            u32x2 t;
            t[0] = pack2(__builtin_fmaf(xv[4 * g], sc, sh), __builtin_fmaf(xv[4 * g + 1], sc, sh));
            t[1] = pack2(__builtin_fmaf(xv[4 * g + 2], sc, sh), __builtin_fmaf(xv[4 * g + 3], sc, sh));
            *(u32x2*)(op + 8 * g) = t;
        }
    }
    STAMP(DBGC, 11); STAMPR(DBGC, 15);
}

__global__ __launch_bounds__(512, 2) void k_attnC(u32x4* ws, const uint16_t* __restrict__ dw, uint16_t* __restrict__ out) {
    __shared__ u32x4 lds[9 * 1024];
    u32x4 ring[32];
    attn_body(ws, ws, ring, lds);
    if (blockIdx.x >= NTILE) return;
    __syncthreads();
    stageC_body(ws, dw, out, ring, lds);
}

extern "C" void kernel_launch(void* const* d_in, const int* in_sizes, int n_in,
                              void* d_out, int out_size, void* d_ws, size_t ws_size,
                              hipStream_t stream) {
    PackArgs p;
    p.w1a = (const uint16_t*)d_in[1]; p.w1b = (const uint16_t*)d_in[2]; p.wqkv = (const uint16_t*)d_in[3]; p.wo = (const uint16_t*)d_in[4];
    p.wp1 = (const uint16_t*)d_in[5]; p.wp2 = (const uint16_t*)d_in[7]; p.w2a = (const uint16_t*)d_in[8]; p.w2b = (const uint16_t*)d_in[9];
    p.ws = (u32x4*)d_ws;
    u32x4* ws = (u32x4*)d_ws;
    const int nchunk = 8 * (NFA + NFC) * 64;
    k_pack<<<nchunk / 256 + 52, 256, 0, stream>>>(p);
    k_stageA<<<NTILE, 512, 0, stream>>>((const uint16_t*)d_in[0], ws);
    k_attnC<<<256, 512, 0, stream>>>(ws, (const uint16_t*)d_in[6], (uint16_t*)d_out);
}
```

```cpp
#include <hip/hip_runtime.h>
#include <hip/hip_bf16.h>
#include <stdint.h>


typedef short bf16x8 __attribute__((ext_vector_type(8)));
typedef float f32x16 __attribute__((ext_vector_type(16)));
typedef float f32x2 __attribute__((ext_vector_type(2)));
typedef float f32x4 __attribute__((ext_vector_type(4)));
typedef unsigned u32x4 __attribute__((ext_vector_type(4)));
typedef unsigned u32x2 __attribute__((ext_vector_type(2)));
typedef __bf16 bf16v2 __attribute__((ext_vector_type(2)));

#define DI __device__ __forceinline__
#define SB() __builtin_amdgcn_sched_barrier(0)
#define MFMA(a, b, c) __builtin_amdgcn_mfma_f32_32x32x16_bf16(__builtin_bit_cast(bf16x8, (a)), __builtin_bit_cast(bf16x8, (b)), (c), 0, 0, 0)

constexpr int T_SEQ = 4000, DM = 256, NTOK = 8000, NTILE = 250, TPB = 125;
constexpr int NFA = 176;
constexpr int NFC = 192;

constexpr size_t WS_WA = 0;
constexpr size_t WS_WC = WS_WA + (size_t)8 * NFA * 64;
constexpr size_t WS_X1 = WS_WC + (size_t)8 * NFC * 64;
constexpr size_t WS_Q  = WS_X1 + (size_t)NTILE * 16 * 64;
constexpr size_t WS_KV = WS_Q + (size_t)8 * TPB * 4 * 64;
constexpr size_t WS_AT = WS_KV + (size_t)8 * TPB * 8 * 64 + 4096;
constexpr size_t WS_FL = WS_AT + (size_t)NTILE * 16 * 64;

DI unsigned pack2(float lo, float hi) {
    f32x2 v = {lo, hi};
    bf16v2 r = __builtin_convertvector(v, bf16v2);
    return __builtin_bit_cast(unsigned, r);
}
DI float bflo(unsigned u) { return __uint_as_float(u << 16); }
DI float bfhi(unsigned u) { return __uint_as_float(u & 0xffff0000u); }
DI u32x4 pack8(const f32x16& a, int s) {
    u32x4 r;
    r[0] = pack2(a[8 * s + 0], a[8 * s + 1]);
    r[1] = pack2(a[8 * s + 2], a[8 * s + 3]);
    r[2] = pack2(a[8 * s + 4], a[8 * s + 5]);
    r[3] = pack2(a[8 * s + 6], a[8 * s + 7]);
    return r;
}
DI void unpack16(f32x16& a, const u32x4& f0, const u32x4& f1) {
#pragma unroll
    for (int i = 0; i < 4; ++i) {
        a[2 * i] = bflo(f0[i]); a[2 * i + 1] = bfhi(f0[i]);
        a[8 + 2 * i] = bflo(f1[i]); a[8 + 2 * i + 1] = bfhi(f1[i]);
    }
}
DI float xor32(float v) { return __shfl_xor(v, 32); }
DI float sigmoidf_(float x) { return __builtin_amdgcn_rcpf(1.0f + __builtin_amdgcn_exp2f(-1.4426950408889634f * x)); }
DI float siluf_(float x) { return x * sigmoidf_(x); }

struct PackArgs {
    const uint16_t* w1a; const uint16_t* w1b; const uint16_t* wqkv; const uint16_t* wo;
    const uint16_t* wp1; const uint16_t* wp2; const uint16_t* w2a; const uint16_t* w2b;
    u32x4* ws;
};

__global__ __launch_bounds__(256) void k_pack(PackArgs p) {
    const int tid2 = blockIdx.x * 256 + threadIdx.x;
    const int gid = 2 * tid2;
    const int lane = gid & 63, fg = gid >> 6;
    if (tid2 < NTILE * 4) p.ws[WS_FL + tid2] = (u32x4){0u, 0u, 0u, 0u};
    if (fg >= 8 * (NFA + NFC)) return;
    const uint16_t* W; int N, nt, ks;
    if (fg < 8 * NFA) {
        const int w = fg / NFA, f = fg % NFA;
        if (f < 64)       { W = p.w1a; N = 1024; ks = f & 15; nt = 4 * w + (f >> 4); }
        else if (f < 128) { W = p.w1b; N = 256; ks = f - 64; nt = w; }
        else              { const int g = f - 128; W = p.wqkv; N = 768; ks = g & 15; nt = w + 8 * (g >> 4); }
    } else {
        const int fg2 = fg - 8 * NFA, w = fg2 / NFC, f = fg2 % NFC;
        if (f < 16)       { W = p.wo; N = 256; ks = f; nt = w; }
        else if (f < 48)  { const int g = f - 16; W = p.wp1; N = 512; ks = g >> 1; nt = w + 8 * (g & 1); }
        else if (f < 64)  { W = p.wp2; N = 256; ks = f - 48; nt = w; }
        else if (f < 128) { const int g = f - 64; W = p.w2a; N = 1024; ks = g & 15; nt = 4 * w + (g >> 4); }
        else              { W = p.w2b; N = 256; ks = f - 128; nt = w; }
    }
    const int r = lane & 31, h = lane >> 5;
    const uint16_t* src = W + (size_t)(16 * ks + 4 * h) * N + 32 * nt + r;
    unsigned e[8];
#pragma unroll
    for (int j = 0; j < 8; ++j) e[j] = *(const unsigned*)(src + (size_t)(8 * (j >> 2) + (j & 3)) * N);
    u32x4 o0, o1;
#pragma unroll
    for (int i = 0; i < 4; ++i) { o0[i] = (e[2 * i] & 0xffffu) | (e[2 * i + 1] << 16); o1[i] = (e[2 * i] >> 16) | (e[2 * i + 1] & 0xffff0000u); }
    p.ws[gid] = o0; p.ws[gid + 1] = o1;
}

DI void glds16(const void* g, unsigned lds_base) {
    unsigned sv;
    asm volatile("s_mov_b32 %0, m0\n\ts_mov_b32 m0, %2\n\ts_nop 0\n\tglobal_load_lds_dwordx4 %1, off\n\ts_mov_b32 m0, %0"
                 : "=&s"(sv) : "v"(g), "s"(lds_base) : "memory");
}
#define WLOAD(F) ring[(F) & 31] = wp[(F) * 64]
#define REFILL(F, PE) do { if ((F) < 16 && NFX == NFA) WLOAD((F) + 16); if ((F) + 32 < (NFX)) WLOAD((F) + 32); } while (0)
#define LD8(F) do { SB(); _Pragma("unroll") for (int i_ = 0; i_ < 8; ++i_) WLOAD((F) + i_); SB(); } while (0)
#define RAWBAR() do { SB(); __builtin_amdgcn_s_barrier(); SB(); } while (0)

DI void ln_partial(const f32x16& v, float* st, int w, int lane) {
    float s = 0.f, q = 0.f;
#pragma unroll
    for (int i = 0; i < 16; ++i) { s += v[i]; q = __builtin_fmaf(v[i], v[i], q); }
    auto r = __builtin_amdgcn_permlane32_swap(__float_as_uint(s), __float_as_uint(q), false, false);
    st[w * 64 + lane] = __uint_as_float(r[0]) + __uint_as_float(r[1]);
}
DI void ln_finish(const float* st, int lane, float& scale, float& shift) {
    const float* p = st + (lane & 31);
    float s = 0.f, q = 0.f;
#pragma unroll
    for (int i = 0; i < 8; ++i) { s += p[i * 64]; q += p[i * 64 + 32]; }
    const float m = s * (1.0f / 256.0f);
    const float var = fmaxf(q * (1.0f / 256.0f) - m * m, 0.f);
    scale = __builtin_amdgcn_rsqf(var + 1e-5f);
    shift = -m * scale;
}
DI void norm_store(const f32x16& v, float scale, float shift, u32x4* dst  , int w, int lane) {
    f32x16 n;
#pragma unroll
    for (int i = 0; i < 16; ++i) n[i] = __builtin_fmaf(v[i], scale, shift);
    dst[(2 * w) * 64 + lane] = pack8(n, 0);
    dst[(2 * w + 1) * 64 + lane] = pack8(n, 1);
}
DI float rbf(float x) { return bflo(pack2(x, 0.f) & 0xffffu); }

#define GEMM16(acc, F0, PE, fxp, SWAP) do { \
        acc = (f32x16){}; u32x4 bq_[4]; \
        _Pragma("unroll") for (int i_ = 0; i_ < 4; ++i_) bq_[i_] = (fxp)[i_ * 64 + lane]; \
        _Pragma("unroll") for (int ks_ = 0; ks_ < 16; ++ks_) { \
            if (SWAP) acc = MFMA(bq_[ks_ & 3], ring[((F0) + ks_) & 31], acc); else acc = MFMA(ring[((F0) + ks_) & 31], bq_[ks_ & 3], acc); \
            if (ks_ + 4 < 16) bq_[ks_ & 3] = (fxp)[(ks_ + 4) * 64 + lane]; \
            REFILL((F0) + ks_, PE); SB(); } } while (0)

#define FFN(acc, F0) do { \
        _Pragma("unroll") for (int t_ = 0; t_ < 4; ++t_) { \
            f32x16 a_; GEMM16(a_, (F0) + 16 * t_, (F0) + 64, fx, false); \
            _Pragma("unroll") for (int r_ = 0; r_ < 8; ++r_) a_[r_] = siluf_(a_[r_]); \
            fh[((4 * w + t_) * 2 + 0) * 64 + lane] = pack8(a_, 0); \
            _Pragma("unroll") for (int r_ = 8; r_ < 16; ++r_) a_[r_] = siluf_(a_[r_]); \
            fh[((4 * w + t_) * 2 + 1) * 64 + lane] = pack8(a_, 1); } \
        __syncthreads(); \
        acc = (f32x16){}; u32x4 bq_[4]; \
        _Pragma("unroll") for (int i_ = 0; i_ < 4; ++i_) bq_[i_] = fh[i_ * 64 + lane]; \
        _Pragma("unroll") for (int ks_ = 0; ks_ < 64; ++ks_) { \
            acc = MFMA(ring[((F0) + 64 + ks_) & 31], bq_[ks_ & 3], acc); \
            if (ks_ + 4 < 64) bq_[ks_ & 3] = fh[(ks_ + 4) * 64 + lane]; \
            REFILL((F0) + 64 + ks_, (F0) + 128); SB(); } } while (0)

__global__ __launch_bounds__(512, 2) void k_stageA(const uint16_t* __restrict__ x, u32x4* __restrict__ ws) {
    __shared__ u32x4 lds[16 * 64 + 64 * 64 + 256 + 512];
    u32x4* fx = lds;
    u32x4* fh = lds + 16 * 64;
    float* st0 = (float*)(lds + 16 * 64 + 64 * 64);
    float* st1 = st0 + 512;
    const int tile = blockIdx.x, tid = threadIdx.x, lane = tid & 63, h = lane >> 5, m = lane & 31;
    const int w = __builtin_amdgcn_readfirstlane(tid >> 6);
    u32x2 xr[4];
    {
        const uint16_t* xp = x + (size_t)(tile * 32 + m) * DM + 32 * w + 4 * h;
#pragma unroll
        for (int g = 0; g < 4; ++g) xr[g] = *(const u32x2*)(xp + 8 * g);
    }
    RAWBAR();
    constexpr int NFX = NFA;
    const u32x4* wp = ws + WS_WA + (size_t)w * NFA * 64 + lane;
    u32x4 ring[32];
    LD8(0); LD8(8);
    f32x16 xv;
#pragma unroll
    for (int g = 0; g < 4; ++g) { xv[4 * g + 0] = bflo(xr[g][0]); xv[4 * g + 1] = bfhi(xr[g][0]); xv[4 * g + 2] = bflo(xr[g][1]); xv[4 * g + 3] = bfhi(xr[g][1]); }
    float sc, sh;
    ln_partial(xv, st0, w, lane);
    __syncthreads();
    ln_finish(st0, lane, sc, sh);
    norm_store(xv, sc, sh, fx, w, lane);
    __syncthreads();
    {
        f32x16 acc;
        FFN(acc, 0);
#pragma unroll
        for (int r = 0; r < 16; ++r) xv[r] = rbf(__builtin_fmaf(0.5f, rbf(acc[r]), xv[r]));
    }
    ln_partial(xv, st1, w, lane);
    {
        u32x4* xo = ws + WS_X1 + (size_t)tile * 16 * 64;
        xo[(2 * w) * 64 + lane] = pack8(xv, 0);
        xo[(2 * w + 1) * 64 + lane] = pack8(xv, 1);
    }
    __syncthreads();
    ln_finish(st1, lane, sc, sh);
    norm_store(xv, sc, sh, fx, w, lane);
    __syncthreads();
    {
        const int b = tile / TPB, tt = tile % TPB, bh = b * 4 + (w >> 1), t2 = w & 1;
        u32x4* qo = ws + WS_Q + ((size_t)(bh * TPB + tt) * 4 + 2 * t2) * 64 + lane;
        u32x4* ko = ws + WS_KV + ((size_t)(bh * TPB + tt) * 8 + 2 * t2) * 64 + lane;
        f32x16 a;
        GEMM16(a, 128, NFA, fx, false);
        const float qs = 0.125f * 1.4426950408889634f;
#pragma unroll
        for (int r = 0; r < 16; ++r) a[r] *= qs;
        qo[0] = pack8(a, 0); qo[64] = pack8(a, 1);
        GEMM16(a, 144, NFA, fx, false);
        ko[0] = pack8(a, 0); ko[64] = pack8(a, 1);
        GEMM16(a, 160, NFA, fx, true);
        ko[4 * 64] = pack8(a, 0); ko[5 * 64] = pack8(a, 1);
    }
}

#define WAIT_BAR(N) asm volatile("s_waitcnt vmcnt(" #N ") lgkmcnt(0)\n\ts_barrier" ::: "memory")

constexpr float AT_BIG = 1.0e30f;
#define PIN(x) asm volatile("" : "+v"(x))
#define EX2(x) __builtin_amdgcn_exp2f(x)

DI void attn_body(const u32x4* ws_in, u32x4* ws, u32x4 (&ring)[32], u32x4* lds  ) {
    const int tid = threadIdx.x, lane = tid & 63;
    const int w = __builtin_amdgcn_readfirstlane(tid >> 6);
    const int bh = blockIdx.x & 7, qb = blockIdx.x >> 3, qi = w & 3, g = w >> 2;
    int qt = 4 * qb + qi; const bool qvalid = qt < TPB; if (!qvalid) qt = TPB - 1;
    const u32x4* kv = ws_in + WS_KV + (size_t)bh * TPB * 8 * 64;
    const unsigned lds0 = (unsigned)(uintptr_t)lds;
    const u32x4* src = kv + (w >> 1) * 512 + (w & 1) * 128 + lane;
    const u32x4* src31 = kv + 124 * 512 + (w & 1) * 128 + lane;
    const unsigned dstk = lds0 + (w >> 1) * 4096 + (w & 1) * 2048;
    const u32x4* srcw = kv + qi * 512 + lane;
    const u32x4* srcw31 = kv + 124 * 512 + lane;
    const unsigned dstw = lds0 + qi * 4096;
#define DMA2(sp, dst) do { glds16((sp), (unsigned)__builtin_amdgcn_readfirstlane(dst)); glds16((sp) + 64, (unsigned)__builtin_amdgcn_readfirstlane((dst) + 1024)); } while (0)
#define DMA_K(step) do { const int st_ = (step); const u32x4* s_ = st_ < 31 ? src + (size_t)st_ * 2048 : src31; DMA2(s_, dstk + (st_ & 3) * 16384); } while (0)
#define DMA_V(step) do { const int st_ = (step); const u32x4* s_ = st_ < 31 ? src + (size_t)st_ * 2048 : src31; DMA2(s_ + 256, dstk + (st_ % 5) * 16384 + 65536); } while (0)
    DMA_K(0); DMA_V(0); DMA_K(1);
    u32x4 q[4];
    {
        const u32x4* qp = ws_in + WS_Q + (size_t)(bh * TPB + qt) * 4 * 64 + lane;
#pragma unroll
        for (int f = 0; f < 4; ++f) q[f] = qp[f * 64];
    }
    DMA_K(2); DMA_V(1);
    const char* lbase = (const char*)lds + (2 * g) * 4096 + lane * 16;
#define KF(off, i) (*(const u32x4*)(lbase + (off) + (i) * 1024))
#define VF(off, i) (*(const u32x4*)(lbase + (off) + 65536 + (i) * 1024))
    f32x16 o0 = (f32x16){}, o1 = (f32x16){};
    float negm;
    f32x4 lsum = (f32x4){0.f, 0.f, 0.f, 0.f};
    u32x4 onesA;
    { const unsigned o_ = ((lane & 15) == ((lane >> 4) & 1)) ? 0x3f803f80u : 0u; onesA = (u32x4){o_, o_, o_, o_}; }
    f32x16 cm, sA, sB;
    u32x4 kf[4], vf[4], pA0, pA1, pB0, pB1;
    WAIT_BAR(4);
    {
#pragma unroll
        for (int i = 0; i < 4; ++i) kf[i] = KF(0, i);
        const f32x16 z = (f32x16){};
        sA = MFMA(kf[0], q[0], z); sA = MFMA(kf[1], q[1], sA); sA = MFMA(kf[2], q[2], sA); sA = MFMA(kf[3], q[3], sA);
#pragma unroll
        for (int i = 0; i < 4; ++i) kf[i] = KF(4096, i);
        float mx = sA[0];
#pragma unroll
        for (int r = 1; r < 16; ++r) mx = fmaxf(mx, sA[r]);
        mx = fmaxf(mx, xor32(mx));
        negm = -mx;
#pragma unroll
        for (int r = 0; r < 16; ++r) { sA[r] += negm; cm[r] = negm; sB[r] = 0.f; }
        pB0 = (u32x4){0u, 0u, 0u, 0u}; pB1 = pB0;
    }
    WAIT_BAR(0);
#define FIN(S, i, PK, e) do { PK[e] = pack2(S[i], S[(i) + 1]); PIN(PK); } while (0)
#define EXP2(S, i) do { S[i] = EX2(S[i]); S[(i) + 1] = EX2(S[(i) + 1]); PIN(S); } while (0)
#define LSUM(PK) do { lsum = __builtin_amdgcn_mfma_f32_16x16x32_bf16(__builtin_bit_cast(bf16x8, onesA), __builtin_bit_cast(bf16x8, PK), lsum, 0, 0, 0); PIN(lsum); } while (0)
#define PERIOD(SC, SN, PP0, PP1, PC0, PC1, koff, voff, D0, D1) do { \
        vf[0] = VF(voff, 0); FIN(SN, 14, PP1, 3); SB(); SN = MFMA(kf[0], q[0], cm); PIN(SN); EXP2(SC, 0); SB(); \
        vf[2] = VF(voff, 2); SB(); SN = MFMA(kf[1], q[1], SN); PIN(SN); EXP2(SC, 2); FIN(SC, 0, PC0, 0); SB(); \
        vf[1] = VF(voff, 1); SB(); SN = MFMA(kf[2], q[2], SN); PIN(SN); EXP2(SC, 4); FIN(SC, 2, PC0, 1); SB(); D0; SB(); \
        vf[3] = VF(voff, 3); SB(); SN = MFMA(kf[3], q[3], SN); PIN(SN); EXP2(SC, 6); FIN(SC, 4, PC0, 2); SB(); \
        kf[0] = KF(koff, 0); SB(); o0 = MFMA(vf[0], PP0, o0); PIN(o0); EXP2(SC, 8); FIN(SC, 6, PC0, 3); SB(); LSUM(PP0); SB(); \
        kf[1] = KF(koff, 1); SB(); o1 = MFMA(vf[2], PP0, o1); PIN(o1); EXP2(SC, 10); FIN(SC, 8, PC1, 0); SB(); \
        kf[2] = KF(koff, 2); SB(); o0 = MFMA(vf[1], PP1, o0); PIN(o0); EXP2(SC, 12); FIN(SC, 10, PC1, 1); SB(); LSUM(PP1); SB(); D1; SB(); \
        kf[3] = KF(koff, 3); SB(); o1 = MFMA(vf[3], PP1, o1); PIN(o1); EXP2(SC, 14); FIN(SC, 12, PC1, 2); SB(); \
    } while (0)
    int vm = 4096, v0 = 0, v1 = 16384;
#define G1(p, d, n) do { if (g == 0) glds16((p) + (n) * 64, (unsigned)__builtin_amdgcn_readfirstlane((d) + (n) * 1024)); } while (0)
#define NEXT5(v) ((v) >= 4 * 16384 ? 0 : (v) + 16384)
    for (int s0 = 0; s0 < 30; s0 += 2) {
        const int k1 = ((s0 + 1) & 3) * 16384, k2 = ((s0 + 2) & 3) * 16384;
        const int v2 = NEXT5(v1), v3 = NEXT5(v2);
        const int sk4 = s0 + 4 < 32 ? s0 + 4 : 31;
        const u32x4* pa_ = s0 + 3 < 31 ? srcw + (size_t)(s0 + 3) * 2048 : srcw31;
        const u32x4* pb_ = srcw + (size_t)(s0 + 2) * 2048 + 256;
        const u32x4* pc_ = sk4 < 31 ? srcw + (size_t)sk4 * 2048 : srcw31;
        const u32x4* pd_ = pa_ + 256;
        const unsigned da_ = dstw + ((s0 + 3) & 3) * 16384, db_ = dstw + 65536 + v2, dc_ = dstw + (sk4 & 3) * 16384, dd_ = dstw + 65536 + v3;
        SB();
        PERIOD(sA, sB, pB0, pB1, pA0, pA1, k1, vm, { G1(pa_, da_, 0); G1(pa_, da_, 1); G1(pa_, da_, 2); }, { G1(pa_, da_, 3); G1(pb_, db_, 0); G1(pb_, db_, 1); });
        PERIOD(sB, sA, pA0, pA1, pB0, pB1, k1 + 4096, v0, { G1(pb_, db_, 2); G1(pb_, db_, 3); G1(pc_, dc_, 0); }, { G1(pc_, dc_, 1); G1(pc_, dc_, 2); G1(pc_, dc_, 3); });
        PERIOD(sA, sB, pB0, pB1, pA0, pA1, k2, v0 + 4096, { G1(pd_, dd_, 0); G1(pd_, dd_, 1); }, { G1(pd_, dd_, 2); G1(pd_, dd_, 3); });
        PERIOD(sB, sA, pA0, pA1, pB0, pB1, k2 + 4096, v1, (void)0, (void)0);
        vm = v1 + 4096; v0 = v2; v1 = v3;
        WAIT_BAR(0);
    }
    {
        PERIOD(sA, sB, pB0, pB1, pA0, pA1, 3 * 16384, vm, (void)0, (void)0);
        PERIOD(sB, sA, pA0, pA1, pB0, pB1, 3 * 16384 + 4096, v0, (void)0, (void)0);
        if (g == 1) {
#pragma unroll
            for (int r = 0; r < 16; ++r) sA[r] = -INFINITY;
        }
        PERIOD(sA, sB, pB0, pB1, pA0, pA1, 0, v0 + 4096, (void)0, (void)0);
#pragma unroll
        for (int r = 0; r < 16; ++r) sB[r] = -INFINITY;
        PERIOD(sB, sA, pA0, pA1, pB0, pB1, 0, v1, (void)0, (void)0);
    }
    float l;
    { const float la = __shfl(lsum[0], lane & 15), lb = __shfl(lsum[1], lane & 15); l = 0.5f * ((lane & 16) ? lb : la); }
    WAIT_BAR(0);
#undef DMA_K
#undef DMA_V
#undef DMA2
#undef PERIOD
#undef FIN
#undef EXP2
#undef LSUM
#undef G1
#undef NEXT5
#undef KF
#undef VF
    {
        bool bad = !(l < AT_BIG);
#pragma unroll
        for (int r = 0; r < 16; ++r) bad = bad || !(fabsf(o0[r]) < INFINITY) || !(fabsf(o1[r]) < INFINITY);
        if (__builtin_expect(__any(bad), 0)) {
            o0 = (f32x16){}; o1 = (f32x16){}; l = 0.f; float mrun = -INFINITY;
            for (int kt = 0; kt < TPB; ++kt) {
                if (((kt >> 1) & 1) != g) continue;
                const u32x4* kg = kv + (size_t)kt * 512 + lane;
                f32x16 sc = (f32x16){};
#pragma unroll
                for (int f = 0; f < 4; ++f) sc = MFMA(kg[f * 64], q[f], sc);
                float mx = sc[0];
#pragma unroll
                for (int r = 1; r < 16; ++r) mx = fmaxf(mx, sc[r]);
                mx = fmaxf(mx, xor32(mx));
                const float mn = fmaxf(mrun, mx), f = EX2(mrun - mn);
                mrun = mn; l *= f;
#pragma unroll
                for (int r = 0; r < 16; ++r) { o0[r] *= f; o1[r] *= f; sc[r] = EX2(sc[r] - mn); l += sc[r]; }
                const u32x4 p0 = pack8(sc, 0), p1 = pack8(sc, 1);
                o0 = MFMA(kg[256], p0, o0); o1 = MFMA(kg[256 + 128], p0, o1);
                o0 = MFMA(kg[256 + 64], p1, o0); o1 = MFMA(kg[256 + 192], p1, o1);
            }
            negm = -mrun;
        }
    }
    l += xor32(l);
    float* cf = (float*)lds;
    if (g == 1) {
        float* dst = cf + qi * 34 * 64 + lane;
#pragma unroll
        for (int r = 0; r < 16; ++r) { dst[r * 64] = o0[r]; dst[(16 + r) * 64] = o1[r]; }
        dst[32 * 64] = negm; dst[33 * 64] = l;
    }
    __syncthreads();
    if (g == 0) {
        const float* s = cf + qi * 34 * 64 + lane;
        const float negm1 = s[32 * 64], l1 = s[33 * 64];
        const float nm = fminf(negm, negm1);
        const float f0 = __builtin_amdgcn_exp2f(nm - negm), f1 = __builtin_amdgcn_exp2f(nm - negm1);
        const float inv = __builtin_amdgcn_rcpf(l * f0 + l1 * f1);
        const float c0 = f0 * inv, c1 = f1 * inv;
#pragma unroll
        for (int r = 0; r < 16; ++r) { o0[r] = o0[r] * c0 + s[r * 64] * c1; o1[r] = o1[r] * c0 + s[(16 + r) * 64] * c1; }
        if (qvalid) {
            const int b = bh >> 2, hd = bh & 3;
            u32x4* ao = ws + WS_AT + ((size_t)(b * TPB + qt) * 16 + 4 * hd) * 64 + lane;
            const u32x4 r0 = pack8(o0, 0), r1 = pack8(o0, 1), r2 = pack8(o1, 0), r3 = pack8(o1, 1);
            asm volatile("global_store_dwordx4 %0, %1, off sc1\n\tglobal_store_dwordx4 %0, %2, off offset:1024 sc1\n\t"
                         "global_store_dwordx4 %0, %3, off offset:2048 sc1\n\tglobal_store_dwordx4 %0, %4, off offset:3072 sc1\n\t"
                         "s_waitcnt vmcnt(0)" :: "v"(ao), "v"(r0), "v"(r1), "v"(r2), "v"(r3) : "memory");
            if (lane == 0) __hip_atomic_fetch_add((unsigned*)(ws + WS_FL) + (b * TPB + qt) * 16, 1u, __ATOMIC_RELAXED, __HIP_MEMORY_SCOPE_AGENT);
        }
    }
    {
        const u32x4* wp = ws_in + WS_WC + (size_t)w * NFC * 64 + lane;
        LD8(0); LD8(8); LD8(16); LD8(24);
    }
}

DI void stageC_body(const u32x4* ws, const uint16_t* __restrict__ dw, uint16_t* __restrict__ out, u32x4 (&ring)[32], u32x4* lds) {
    u32x4* fx = lds;
    u32x4* fy = lds + 16 * 64;
    u32x4* fh = lds + 32 * 64;
    float* st0 = (float*)(lds + 32 * 64 + 64 * 64);
    float* st1 = st0 + 512;
    float* st2 = st0 + 1024;
    u32x4* dwl = lds + 32 * 64 + 64 * 64 + 384;
    const int tile = blockIdx.x, tid = threadIdx.x, lane = tid & 63, h = lane >> 5, m = lane & 31;
    const int w = __builtin_amdgcn_readfirstlane(tid >> 6);

#define MFMA16(a, b, c) __builtin_amdgcn_mfma_f32_16x16x32_bf16(__builtin_bit_cast(bf16x8, (a)), __builtin_bit_cast(bf16x8, (b)), (c), 0, 0, 0)
    const int tt = tile % TPB;
    const int c16 = lane & 15, g4 = lane >> 4, hm = lane & 3;
    int toff = hm < 2 ? hm - 2 : 30 + hm;
    const int tl = tt * 32 + toff;
    const bool hvalid = (tl >= 0) && (tl < T_SEQ);
    if (!hvalid) toff = 0;
    const int gtok = tile * 32 + toff;
    u32x4* hal = fy;
    const u32x4* hrd = (c16 < 8 && (c16 >> 2) == (g4 & 1)) ? hal + hm * 32 + (g4 >> 1) : hal + 128;

    u32x4 in[4]; u32x2 xh;
    {
        const u32x4* x1 = ws + WS_X1;
        const size_t mb = (size_t)tile * 16 * 64 + lane;
        in[2] = x1[mb + (2 * w) * 64]; in[3] = x1[mb + (2 * w + 1) * 64];
        xh = *(const u32x2*)((const char*)(x1 + ((size_t)(gtok >> 5) * 16 + 2 * w + ((c16 >> 2) & 1)) * 64 + (g4 & 1) * 32 + (gtok & 31)) + 8 * (g4 >> 1));
    }
    u32x4 dwr;
    if (tid < 160) dwr = ((const u32x4*)dw)[tid];
    constexpr int NFX = NFC;
    const u32x4* wp = ws + WS_WC + (size_t)w * NFC * 64 + lane;
    if (w == 0) {
        const int tn = tt + lane - 1;
        const bool need = lane < 3 && tn >= 0 && tn < TPB;
        const unsigned* fl = (const unsigned*)(ws + WS_FL) + (need ? tile + lane - 1 : tile) * 16;
        for (unsigned spin = 0; spin < (1u << 24); ++spin) {
            const unsigned v = __hip_atomic_load(fl, __ATOMIC_RELAXED, __HIP_MEMORY_SCOPE_AGENT);
            if (__all(v >= 4u)) break;
            __builtin_amdgcn_s_sleep(1);
        }
    }
    __syncthreads();
    u32x4 hin;
    {
        const __amdgpu_buffer_rsrc_t rs = __builtin_amdgcn_make_buffer_rsrc((void*)(ws + WS_AT), 0, NTILE * 16 * 1024, 0x00020000);
        const int mo = (tile * 16 * 64 + lane + 2 * w * 64) * 16;
        in[0] = __builtin_bit_cast(u32x4, __builtin_amdgcn_raw_buffer_load_b128(rs, mo, 0, 16));
        in[1] = __builtin_bit_cast(u32x4, __builtin_amdgcn_raw_buffer_load_b128(rs, mo + 1024, 0, 16));
        const int ho = (((gtok >> 5) * 16 + 2 * w + ((lane >> 3) & 1)) * 64 + ((lane >> 2) & 1) * 32 + (gtok & 31)) * 16;
        hin = __builtin_bit_cast(u32x4, __builtin_amdgcn_raw_buffer_load_b128(rs, ho, 0, 16));
    }
    f32x16 xv; f32x4 yv;
    fx[(2 * w) * 64 + lane] = in[0]; fx[(2 * w + 1) * 64 + lane] = in[1];
    if (lane < 16) hal[hm * 32 + (2 * w + ((lane >> 3) & 1)) * 2 + ((lane >> 2) & 1)] = hin;
    if (tid >= 480) hal[128 + tid - 480] = (u32x4){0u, 0u, 0u, 0u};
    if (tid < 160) dwl[tid] = dwr;
    unpack16(xv, in[2], in[3]);
    yv = (f32x4){bflo(xh[0]), bfhi(xh[0]), bflo(xh[1]), bfhi(xh[1])};
    __syncthreads();
    {
        f32x16 a = (f32x16){}; f32x4 b = (f32x4){0.f, 0.f, 0.f, 0.f};
        u32x4 bx[2], by[2];
        bx[0] = fx[lane]; by[0] = hrd[0]; bx[1] = fx[64 + lane]; by[1] = hrd[2];
#pragma unroll
        for (int ks = 0; ks < 16; ++ks) {
            a = MFMA(ring[ks & 31], bx[ks & 1], a);
            b = MFMA16(ring[ks & 31], by[ks & 1], b);
            if (ks + 2 < 16) { bx[ks & 1] = fx[(ks + 2) * 64 + lane]; by[ks & 1] = hrd[2 * (ks + 2)]; }
            REFILL(ks, 0);
            SB();
        }
#pragma unroll
        for (int r = 0; r < 16; ++r) xv[r] = rbf(xv[r] + rbf(a[r]));
#pragma unroll
        for (int r = 0; r < 4; ++r) yv[r] = rbf(yv[r] + rbf(b[r]));
    }
    float sc, sh;
    ln_partial(xv, st0, w, lane);
    {
        float s_ = (yv[0] + yv[1]) + (yv[2] + yv[3]), q_ = (yv[0] * yv[0] + yv[1] * yv[1]) + (yv[2] * yv[2] + yv[3] * yv[3]);
        s_ += __shfl_xor(s_, 4); q_ += __shfl_xor(q_, 4);
        s_ += __shfl_xor(s_, 16); q_ += __shfl_xor(q_, 16);
        s_ += __shfl_xor(s_, 32); q_ += __shfl_xor(q_, 32);
        if (lane < 4) { st1[w * 8 + lane] = s_; st1[w * 8 + 4 + lane] = q_; }
    }
    __syncthreads();
    ln_finish(st0, lane, sc, sh);
    norm_store(xv, sc, sh, fx, w, lane);
    {
        float s_ = 0.f, q_ = 0.f;
#pragma unroll
        for (int i = 0; i < 8; ++i) { s_ += st1[i * 8 + hm]; q_ += st1[i * 8 + 4 + hm]; }
        const float m_ = s_ * (1.0f / 256.0f);
        const float sc2 = __builtin_amdgcn_rsqf(fmaxf(q_ * (1.0f / 256.0f) - m_ * m_, 0.f) + 1e-5f), sh2 = -m_ * sc2;
        const u32x2 o_ = {pack2(__builtin_fmaf(yv[0], sc2, sh2), __builtin_fmaf(yv[1], sc2, sh2)), pack2(__builtin_fmaf(yv[2], sc2, sh2), __builtin_fmaf(yv[3], sc2, sh2))};
        if (c16 < 8) *(u32x2*)((char*)(hal + hm * 32 + (2 * w + (c16 >> 2)) * 2 + (g4 & 1)) + 8 * (g4 >> 1)) = o_;
    }
    __syncthreads();
    {
        f32x16 a = (f32x16){}, b = (f32x16){}; f32x4 c = (f32x4){0.f, 0.f, 0.f, 0.f}, d = c;
        u32x4 bxn = fx[lane], byn = hrd[0];
#pragma unroll
        for (int ks = 0; ks < 16; ++ks) {
            const int F = 16 + 2 * ks;
            const u32x4 bx = bxn, by = byn;
            if (ks + 1 < 16) { bxn = fx[(ks + 1) * 64 + lane]; byn = hrd[2 * (ks + 1)]; }
            a = MFMA(ring[F & 31], bx, a); c = MFMA16(ring[F & 31], by, c); REFILL(F, 0);
            b = MFMA(ring[(F + 1) & 31], bx, b); d = MFMA16(ring[(F + 1) & 31], by, d); REFILL(F + 1, 0);
            SB();
        }
#pragma unroll
        for (int r = 0; r < 16; ++r) a[r] = a[r] * sigmoidf_(b[r]);
#pragma unroll
        for (int r = 0; r < 4; ++r) c[r] = hvalid ? c[r] * sigmoidf_(d[r]) : 0.f;
        f32x4* cs = (f32x4*)fh + w * (4 * 2 * 36);
#pragma unroll
        for (int g = 0; g < 4; ++g) cs[(g * 2 + h) * 36 + m + 2] = (f32x4){a[4 * g], a[4 * g + 1], a[4 * g + 2], a[4 * g + 3]};
        if (c16 < 8) cs[((2 * (c16 >> 2) + (g4 >> 1)) * 2 + (g4 & 1)) * 36 + (hm < 2 ? hm : 32 + hm)] = c;
        __syncthreads();
        const uint16_t* dp = (const uint16_t*)dwl + 32 * w + 4 * h;
#pragma unroll
        for (int g = 0; g < 4; ++g) {
            f32x4 acc = (f32x4){0.f, 0.f, 0.f, 0.f};
#pragma unroll
            for (int i = 0; i < 5; ++i) {
                const f32x4 hv = cs[(g * 2 + h) * 36 + m + i];
                const u32x2 t = *(const u32x2*)(dp + i * DM + 8 * g);
                acc[0] += hv[0] * bflo(t[0]); acc[1] += hv[1] * bfhi(t[0]); acc[2] += hv[2] * bflo(t[1]); acc[3] += hv[3] * bfhi(t[1]);
            }
#pragma unroll
            for (int i = 0; i < 4; ++i) a[4 * g + i] = siluf_(acc[i]);
        }
        fx[(2 * w) * 64 + lane] = pack8(a, 0);
        fx[(2 * w + 1) * 64 + lane] = pack8(a, 1);
    }
    __syncthreads();
    {
        f32x16 a;
        GEMM16(a, 48, 64, fx, false);
#pragma unroll
        for (int r = 0; r < 16; ++r) xv[r] = rbf(xv[r] + rbf(a[r]));
    }
    ln_partial(xv, st2, w, lane);
    __syncthreads();
    ln_finish(st2, lane, sc, sh);
    norm_store(xv, sc, sh, fx, w, lane);
    __syncthreads();
    {
        f32x16 acc;
        FFN(acc, 64);
#pragma unroll
        for (int r = 0; r < 16; ++r) xv[r] = rbf(__builtin_fmaf(0.5f, rbf(acc[r]), xv[r]));
    }
    ln_partial(xv, st0, w, lane);
    __syncthreads();
    ln_finish(st0, lane, sc, sh);
    {
        uint16_t* op = out + (size_t)(tile * 32 + m) * DM + 32 * w + 4 * h;
#pragma unroll
        for (int g = 0; g < 4; ++g) {
            u32x2 t;
            t[0] = pack2(__builtin_fmaf(xv[4 * g], sc, sh), __builtin_fmaf(xv[4 * g + 1], sc, sh));
            t[1] = pack2(__builtin_fmaf(xv[4 * g + 2], sc, sh), __builtin_fmaf(xv[4 * g + 3], sc, sh));
            *(u32x2*)(op + 8 * g) = t;
        }
    }
}

__global__ __launch_bounds__(512, 2) void k_attnC(u32x4* ws, const uint16_t* __restrict__ dw, uint16_t* __restrict__ out) {
    __shared__ u32x4 lds[9 * 1024];
    u32x4 ring[32];
    attn_body(ws, ws, ring, lds);
    if (blockIdx.x >= NTILE) return;
    __syncthreads();
    stageC_body(ws, dw, out, ring, lds);
}

extern "C" void kernel_launch(void* const* d_in, const int* in_sizes, int n_in,
                              void* d_out, int out_size, void* d_ws, size_t ws_size,
                              hipStream_t stream) {
    PackArgs p;
    p.w1a = (const uint16_t*)d_in[1]; p.w1b = (const uint16_t*)d_in[2]; p.wqkv = (const uint16_t*)d_in[3]; p.wo = (const uint16_t*)d_in[4];
    p.wp1 = (const uint16_t*)d_in[5]; p.wp2 = (const uint16_t*)d_in[7]; p.w2a = (const uint16_t*)d_in[8]; p.w2b = (const uint16_t*)d_in[9];
    p.ws = (u32x4*)d_ws;
    u32x4* ws = (u32x4*)d_ws;
    const int nchunk = 8 * (NFA + NFC) * 64;
    k_pack<<<(nchunk / 2 + 255) / 256, 256, 0, stream>>>(p);
    k_stageA<<<NTILE, 512, 0, stream>>>((const uint16_t*)d_in[0], ws);
    k_attnC<<<256, 512, 0, stream>>>(ws, (const uint16_t*)d_in[6], (uint16_t*)d_out);
}
```

```cpp
#include <hip/hip_runtime.h>
#include <hip/hip_bf16.h>
#include <stdint.h>


typedef short bf16x8 __attribute__((ext_vector_type(8)));
typedef float f32x16 __attribute__((ext_vector_type(16)));
typedef float f32x2 __attribute__((ext_vector_type(2)));
typedef float f32x4 __attribute__((ext_vector_type(4)));
typedef unsigned u32x4 __attribute__((ext_vector_type(4)));
typedef unsigned u32x2 __attribute__((ext_vector_type(2)));
typedef __bf16 bf16v2 __attribute__((ext_vector_type(2)));

#define DI __device__ __forceinline__
#define SB() __builtin_amdgcn_sched_barrier(0)
#define MFMA(a, b, c) __builtin_amdgcn_mfma_f32_32x32x16_bf16(__builtin_bit_cast(bf16x8, (a)), __builtin_bit_cast(bf16x8, (b)), (c), 0, 0, 0)

constexpr int T_SEQ = 4000, DM = 256, NTOK = 8000, NTILE = 250, TPB = 125;
constexpr int NFA = 176;
constexpr int NFC = 192;

constexpr size_t WS_WA = 0;
constexpr size_t WS_WC = WS_WA + (size_t)8 * NFA * 64;
constexpr size_t WS_X1 = WS_WC + (size_t)8 * NFC * 64;
constexpr size_t WS_Q  = WS_X1 + (size_t)NTILE * 16 * 64;
constexpr size_t WS_KV = WS_Q + (size_t)8 * TPB * 4 * 64;
constexpr size_t WS_AT = WS_KV + (size_t)8 * TPB * 8 * 64 + 4096;
constexpr size_t WS_FL = WS_AT + (size_t)NTILE * 16 * 64;

DI unsigned pack2(float lo, float hi) {
    f32x2 v = {lo, hi};
    bf16v2 r = __builtin_convertvector(v, bf16v2);
    return __builtin_bit_cast(unsigned, r);
}
DI float bflo(unsigned u) { return __uint_as_float(u << 16); }
DI float bfhi(unsigned u) { return __uint_as_float(u & 0xffff0000u); }
DI u32x4 pack8(const f32x16& a, int s) {
    u32x4 r;
    r[0] = pack2(a[8 * s + 0], a[8 * s + 1]);
    r[1] = pack2(a[8 * s + 2], a[8 * s + 3]);
    r[2] = pack2(a[8 * s + 4], a[8 * s + 5]);
    r[3] = pack2(a[8 * s + 6], a[8 * s + 7]);
    return r;
}
DI void unpack16(f32x16& a, const u32x4& f0, const u32x4& f1) {
#pragma unroll
    for (int i = 0; i < 4; ++i) {
        a[2 * i] = bflo(f0[i]); a[2 * i + 1] = bfhi(f0[i]);
        a[8 + 2 * i] = bflo(f1[i]); a[8 + 2 * i + 1] = bfhi(f1[i]);
    }
}
DI float xor32(float v) { return __shfl_xor(v, 32); }
DI float sigmoidf_(float x) { return __builtin_amdgcn_rcpf(1.0f + __builtin_amdgcn_exp2f(-1.4426950408889634f * x)); }
DI float siluf_(float x) { return x * sigmoidf_(x); }

struct PackArgs {
    const uint16_t* w1a; const uint16_t* w1b; const uint16_t* wqkv; const uint16_t* wo;
    const uint16_t* wp1; const uint16_t* wp2; const uint16_t* w2a; const uint16_t* w2b;
    u32x4* ws; const u32x4* x;
};

__global__ __launch_bounds__(256) void k_pack(PackArgs p) {
    const int gid = blockIdx.x * 256 + threadIdx.x;
    const int lane = gid & 63, fg = gid >> 6;
    if (gid < NTILE * 4) p.ws[WS_FL + gid] = (u32x4){0u, 0u, 0u, 0u};
    if (fg >= 8 * (NFA + NFC)) {
        const int i = gid - 8 * (NFA + NFC) * 64;
        const u32x4 v = p.x[i];
        asm volatile("" :: "v"(v));
        return;
    }
    const uint16_t* W; int N, nt, ks;
    if (fg < 8 * NFA) {
        const int w = fg / NFA, f = fg % NFA;
        if (f < 64)       { W = p.w1a; N = 1024; ks = f & 15; nt = 4 * w + (f >> 4); }
        else if (f < 128) { W = p.w1b; N = 256; ks = f - 64; nt = w; }
        else              { const int g = f - 128; W = p.wqkv; N = 768; ks = g & 15; nt = w + 8 * (g >> 4); }
    } else {
        const int fg2 = fg - 8 * NFA, w = fg2 / NFC, f = fg2 % NFC;
        if (f < 16)       { W = p.wo; N = 256; ks = f; nt = w; }
        else if (f < 48)  { const int g = f - 16; W = p.wp1; N = 512; ks = g >> 1; nt = w + 8 * (g & 1); }
        else if (f < 64)  { W = p.wp2; N = 256; ks = f - 48; nt = w; }
        else if (f < 128) { const int g = f - 64; W = p.w2a; N = 1024; ks = g & 15; nt = 4 * w + (g >> 4); }
        else              { W = p.w2b; N = 256; ks = f - 128; nt = w; }
    }
    const int r = lane & 31, h = lane >> 5;
    const uint16_t* src = W + (size_t)(16 * ks + 4 * h) * N + 32 * nt + r;
    unsigned e[8];
#pragma unroll
    for (int j = 0; j < 8; ++j) e[j] = src[(size_t)(8 * (j >> 2) + (j & 3)) * N];
    u32x4 o;
#pragma unroll
    for (int i = 0; i < 4; ++i) o[i] = e[2 * i] | (e[2 * i + 1] << 16);
    p.ws[gid] = o;
}

DI void glds16(const void* g, unsigned lds_base) {
    unsigned sv;
    asm volatile("s_mov_b32 %0, m0\n\ts_mov_b32 m0, %2\n\ts_nop 0\n\tglobal_load_lds_dwordx4 %1, off\n\ts_mov_b32 m0, %0"
                 : "=&s"(sv) : "v"(g), "s"(lds_base) : "memory");
}
#define WLOAD(F) ring[(F) & 31] = wp[(F) * 64]
#define REFILL(F, PE) do { if ((F) < 16 && NFX == NFA) WLOAD((F) + 16); if ((F) + 32 < (NFX)) WLOAD((F) + 32); } while (0)
#define LD8(F) do { SB(); _Pragma("unroll") for (int i_ = 0; i_ < 8; ++i_) WLOAD((F) + i_); SB(); } while (0)
#define RAWBAR() do { SB(); __builtin_amdgcn_s_barrier(); SB(); } while (0)

DI void ln_partial(const f32x16& v, float* st, int w, int lane) {
    float s = 0.f, q = 0.f;
#pragma unroll
    for (int i = 0; i < 16; ++i) { s += v[i]; q = __builtin_fmaf(v[i], v[i], q); }
    auto r = __builtin_amdgcn_permlane32_swap(__float_as_uint(s), __float_as_uint(q), false, false);
    st[w * 64 + lane] = __uint_as_float(r[0]) + __uint_as_float(r[1]);
}
DI void ln_finish(const float* st, int lane, float& scale, float& shift) {
    const float* p = st + (lane & 31);
    float s = 0.f, q = 0.f;
#pragma unroll
    for (int i = 0; i < 8; ++i) { s += p[i * 64]; q += p[i * 64 + 32]; }
    const float m = s * (1.0f / 256.0f);
    const float var = fmaxf(q * (1.0f / 256.0f) - m * m, 0.f);
    scale = __builtin_amdgcn_rsqf(var + 1e-5f);
    shift = -m * scale;
}
DI void norm_store(const f32x16& v, float scale, float shift, u32x4* dst  , int w, int lane) {
    f32x16 n;
#pragma unroll
    for (int i = 0; i < 16; ++i) n[i] = __builtin_fmaf(v[i], scale, shift);
    dst[(2 * w) * 64 + lane] = pack8(n, 0);
    dst[(2 * w + 1) * 64 + lane] = pack8(n, 1);
}
DI float rbf(float x) { return bflo(pack2(x, 0.f) & 0xffffu); }

#define GEMM16(acc, F0, PE, fxp, SWAP) do { \
        acc = (f32x16){}; u32x4 bq_[4]; \
        _Pragma("unroll") for (int i_ = 0; i_ < 4; ++i_) bq_[i_] = (fxp)[i_ * 64 + lane]; \
        _Pragma("unroll") for (int ks_ = 0; ks_ < 16; ++ks_) { \
            if (SWAP) acc = MFMA(bq_[ks_ & 3], ring[((F0) + ks_) & 31], acc); else acc = MFMA(ring[((F0) + ks_) & 31], bq_[ks_ & 3], acc); \
            if (ks_ + 4 < 16) bq_[ks_ & 3] = (fxp)[(ks_ + 4) * 64 + lane]; \
            REFILL((F0) + ks_, PE); SB(); } } while (0)

#define FFN(acc, F0) do { \
        _Pragma("unroll") for (int t_ = 0; t_ < 4; ++t_) { \
            f32x16 a_; GEMM16(a_, (F0) + 16 * t_, (F0) + 64, fx, false); \
            _Pragma("unroll") for (int r_ = 0; r_ < 8; ++r_) a_[r_] = siluf_(a_[r_]); \
            fh[((4 * w + t_) * 2 + 0) * 64 + lane] = pack8(a_, 0); \
            _Pragma("unroll") for (int r_ = 8; r_ < 16; ++r_) a_[r_] = siluf_(a_[r_]); \
            fh[((4 * w + t_) * 2 + 1) * 64 + lane] = pack8(a_, 1); } \
        __syncthreads(); \
        acc = (f32x16){}; u32x4 bq_[4]; \
        _Pragma("unroll") for (int i_ = 0; i_ < 4; ++i_) bq_[i_] = fh[i_ * 64 + lane]; \
        _Pragma("unroll") for (int ks_ = 0; ks_ < 64; ++ks_) { \
            acc = MFMA(ring[((F0) + 64 + ks_) & 31], bq_[ks_ & 3], acc); \
            if (ks_ + 4 < 64) bq_[ks_ & 3] = fh[(ks_ + 4) * 64 + lane]; \
            REFILL((F0) + 64 + ks_, (F0) + 128); SB(); } } while (0)

__global__ __launch_bounds__(512, 2) void k_stageA(const uint16_t* __restrict__ x, u32x4* __restrict__ ws) {
    __shared__ u32x4 lds[16 * 64 + 64 * 64 + 256 + 512];
    u32x4* fx = lds;
    u32x4* fh = lds + 16 * 64;
    float* st0 = (float*)(lds + 16 * 64 + 64 * 64);
    float* st1 = st0 + 512;
    const int tile = blockIdx.x, tid = threadIdx.x, lane = tid & 63, h = lane >> 5, m = lane & 31;
    const int w = __builtin_amdgcn_readfirstlane(tid >> 6);
    u32x2 xr[4];
    {
        const uint16_t* xp = x + (size_t)(tile * 32 + m) * DM + 32 * w + 4 * h;
#pragma unroll
        for (int g = 0; g < 4; ++g) xr[g] = *(const u32x2*)(xp + 8 * g);
    }
    RAWBAR();
    constexpr int NFX = NFA;
    const u32x4* wp = ws + WS_WA + (size_t)w * NFA * 64 + lane;
    u32x4 ring[32];
    LD8(0); LD8(8);
    f32x16 xv;
#pragma unroll
    for (int g = 0; g < 4; ++g) { xv[4 * g + 0] = bflo(xr[g][0]); xv[4 * g + 1] = bfhi(xr[g][0]); xv[4 * g + 2] = bflo(xr[g][1]); xv[4 * g + 3] = bfhi(xr[g][1]); }
    float sc, sh;
    ln_partial(xv, st0, w, lane);
    __syncthreads();
    ln_finish(st0, lane, sc, sh);
    norm_store(xv, sc, sh, fx, w, lane);
    __syncthreads();
    {
        f32x16 acc;
        FFN(acc, 0);
#pragma unroll
        for (int r = 0; r < 16; ++r) xv[r] = rbf(__builtin_fmaf(0.5f, rbf(acc[r]), xv[r]));
    }
    ln_partial(xv, st1, w, lane);
    {
        u32x4* xo = ws + WS_X1 + (size_t)tile * 16 * 64;
        xo[(2 * w) * 64 + lane] = pack8(xv, 0);
        xo[(2 * w + 1) * 64 + lane] = pack8(xv, 1);
    }
    __syncthreads();
    ln_finish(st1, lane, sc, sh);
    norm_store(xv, sc, sh, fx, w, lane);
    __syncthreads();
    {
        const int b = tile / TPB, tt = tile % TPB, bh = b * 4 + (w >> 1), t2 = w & 1;
        u32x4* qo = ws + WS_Q + ((size_t)(bh * TPB + tt) * 4 + 2 * t2) * 64 + lane;
        u32x4* ko = ws + WS_KV + ((size_t)(bh * TPB + tt) * 8 + 2 * t2) * 64 + lane;
        f32x16 a;
        GEMM16(a, 128, NFA, fx, false);
        const float qs = 0.125f * 1.4426950408889634f;
#pragma unroll
        for (int r = 0; r < 16; ++r) a[r] *= qs;
        qo[0] = pack8(a, 0); qo[64] = pack8(a, 1);
        GEMM16(a, 144, NFA, fx, false);
        ko[0] = pack8(a, 0); ko[64] = pack8(a, 1);
        GEMM16(a, 160, NFA, fx, true);
        ko[4 * 64] = pack8(a, 0); ko[5 * 64] = pack8(a, 1);
    }
}

#define WAIT_BAR(N) asm volatile("s_waitcnt vmcnt(" #N ") lgkmcnt(0)\n\ts_barrier" ::: "memory")

constexpr float AT_BIG = 1.0e30f;
#define PIN(x) asm volatile("" : "+v"(x))
#define EX2(x) __builtin_amdgcn_exp2f(x)

DI void attn_body(const u32x4* ws_in, u32x4* ws, u32x4 (&ring)[32], u32x4* lds  ) {
    const int tid = threadIdx.x, lane = tid & 63;
    const int w = __builtin_amdgcn_readfirstlane(tid >> 6);
    const int bh = blockIdx.x & 7, qb = blockIdx.x >> 3, qi = w & 3, g = w >> 2;
    int qt = 4 * qb + qi; const bool qvalid = qt < TPB; if (!qvalid) qt = TPB - 1;
    const u32x4* kv = ws_in + WS_KV + (size_t)bh * TPB * 8 * 64;
    const unsigned lds0 = (unsigned)(uintptr_t)lds;
    const u32x4* src = kv + (w >> 1) * 512 + (w & 1) * 128 + lane;
    const u32x4* src31 = kv + 124 * 512 + (w & 1) * 128 + lane;
    const unsigned dstk = lds0 + (w >> 1) * 4096 + (w & 1) * 2048;
    const u32x4* srcw = kv + qi * 512 + lane;
    const u32x4* srcw31 = kv + 124 * 512 + lane;
    const unsigned dstw = lds0 + qi * 4096;
#define DMA2(sp, dst) do { glds16((sp), (unsigned)__builtin_amdgcn_readfirstlane(dst)); glds16((sp) + 64, (unsigned)__builtin_amdgcn_readfirstlane((dst) + 1024)); } while (0)
#define DMA_K(step) do { const int st_ = (step); const u32x4* s_ = st_ < 31 ? src + (size_t)st_ * 2048 : src31; DMA2(s_, dstk + (st_ & 3) * 16384); } while (0)
#define DMA_V(step) do { const int st_ = (step); const u32x4* s_ = st_ < 31 ? src + (size_t)st_ * 2048 : src31; DMA2(s_ + 256, dstk + (st_ % 5) * 16384 + 65536); } while (0)
    DMA_K(0); DMA_V(0); DMA_K(1);
    u32x4 q[4];
    {
        const u32x4* qp = ws_in + WS_Q + (size_t)(bh * TPB + qt) * 4 * 64 + lane;
#pragma unroll
        for (int f = 0; f < 4; ++f) q[f] = qp[f * 64];
    }
    DMA_K(2); DMA_V(1);
    const char* lbase = (const char*)lds + (2 * g) * 4096 + lane * 16;
#define KF(off, i) (*(const u32x4*)(lbase + (off) + (i) * 1024))
#define VF(off, i) (*(const u32x4*)(lbase + (off) + 65536 + (i) * 1024))
    f32x16 o0 = (f32x16){}, o1 = (f32x16){};
    float negm;
    f32x4 lsum = (f32x4){0.f, 0.f, 0.f, 0.f};
    u32x4 onesA;
    { const unsigned o_ = ((lane & 15) == ((lane >> 4) & 1)) ? 0x3f803f80u : 0u; onesA = (u32x4){o_, o_, o_, o_}; }
    f32x16 cm, sA, sB;
    u32x4 kf[4], vf[4], pA0, pA1, pB0, pB1;
    WAIT_BAR(4);
    {
#pragma unroll
        for (int i = 0; i < 4; ++i) kf[i] = KF(0, i);
        const f32x16 z = (f32x16){};
        sA = MFMA(kf[0], q[0], z); sA = MFMA(kf[1], q[1], sA); sA = MFMA(kf[2], q[2], sA); sA = MFMA(kf[3], q[3], sA);
#pragma unroll
        for (int i = 0; i < 4; ++i) kf[i] = KF(4096, i);
        float mx = sA[0];
#pragma unroll
        for (int r = 1; r < 16; ++r) mx = fmaxf(mx, sA[r]);
        mx = fmaxf(mx, xor32(mx));
        negm = -mx;
#pragma unroll
        for (int r = 0; r < 16; ++r) { sA[r] += negm; cm[r] = negm; sB[r] = 0.f; }
        pB0 = (u32x4){0u, 0u, 0u, 0u}; pB1 = pB0;
    }
    WAIT_BAR(0);
#define FIN(S, i, PK, e) do { PK[e] = pack2(S[i], S[(i) + 1]); PIN(PK); } while (0)
#define EXP2(S, i) do { S[i] = EX2(S[i]); S[(i) + 1] = EX2(S[(i) + 1]); PIN(S); } while (0)
#define LSUM(PK) do { lsum = __builtin_amdgcn_mfma_f32_16x16x32_bf16(__builtin_bit_cast(bf16x8, onesA), __builtin_bit_cast(bf16x8, PK), lsum, 0, 0, 0); PIN(lsum); } while (0)
#define PERIOD(SC, SN, PP0, PP1, PC0, PC1, koff, voff, D0, D1) do { \
        vf[0] = VF(voff, 0); FIN(SN, 14, PP1, 3); SB(); SN = MFMA(kf[0], q[0], cm); PIN(SN); EXP2(SC, 0); SB(); \
        vf[2] = VF(voff, 2); SB(); SN = MFMA(kf[1], q[1], SN); PIN(SN); EXP2(SC, 2); FIN(SC, 0, PC0, 0); SB(); \
        vf[1] = VF(voff, 1); SB(); SN = MFMA(kf[2], q[2], SN); PIN(SN); EXP2(SC, 4); FIN(SC, 2, PC0, 1); SB(); D0; SB(); \
        vf[3] = VF(voff, 3); SB(); SN = MFMA(kf[3], q[3], SN); PIN(SN); EXP2(SC, 6); FIN(SC, 4, PC0, 2); SB(); \
        kf[0] = KF(koff, 0); SB(); o0 = MFMA(vf[0], PP0, o0); PIN(o0); EXP2(SC, 8); FIN(SC, 6, PC0, 3); SB(); LSUM(PP0); SB(); \
        kf[1] = KF(koff, 1); SB(); o1 = MFMA(vf[2], PP0, o1); PIN(o1); EXP2(SC, 10); FIN(SC, 8, PC1, 0); SB(); \
        kf[2] = KF(koff, 2); SB(); o0 = MFMA(vf[1], PP1, o0); PIN(o0); EXP2(SC, 12); FIN(SC, 10, PC1, 1); SB(); LSUM(PP1); SB(); D1; SB(); \
        kf[3] = KF(koff, 3); SB(); o1 = MFMA(vf[3], PP1, o1); PIN(o1); EXP2(SC, 14); FIN(SC, 12, PC1, 2); SB(); \
    } while (0)
    int vm = 4096, v0 = 0, v1 = 16384;
#define G1(p, d, n) do { if (g == 0) glds16((p) + (n) * 64, (unsigned)__builtin_amdgcn_readfirstlane((d) + (n) * 1024)); } while (0)
#define NEXT5(v) ((v) >= 4 * 16384 ? 0 : (v) + 16384)
    for (int s0 = 0; s0 < 30; s0 += 2) {
        const int k1 = ((s0 + 1) & 3) * 16384, k2 = ((s0 + 2) & 3) * 16384;
        const int v2 = NEXT5(v1), v3 = NEXT5(v2);
        const int sk4 = s0 + 4 < 32 ? s0 + 4 : 31;
        const u32x4* pa_ = s0 + 3 < 31 ? srcw + (size_t)(s0 + 3) * 2048 : srcw31;
        const u32x4* pb_ = srcw + (size_t)(s0 + 2) * 2048 + 256;
        const u32x4* pc_ = sk4 < 31 ? srcw + (size_t)sk4 * 2048 : srcw31;
        const u32x4* pd_ = pa_ + 256;
        const unsigned da_ = dstw + ((s0 + 3) & 3) * 16384, db_ = dstw + 65536 + v2, dc_ = dstw + (sk4 & 3) * 16384, dd_ = dstw + 65536 + v3;
        SB();
        PERIOD(sA, sB, pB0, pB1, pA0, pA1, k1, vm, { G1(pa_, da_, 0); G1(pa_, da_, 1); G1(pa_, da_, 2); }, { G1(pa_, da_, 3); G1(pb_, db_, 0); G1(pb_, db_, 1); });
        PERIOD(sB, sA, pA0, pA1, pB0, pB1, k1 + 4096, v0, { G1(pb_, db_, 2); G1(pb_, db_, 3); G1(pc_, dc_, 0); }, { G1(pc_, dc_, 1); G1(pc_, dc_, 2); G1(pc_, dc_, 3); });
        PERIOD(sA, sB, pB0, pB1, pA0, pA1, k2, v0 + 4096, { G1(pd_, dd_, 0); G1(pd_, dd_, 1); }, { G1(pd_, dd_, 2); G1(pd_, dd_, 3); });
        PERIOD(sB, sA, pA0, pA1, pB0, pB1, k2 + 4096, v1, (void)0, (void)0);
        vm = v1 + 4096; v0 = v2; v1 = v3;
        WAIT_BAR(0);
    }
    {
        PERIOD(sA, sB, pB0, pB1, pA0, pA1, 3 * 16384, vm, (void)0, (void)0);
        PERIOD(sB, sA, pA0, pA1, pB0, pB1, 3 * 16384 + 4096, v0, (void)0, (void)0);
        if (g == 1) {
#pragma unroll
            for (int r = 0; r < 16; ++r) sA[r] = -INFINITY;
        }
        PERIOD(sA, sB, pB0, pB1, pA0, pA1, 0, v0 + 4096, (void)0, (void)0);
#pragma unroll
        for (int r = 0; r < 16; ++r) sB[r] = -INFINITY;
        PERIOD(sB, sA, pA0, pA1, pB0, pB1, 0, v1, (void)0, (void)0);
    }
    float l;
    { const float la = __shfl(lsum[0], lane & 15), lb = __shfl(lsum[1], lane & 15); l = 0.5f * ((lane & 16) ? lb : la); }
    WAIT_BAR(0);
#undef DMA_K
#undef DMA_V
#undef DMA2
#undef PERIOD
#undef FIN
#undef EXP2
#undef LSUM
#undef G1
#undef NEXT5
#undef KF
#undef VF
    {
        bool bad = !(l < AT_BIG);
#pragma unroll
        for (int r = 0; r < 16; ++r) bad = bad || !(fabsf(o0[r]) < INFINITY) || !(fabsf(o1[r]) < INFINITY);
        if (__builtin_expect(__any(bad), 0)) {
            o0 = (f32x16){}; o1 = (f32x16){}; l = 0.f; float mrun = -INFINITY;
            for (int kt = 0; kt < TPB; ++kt) {
                if (((kt >> 1) & 1) != g) continue;
                const u32x4* kg = kv + (size_t)kt * 512 + lane;
                f32x16 sc = (f32x16){};
#pragma unroll
                for (int f = 0; f < 4; ++f) sc = MFMA(kg[f * 64], q[f], sc);
                float mx = sc[0];
#pragma unroll
                for (int r = 1; r < 16; ++r) mx = fmaxf(mx, sc[r]);
                mx = fmaxf(mx, xor32(mx));
                const float mn = fmaxf(mrun, mx), f = EX2(mrun - mn);
                mrun = mn; l *= f;
#pragma unroll
                for (int r = 0; r < 16; ++r) { o0[r] *= f; o1[r] *= f; sc[r] = EX2(sc[r] - mn); l += sc[r]; }
                const u32x4 p0 = pack8(sc, 0), p1 = pack8(sc, 1);
                o0 = MFMA(kg[256], p0, o0); o1 = MFMA(kg[256 + 128], p0, o1);
                o0 = MFMA(kg[256 + 64], p1, o0); o1 = MFMA(kg[256 + 192], p1, o1);
            }
            negm = -mrun;
        }
    }
    l += xor32(l);
    float* cf = (float*)lds;
    if (g == 1) {
        float* dst = cf + qi * 34 * 64 + lane;
#pragma unroll
        for (int r = 0; r < 16; ++r) { dst[r * 64] = o0[r]; dst[(16 + r) * 64] = o1[r]; }
        dst[32 * 64] = negm; dst[33 * 64] = l;
    }
    __syncthreads();
    if (g == 0) {
        const float* s = cf + qi * 34 * 64 + lane;
        const float negm1 = s[32 * 64], l1 = s[33 * 64];
        const float nm = fminf(negm, negm1);
        const float f0 = __builtin_amdgcn_exp2f(nm - negm), f1 = __builtin_amdgcn_exp2f(nm - negm1);
        const float inv = __builtin_amdgcn_rcpf(l * f0 + l1 * f1);
        const float c0 = f0 * inv, c1 = f1 * inv;
#pragma unroll
        for (int r = 0; r < 16; ++r) { o0[r] = o0[r] * c0 + s[r * 64] * c1; o1[r] = o1[r] * c0 + s[(16 + r) * 64] * c1; }
        if (qvalid) {
            const int b = bh >> 2, hd = bh & 3;
            u32x4* ao = ws + WS_AT + ((size_t)(b * TPB + qt) * 16 + 4 * hd) * 64 + lane;
            const u32x4 r0 = pack8(o0, 0), r1 = pack8(o0, 1), r2 = pack8(o1, 0), r3 = pack8(o1, 1);
            asm volatile("global_store_dwordx4 %0, %1, off sc1\n\tglobal_store_dwordx4 %0, %2, off offset:1024 sc1\n\t"
                         "global_store_dwordx4 %0, %3, off offset:2048 sc1\n\tglobal_store_dwordx4 %0, %4, off offset:3072 sc1\n\t"
                         "s_waitcnt vmcnt(0)" :: "v"(ao), "v"(r0), "v"(r1), "v"(r2), "v"(r3) : "memory");
            if (lane == 0) __hip_atomic_fetch_add((unsigned*)(ws + WS_FL) + (b * TPB + qt) * 16, 1u, __ATOMIC_RELAXED, __HIP_MEMORY_SCOPE_AGENT);
        }
    }
    {
        const u32x4* wp = ws_in + WS_WC + (size_t)w * NFC * 64 + lane;
        LD8(0); LD8(8); LD8(16); LD8(24);
    }
}

DI void stageC_body(const u32x4* ws, const uint16_t* __restrict__ dw, uint16_t* __restrict__ out, u32x4 (&ring)[32], u32x4* lds) {
    u32x4* fx = lds;
    u32x4* fy = lds + 16 * 64;
    u32x4* fh = lds + 32 * 64;
    float* st0 = (float*)(lds + 32 * 64 + 64 * 64);
    float* st1 = st0 + 512;
    float* st2 = st0 + 1024;
    u32x4* dwl = lds + 32 * 64 + 64 * 64 + 384;
    const int tile = blockIdx.x, tid = threadIdx.x, lane = tid & 63, h = lane >> 5, m = lane & 31;
    const int w = __builtin_amdgcn_readfirstlane(tid >> 6);

#define MFMA16(a, b, c) __builtin_amdgcn_mfma_f32_16x16x32_bf16(__builtin_bit_cast(bf16x8, (a)), __builtin_bit_cast(bf16x8, (b)), (c), 0, 0, 0)
    const int tt = tile % TPB;
    const int c16 = lane & 15, g4 = lane >> 4, hm = lane & 3;
    int toff = hm < 2 ? hm - 2 : 30 + hm;
    const int tl = tt * 32 + toff;
    const bool hvalid = (tl >= 0) && (tl < T_SEQ);
    if (!hvalid) toff = 0;
    const int gtok = tile * 32 + toff;
    u32x4* hal = fy;
    const u32x4* hrd = (c16 < 8 && (c16 >> 2) == (g4 & 1)) ? hal + hm * 32 + (g4 >> 1) : hal + 128;

    u32x4 in[4]; u32x2 xh;
    {
        const u32x4* x1 = ws + WS_X1;
        const size_t mb = (size_t)tile * 16 * 64 + lane;
        in[2] = x1[mb + (2 * w) * 64]; in[3] = x1[mb + (2 * w + 1) * 64];
        xh = *(const u32x2*)((const char*)(x1 + ((size_t)(gtok >> 5) * 16 + 2 * w + ((c16 >> 2) & 1)) * 64 + (g4 & 1) * 32 + (gtok & 31)) + 8 * (g4 >> 1));
    }
    u32x4 dwr;
    if (tid < 160) dwr = ((const u32x4*)dw)[tid];
    constexpr int NFX = NFC;
    const u32x4* wp = ws + WS_WC + (size_t)w * NFC * 64 + lane;
    if (w == 0) {
        const int tn = tt + lane - 1;
        const bool need = lane < 3 && tn >= 0 && tn < TPB;
        const unsigned* fl = (const unsigned*)(ws + WS_FL) + (need ? tile + lane - 1 : tile) * 16;
        for (unsigned spin = 0; spin < (1u << 24); ++spin) {
            const unsigned v = __hip_atomic_load(fl, __ATOMIC_RELAXED, __HIP_MEMORY_SCOPE_AGENT);
            if (__all(v >= 4u)) break;
            __builtin_amdgcn_s_sleep(1);
        }
    }
    __syncthreads();
    u32x4 hin;
    {
        const __amdgpu_buffer_rsrc_t rs = __builtin_amdgcn_make_buffer_rsrc((void*)(ws + WS_AT), 0, NTILE * 16 * 1024, 0x00020000);
        const int mo = (tile * 16 * 64 + lane + 2 * w * 64) * 16;
        in[0] = __builtin_bit_cast(u32x4, __builtin_amdgcn_raw_buffer_load_b128(rs, mo, 0, 16));
        in[1] = __builtin_bit_cast(u32x4, __builtin_amdgcn_raw_buffer_load_b128(rs, mo + 1024, 0, 16));
        const int ho = (((gtok >> 5) * 16 + 2 * w + ((lane >> 3) & 1)) * 64 + ((lane >> 2) & 1) * 32 + (gtok & 31)) * 16;
        hin = __builtin_bit_cast(u32x4, __builtin_amdgcn_raw_buffer_load_b128(rs, ho, 0, 16));
    }
    f32x16 xv; f32x4 yv;
    fx[(2 * w) * 64 + lane] = in[0]; fx[(2 * w + 1) * 64 + lane] = in[1];
    if (lane < 16) hal[hm * 32 + (2 * w + ((lane >> 3) & 1)) * 2 + ((lane >> 2) & 1)] = hin;
    if (tid >= 480) hal[128 + tid - 480] = (u32x4){0u, 0u, 0u, 0u};
    if (tid < 160) dwl[tid] = dwr;
    unpack16(xv, in[2], in[3]);
    yv = (f32x4){bflo(xh[0]), bfhi(xh[0]), bflo(xh[1]), bfhi(xh[1])};
    __syncthreads();
    {
        f32x16 a = (f32x16){}; f32x4 b = (f32x4){0.f, 0.f, 0.f, 0.f};
        u32x4 bx[2], by[2];
        bx[0] = fx[lane]; by[0] = hrd[0]; bx[1] = fx[64 + lane]; by[1] = hrd[2];
#pragma unroll
        for (int ks = 0; ks < 16; ++ks) {
            a = MFMA(ring[ks & 31], bx[ks & 1], a);
            b = MFMA16(ring[ks & 31], by[ks & 1], b);
            if (ks + 2 < 16) { bx[ks & 1] = fx[(ks + 2) * 64 + lane]; by[ks & 1] = hrd[2 * (ks + 2)]; }
            REFILL(ks, 0);
            SB();
        }
#pragma unroll
        for (int r = 0; r < 16; ++r) xv[r] = rbf(xv[r] + rbf(a[r]));
#pragma unroll
        for (int r = 0; r < 4; ++r) yv[r] = rbf(yv[r] + rbf(b[r]));
    }
    float sc, sh;
    ln_partial(xv, st0, w, lane);
    {
        float s_ = (yv[0] + yv[1]) + (yv[2] + yv[3]), q_ = (yv[0] * yv[0] + yv[1] * yv[1]) + (yv[2] * yv[2] + yv[3] * yv[3]);
        s_ += __shfl_xor(s_, 4); q_ += __shfl_xor(q_, 4);
        s_ += __shfl_xor(s_, 16); q_ += __shfl_xor(q_, 16);
        s_ += __shfl_xor(s_, 32); q_ += __shfl_xor(q_, 32);
        if (lane < 4) { st1[w * 8 + lane] = s_; st1[w * 8 + 4 + lane] = q_; }
    }
    __syncthreads();
    ln_finish(st0, lane, sc, sh);
    norm_store(xv, sc, sh, fx, w, lane);
    {
        float s_ = 0.f, q_ = 0.f;
#pragma unroll
        for (int i = 0; i < 8; ++i) { s_ += st1[i * 8 + hm]; q_ += st1[i * 8 + 4 + hm]; }
        const float m_ = s_ * (1.0f / 256.0f);
        const float sc2 = __builtin_amdgcn_rsqf(fmaxf(q_ * (1.0f / 256.0f) - m_ * m_, 0.f) + 1e-5f), sh2 = -m_ * sc2;
        const u32x2 o_ = {pack2(__builtin_fmaf(yv[0], sc2, sh2), __builtin_fmaf(yv[1], sc2, sh2)), pack2(__builtin_fmaf(yv[2], sc2, sh2), __builtin_fmaf(yv[3], sc2, sh2))};
        if (c16 < 8) *(u32x2*)((char*)(hal + hm * 32 + (2 * w + (c16 >> 2)) * 2 + (g4 & 1)) + 8 * (g4 >> 1)) = o_;
    }
    __syncthreads();
    {
        f32x16 a = (f32x16){}, b = (f32x16){}; f32x4 c = (f32x4){0.f, 0.f, 0.f, 0.f}, d = c;
        u32x4 bxn = fx[lane], byn = hrd[0];
#pragma unroll
        for (int ks = 0; ks < 16; ++ks) {
            const int F = 16 + 2 * ks;
            const u32x4 bx = bxn, by = byn;
            if (ks + 1 < 16) { bxn = fx[(ks + 1) * 64 + lane]; byn = hrd[2 * (ks + 1)]; }
            a = MFMA(ring[F & 31], bx, a); c = MFMA16(ring[F & 31], by, c); REFILL(F, 0);
            b = MFMA(ring[(F + 1) & 31], bx, b); d = MFMA16(ring[(F + 1) & 31], by, d); REFILL(F + 1, 0);
            SB();
        }
#pragma unroll
        for (int r = 0; r < 16; ++r) a[r] = a[r] * sigmoidf_(b[r]);
#pragma unroll
        for (int r = 0; r < 4; ++r) c[r] = hvalid ? c[r] * sigmoidf_(d[r]) : 0.f;
        f32x4* cs = (f32x4*)fh + w * (4 * 2 * 36);
#pragma unroll
        for (int g = 0; g < 4; ++g) cs[(g * 2 + h) * 36 + m + 2] = (f32x4){a[4 * g], a[4 * g + 1], a[4 * g + 2], a[4 * g + 3]};
        if (c16 < 8) cs[((2 * (c16 >> 2) + (g4 >> 1)) * 2 + (g4 & 1)) * 36 + (hm < 2 ? hm : 32 + hm)] = c;
        __syncthreads();
        const uint16_t* dp = (const uint16_t*)dwl + 32 * w + 4 * h;
#pragma unroll
        for (int g = 0; g < 4; ++g) {
            f32x4 acc = (f32x4){0.f, 0.f, 0.f, 0.f};
#pragma unroll
            for (int i = 0; i < 5; ++i) {
                const f32x4 hv = cs[(g * 2 + h) * 36 + m + i];
                const u32x2 t = *(const u32x2*)(dp + i * DM + 8 * g);
                acc[0] += hv[0] * bflo(t[0]); acc[1] += hv[1] * bfhi(t[0]); acc[2] += hv[2] * bflo(t[1]); acc[3] += hv[3] * bfhi(t[1]);
            }
#pragma unroll
            for (int i = 0; i < 4; ++i) a[4 * g + i] = siluf_(acc[i]);
        }
        fx[(2 * w) * 64 + lane] = pack8(a, 0);
        fx[(2 * w + 1) * 64 + lane] = pack8(a, 1);
    }
    __syncthreads();
    {
        f32x16 a;
        GEMM16(a, 48, 64, fx, false);
#pragma unroll
        for (int r = 0; r < 16; ++r) xv[r] = rbf(xv[r] + rbf(a[r]));
    }
    ln_partial(xv, st2, w, lane);
    __syncthreads();
    ln_finish(st2, lane, sc, sh);
    norm_store(xv, sc, sh, fx, w, lane);
    __syncthreads();
    {
        f32x16 acc;
        FFN(acc, 64);
#pragma unroll
        for (int r = 0; r < 16; ++r) xv[r] = rbf(__builtin_fmaf(0.5f, rbf(acc[r]), xv[r]));
    }
    ln_partial(xv, st0, w, lane);
    __syncthreads();
    ln_finish(st0, lane, sc, sh);
    {
        uint16_t* op = out + (size_t)(tile * 32 + m) * DM + 32 * w + 4 * h;
#pragma unroll
        for (int g = 0; g < 4; ++g) {
            u32x2 t;
            t[0] = pack2(__builtin_fmaf(xv[4 * g], sc, sh), __builtin_fmaf(xv[4 * g + 1], sc, sh));
            t[1] = pack2(__builtin_fmaf(xv[4 * g + 2], sc, sh), __builtin_fmaf(xv[4 * g + 3], sc, sh));
            *(u32x2*)(op + 8 * g) = t;
        }
    }
}

__global__ __launch_bounds__(512, 2) void k_attnC(u32x4* ws, const uint16_t* __restrict__ dw, uint16_t* __restrict__ out) {
    __shared__ u32x4 lds[9 * 1024];
    u32x4 ring[32];
    attn_body(ws, ws, ring, lds);
    if (blockIdx.x >= NTILE) return;
    __syncthreads();
    stageC_body(ws, dw, out, ring, lds);
}

extern "C" void kernel_launch(void* const* d_in, const int* in_sizes, int n_in,
                              void* d_out, int out_size, void* d_ws, size_t ws_size,
                              hipStream_t stream) {
    PackArgs p;
    p.w1a = (const uint16_t*)d_in[1]; p.w1b = (const uint16_t*)d_in[2]; p.wqkv = (const uint16_t*)d_in[3]; p.wo = (const uint16_t*)d_in[4];
    p.wp1 = (const uint16_t*)d_in[5]; p.wp2 = (const uint16_t*)d_in[7]; p.w2a = (const uint16_t*)d_in[8]; p.w2b = (const uint16_t*)d_in[9];
    p.ws = (u32x4*)d_ws; p.x = (const u32x4*)d_in[0];
    u32x4* ws = (u32x4*)d_ws;
    const int nchunk = 8 * (NFA + NFC) * 64;
    k_pack<<<nchunk / 256 + 1000, 256, 0, stream>>>(p);
    k_stageA<<<NTILE, 512, 0, stream>>>((const uint16_t*)d_in[0], ws);
    k_attnC<<<256, 512, 0, stream>>>(ws, (const uint16_t*)d_in[6], (uint16_t*)d_out);
}
```

```cpp
#include <hip/hip_runtime.h>
#include <hip/hip_bf16.h>
#include <stdint.h>


typedef short bf16x8 __attribute__((ext_vector_type(8)));
typedef float f32x16 __attribute__((ext_vector_type(16)));
typedef float f32x2 __attribute__((ext_vector_type(2)));
typedef float f32x4 __attribute__((ext_vector_type(4)));
typedef unsigned u32x4 __attribute__((ext_vector_type(4)));
typedef unsigned u32x2 __attribute__((ext_vector_type(2)));
typedef __bf16 bf16v2 __attribute__((ext_vector_type(2)));

#define DI __device__ __forceinline__
#define SB() __builtin_amdgcn_sched_barrier(0)
#define MFMA(a, b, c) __builtin_amdgcn_mfma_f32_32x32x16_bf16(__builtin_bit_cast(bf16x8, (a)), __builtin_bit_cast(bf16x8, (b)), (c), 0, 0, 0)

constexpr int T_SEQ = 4000, DM = 256, NTOK = 8000, NTILE = 250, TPB = 125;
constexpr int NFA = 176;
constexpr int NFC = 192;

constexpr size_t WS_WA = 0;
constexpr size_t WS_WC = WS_WA + (size_t)8 * NFA * 64;
constexpr size_t WS_X1 = WS_WC + (size_t)8 * NFC * 64;
constexpr size_t WS_Q  = WS_X1 + (size_t)NTILE * 16 * 64;
constexpr size_t WS_KV = WS_Q + (size_t)8 * TPB * 4 * 64;
constexpr size_t WS_AT = WS_KV + (size_t)8 * TPB * 8 * 64 + 4096;
constexpr size_t WS_FL = WS_AT + (size_t)NTILE * 16 * 64;

DI unsigned pack2(float lo, float hi) {
    f32x2 v = {lo, hi};
    bf16v2 r = __builtin_convertvector(v, bf16v2);
    return __builtin_bit_cast(unsigned, r);
}
DI float bflo(unsigned u) { return __uint_as_float(u << 16); }
DI float bfhi(unsigned u) { return __uint_as_float(u & 0xffff0000u); }
DI u32x4 pack8(const f32x16& a, int s) {
    u32x4 r;
    r[0] = pack2(a[8 * s + 0], a[8 * s + 1]);
    r[1] = pack2(a[8 * s + 2], a[8 * s + 3]);
    r[2] = pack2(a[8 * s + 4], a[8 * s + 5]);
    r[3] = pack2(a[8 * s + 6], a[8 * s + 7]);
    return r;
}
DI void unpack16(f32x16& a, const u32x4& f0, const u32x4& f1) {
#pragma unroll
    for (int i = 0; i < 4; ++i) {
        a[2 * i] = bflo(f0[i]); a[2 * i + 1] = bfhi(f0[i]);
        a[8 + 2 * i] = bflo(f1[i]); a[8 + 2 * i + 1] = bfhi(f1[i]);
    }
}
DI float xor32(float v) { return __shfl_xor(v, 32); }
DI float sigmoidf_(float x) { return __builtin_amdgcn_rcpf(1.0f + __builtin_amdgcn_exp2f(-1.4426950408889634f * x)); }
DI float siluf_(float x) { return x * sigmoidf_(x); }

struct PackArgs {
    const uint16_t* w1a; const uint16_t* w1b; const uint16_t* wqkv; const uint16_t* wo;
    const uint16_t* wp1; const uint16_t* wp2; const uint16_t* w2a; const uint16_t* w2b;
    u32x4* ws;
};

__global__ __launch_bounds__(256) void k_pack(PackArgs p) {
    const int gid = blockIdx.x * 256 + threadIdx.x;
    const int lane = gid & 63, fg = gid >> 6;
    if (gid < NTILE * 4) p.ws[WS_FL + gid] = (u32x4){0u, 0u, 0u, 0u};
    if (fg >= 8 * (NFA + NFC)) return;
    const uint16_t* W; int N, nt, ks;
    if (fg < 8 * NFA) {
        const int w = fg / NFA, f = fg % NFA;
        if (f < 64)       { W = p.w1a; N = 1024; ks = f & 15; nt = 4 * w + (f >> 4); }
        else if (f < 128) { W = p.w1b; N = 256; ks = f - 64; nt = w; }
        else              { const int g = f - 128; W = p.wqkv; N = 768; ks = g & 15; nt = w + 8 * (g >> 4); }
    } else {
        const int fg2 = fg - 8 * NFA, w = fg2 / NFC, f = fg2 % NFC;
        if (f < 16)       { W = p.wo; N = 256; ks = f; nt = w; }
        else if (f < 48)  { const int g = f - 16; W = p.wp1; N = 512; ks = g >> 1; nt = w + 8 * (g & 1); }
        else if (f < 64)  { W = p.wp2; N = 256; ks = f - 48; nt = w; }
        else if (f < 128) { const int g = f - 64; W = p.w2a; N = 1024; ks = g & 15; nt = 4 * w + (g >> 4); }
        else              { W = p.w2b; N = 256; ks = f - 128; nt = w; }
    }
    const int r = lane & 31, h = lane >> 5;
    const uint16_t* src = W + (size_t)(16 * ks + 4 * h) * N + 32 * nt + r;
    unsigned e[8];
#pragma unroll
    for (int j = 0; j < 8; ++j) e[j] = src[(size_t)(8 * (j >> 2) + (j & 3)) * N];
    u32x4 o;
#pragma unroll
    for (int i = 0; i < 4; ++i) o[i] = e[2 * i] | (e[2 * i + 1] << 16);
    p.ws[gid] = o;
}

DI void glds16(const void* g, unsigned lds_base) {
    unsigned sv;
    asm volatile("s_mov_b32 %0, m0\n\ts_mov_b32 m0, %2\n\ts_nop 0\n\tglobal_load_lds_dwordx4 %1, off\n\ts_mov_b32 m0, %0"
                 : "=&s"(sv) : "v"(g), "s"(lds_base) : "memory");
}
#define WLOAD(F) ring[(F) & 31] = wp[(F) * 64]
#define REFILL(F, PE) do { if ((F) < 16 && NFX == NFA) WLOAD((F) + 16); if ((F) + 32 < (NFX)) WLOAD((F) + 32); } while (0)
#define LD8(F) do { SB(); _Pragma("unroll") for (int i_ = 0; i_ < 8; ++i_) WLOAD((F) + i_); SB(); } while (0)
#define RAWBAR() do { SB(); __builtin_amdgcn_s_barrier(); SB(); } while (0)

DI void ln_partial(const f32x16& v, float* st, int w, int lane) {
    float s = 0.f, q = 0.f;
#pragma unroll
    for (int i = 0; i < 16; ++i) { s += v[i]; q = __builtin_fmaf(v[i], v[i], q); }
    auto r = __builtin_amdgcn_permlane32_swap(__float_as_uint(s), __float_as_uint(q), false, false);
    st[w * 64 + lane] = __uint_as_float(r[0]) + __uint_as_float(r[1]);
}
DI void ln_finish(const float* st, int lane, float& scale, float& shift) {
    const float* p = st + (lane & 31);
    float s = 0.f, q = 0.f;
#pragma unroll
    for (int i = 0; i < 8; ++i) { s += p[i * 64]; q += p[i * 64 + 32]; }
    const float m = s * (1.0f / 256.0f);
    const float var = fmaxf(q * (1.0f / 256.0f) - m * m, 0.f);
    scale = __builtin_amdgcn_rsqf(var + 1e-5f);
    shift = -m * scale;
}
DI void norm_store(const f32x16& v, float scale, float shift, u32x4* dst  , int w, int lane) {
    f32x16 n;
#pragma unroll
    for (int i = 0; i < 16; ++i) n[i] = __builtin_fmaf(v[i], scale, shift);
    dst[(2 * w) * 64 + lane] = pack8(n, 0);
    dst[(2 * w + 1) * 64 + lane] = pack8(n, 1);
}
DI float rbf(float x) { return bflo(pack2(x, 0.f) & 0xffffu); }

#define GEMM16(acc, F0, PE, fxp, SWAP) do { \
        acc = (f32x16){}; u32x4 bq_[4]; \
        _Pragma("unroll") for (int i_ = 0; i_ < 4; ++i_) bq_[i_] = (fxp)[i_ * 64 + lane]; \
        _Pragma("unroll") for (int ks_ = 0; ks_ < 16; ++ks_) { \
            if (SWAP) acc = MFMA(bq_[ks_ & 3], ring[((F0) + ks_) & 31], acc); else acc = MFMA(ring[((F0) + ks_) & 31], bq_[ks_ & 3], acc); \
            if (ks_ + 4 < 16) bq_[ks_ & 3] = (fxp)[(ks_ + 4) * 64 + lane]; \
            REFILL((F0) + ks_, PE); SB(); } } while (0)

#define FFN(acc, F0) do { \
        _Pragma("unroll") for (int t_ = 0; t_ < 4; ++t_) { \
            f32x16 a_; GEMM16(a_, (F0) + 16 * t_, (F0) + 64, fx, false); \
            _Pragma("unroll") for (int r_ = 0; r_ < 8; ++r_) a_[r_] = siluf_(a_[r_]); \
            fh[((4 * w + t_) * 2 + 0) * 64 + lane] = pack8(a_, 0); \
            _Pragma("unroll") for (int r_ = 8; r_ < 16; ++r_) a_[r_] = siluf_(a_[r_]); \
            fh[((4 * w + t_) * 2 + 1) * 64 + lane] = pack8(a_, 1); } \
        __syncthreads(); \
        acc = (f32x16){}; u32x4 bq_[4]; \
        _Pragma("unroll") for (int i_ = 0; i_ < 4; ++i_) bq_[i_] = fh[i_ * 64 + lane]; \
        _Pragma("unroll") for (int ks_ = 0; ks_ < 64; ++ks_) { \
            acc = MFMA(ring[((F0) + 64 + ks_) & 31], bq_[ks_ & 3], acc); \
            if (ks_ + 4 < 64) bq_[ks_ & 3] = fh[(ks_ + 4) * 64 + lane]; \
            REFILL((F0) + 64 + ks_, (F0) + 128); SB(); } } while (0)

__global__ __launch_bounds__(512, 2) void k_stageA(const uint16_t* __restrict__ x, u32x4* __restrict__ ws) {
    __shared__ u32x4 lds[16 * 64 + 64 * 64 + 256 + 512];
    u32x4* fx = lds;
    u32x4* fh = lds + 16 * 64;
    float* st0 = (float*)(lds + 16 * 64 + 64 * 64);
    float* st1 = st0 + 512;
    const int tile = blockIdx.x, tid = threadIdx.x, lane = tid & 63, h = lane >> 5, m = lane & 31;
    const int w = __builtin_amdgcn_readfirstlane(tid >> 6);
    u32x2 xr[4];
    {
        const uint16_t* xp = x + (size_t)(tile * 32 + m) * DM + 32 * w + 4 * h;
#pragma unroll
        for (int g = 0; g < 4; ++g) xr[g] = *(const u32x2*)(xp + 8 * g);
    }
    RAWBAR();
    constexpr int NFX = NFA;
    const u32x4* wp = ws + WS_WA + (size_t)w * NFA * 64 + lane;
    u32x4 ring[32];
    LD8(0); LD8(8);
    f32x16 xv;
#pragma unroll
    for (int g = 0; g < 4; ++g) { xv[4 * g + 0] = bflo(xr[g][0]); xv[4 * g + 1] = bfhi(xr[g][0]); xv[4 * g + 2] = bflo(xr[g][1]); xv[4 * g + 3] = bfhi(xr[g][1]); }
    float sc, sh;
    ln_partial(xv, st0, w, lane);
    __syncthreads();
    ln_finish(st0, lane, sc, sh);
    norm_store(xv, sc, sh, fx, w, lane);
    __syncthreads();
    {
        f32x16 acc;
        FFN(acc, 0);
#pragma unroll
        for (int r = 0; r < 16; ++r) xv[r] = rbf(__builtin_fmaf(0.5f, rbf(acc[r]), xv[r]));
    }
    ln_partial(xv, st1, w, lane);
    {
        u32x4* xo = ws + WS_X1 + (size_t)tile * 16 * 64;
        xo[(2 * w) * 64 + lane] = pack8(xv, 0);
        xo[(2 * w + 1) * 64 + lane] = pack8(xv, 1);
    }
    __syncthreads();
    ln_finish(st1, lane, sc, sh);
    norm_store(xv, sc, sh, fx, w, lane);
    __syncthreads();
    {
        const int b = tile / TPB, tt = tile % TPB, bh = b * 4 + (w >> 1), t2 = w & 1;
        u32x4* qo = ws + WS_Q + ((size_t)(bh * TPB + tt) * 4 + 2 * t2) * 64 + lane;
        u32x4* ko = ws + WS_KV + ((size_t)(bh * TPB + tt) * 8 + 2 * t2) * 64 + lane;
        f32x16 a;
        GEMM16(a, 128, NFA, fx, false);
        const float qs = 0.125f * 1.4426950408889634f;
#pragma unroll
        for (int r = 0; r < 16; ++r) a[r] *= qs;
        qo[0] = pack8(a, 0); qo[64] = pack8(a, 1);
        GEMM16(a, 144, NFA, fx, false);
        ko[0] = pack8(a, 0); ko[64] = pack8(a, 1);
        GEMM16(a, 160, NFA, fx, true);
        ko[4 * 64] = pack8(a, 0); ko[5 * 64] = pack8(a, 1);
    }
}

#define WAIT_BAR(N) asm volatile("s_waitcnt vmcnt(" #N ") lgkmcnt(0)\n\ts_barrier" ::: "memory")

constexpr float AT_BIG = 1.0e30f;
#define PIN(x) asm volatile("" : "+v"(x))
#define EX2(x) __builtin_amdgcn_exp2f(x)

DI void attn_body(const u32x4* ws_in, u32x4* ws, u32x4 (&ring)[32], u32x4* lds  ) {
    const int tid = threadIdx.x, lane = tid & 63;
    const int w = __builtin_amdgcn_readfirstlane(tid >> 6);
    const int bh = blockIdx.x & 7, qb = blockIdx.x >> 3, qi = w & 3, g = w >> 2;
    int qt = 4 * qb + qi; const bool qvalid = qt < TPB; if (!qvalid) qt = TPB - 1;
    const u32x4* kv = ws_in + WS_KV + (size_t)bh * TPB * 8 * 64;
    const unsigned lds0 = (unsigned)(uintptr_t)lds;
    const u32x4* src = kv + (w >> 1) * 512 + (w & 1) * 128 + lane;
    const u32x4* src31 = kv + 124 * 512 + (w & 1) * 128 + lane;
    const unsigned dstk = lds0 + (w >> 1) * 4096 + (w & 1) * 2048;
    const u32x4* srcw = kv + qi * 512 + lane;
    const u32x4* srcw31 = kv + 124 * 512 + lane;
    const unsigned dstw = lds0 + qi * 4096;
#define DMA2(sp, dst) do { glds16((sp), (unsigned)__builtin_amdgcn_readfirstlane(dst)); glds16((sp) + 64, (unsigned)__builtin_amdgcn_readfirstlane((dst) + 1024)); } while (0)
#define DMA_K(step) do { const int st_ = (step); const u32x4* s_ = st_ < 31 ? src + (size_t)st_ * 2048 : src31; DMA2(s_, dstk + (st_ & 3) * 16384); } while (0)
#define DMA_V(step) do { const int st_ = (step); const u32x4* s_ = st_ < 31 ? src + (size_t)st_ * 2048 : src31; DMA2(s_ + 256, dstk + (st_ % 5) * 16384 + 65536); } while (0)
    DMA_K(0); DMA_V(0); DMA_K(1);
    u32x4 q[4];
    {
        const u32x4* qp = ws_in + WS_Q + (size_t)(bh * TPB + qt) * 4 * 64 + lane;
#pragma unroll
        for (int f = 0; f < 4; ++f) q[f] = qp[f * 64];
    }
    DMA_K(2); DMA_V(1);
    const char* lbase = (const char*)lds + (2 * g) * 4096 + lane * 16;
#define KF(off, i) (*(const u32x4*)(lbase + (off) + (i) * 1024))
#define VF(off, i) (*(const u32x4*)(lbase + (off) + 65536 + (i) * 1024))
    f32x16 o0 = (f32x16){}, o1 = (f32x16){};
    float negm;
    f32x4 lsum = (f32x4){0.f, 0.f, 0.f, 0.f};
    u32x4 onesA;
    { const unsigned o_ = ((lane & 15) == ((lane >> 4) & 1)) ? 0x3f803f80u : 0u; onesA = (u32x4){o_, o_, o_, o_}; }
    f32x16 cm, sA, sB;
    u32x4 kf[4], vf[4], pA0, pA1, pB0, pB1;
    WAIT_BAR(4);
    {
#pragma unroll
        for (int i = 0; i < 4; ++i) kf[i] = KF(0, i);
        const f32x16 z = (f32x16){};
        sA = MFMA(kf[0], q[0], z); sA = MFMA(kf[1], q[1], sA); sA = MFMA(kf[2], q[2], sA); sA = MFMA(kf[3], q[3], sA);
#pragma unroll
        for (int i = 0; i < 4; ++i) kf[i] = KF(4096, i);
        float mx = sA[0];
#pragma unroll
        for (int r = 1; r < 16; ++r) mx = fmaxf(mx, sA[r]);
        mx = fmaxf(mx, xor32(mx));
        negm = -mx;
#pragma unroll
        for (int r = 0; r < 16; ++r) { sA[r] += negm; cm[r] = negm; sB[r] = 0.f; }
        pB0 = (u32x4){0u, 0u, 0u, 0u}; pB1 = pB0;
    }
    WAIT_BAR(0);
#define FIN(S, i, PK, e) do { PK[e] = pack2(S[i], S[(i) + 1]); PIN(PK); } while (0)
#define EXP2(S, i) do { S[i] = EX2(S[i]); S[(i) + 1] = EX2(S[(i) + 1]); PIN(S); } while (0)
#define LSUM(PK) do { lsum = __builtin_amdgcn_mfma_f32_16x16x32_bf16(__builtin_bit_cast(bf16x8, onesA), __builtin_bit_cast(bf16x8, PK), lsum, 0, 0, 0); PIN(lsum); } while (0)
#define PERIOD(SC, SN, PP0, PP1, PC0, PC1, koff, voff, D0, D1) do { \
        vf[0] = VF(voff, 0); FIN(SN, 14, PP1, 3); SB(); SN = MFMA(kf[0], q[0], cm); PIN(SN); EXP2(SC, 0); SB(); \
        vf[2] = VF(voff, 2); SB(); SN = MFMA(kf[1], q[1], SN); PIN(SN); EXP2(SC, 2); FIN(SC, 0, PC0, 0); SB(); \
        vf[1] = VF(voff, 1); SB(); SN = MFMA(kf[2], q[2], SN); PIN(SN); EXP2(SC, 4); FIN(SC, 2, PC0, 1); SB(); D0; SB(); \
        vf[3] = VF(voff, 3); SB(); SN = MFMA(kf[3], q[3], SN); PIN(SN); EXP2(SC, 6); FIN(SC, 4, PC0, 2); SB(); \
        kf[0] = KF(koff, 0); SB(); o0 = MFMA(vf[0], PP0, o0); PIN(o0); EXP2(SC, 8); FIN(SC, 6, PC0, 3); SB(); LSUM(PP0); SB(); \
        kf[1] = KF(koff, 1); SB(); o1 = MFMA(vf[2], PP0, o1); PIN(o1); EXP2(SC, 10); FIN(SC, 8, PC1, 0); SB(); \
        kf[2] = KF(koff, 2); SB(); o0 = MFMA(vf[1], PP1, o0); PIN(o0); EXP2(SC, 12); FIN(SC, 10, PC1, 1); SB(); LSUM(PP1); SB(); D1; SB(); \
        kf[3] = KF(koff, 3); SB(); o1 = MFMA(vf[3], PP1, o1); PIN(o1); EXP2(SC, 14); FIN(SC, 12, PC1, 2); SB(); \
    } while (0)
    int vm = 4096, v0 = 0, v1 = 16384;
#define G1(p, d, n) do { if (g == 0) glds16((p) + (n) * 64, (unsigned)__builtin_amdgcn_readfirstlane((d) + (n) * 1024)); } while (0)
#define NEXT5(v) ((v) >= 4 * 16384 ? 0 : (v) + 16384)
    for (int s0 = 0; s0 < 30; s0 += 2) {
        const int k1 = ((s0 + 1) & 3) * 16384, k2 = ((s0 + 2) & 3) * 16384;
        const int v2 = NEXT5(v1), v3 = NEXT5(v2);
        const int sk4 = s0 + 4 < 32 ? s0 + 4 : 31;
        const u32x4* pa_ = s0 + 3 < 31 ? srcw + (size_t)(s0 + 3) * 2048 : srcw31;
        const u32x4* pb_ = srcw + (size_t)(s0 + 2) * 2048 + 256;
        const u32x4* pc_ = sk4 < 31 ? srcw + (size_t)sk4 * 2048 : srcw31;
        const u32x4* pd_ = pa_ + 256;
        const unsigned da_ = dstw + ((s0 + 3) & 3) * 16384, db_ = dstw + 65536 + v2, dc_ = dstw + (sk4 & 3) * 16384, dd_ = dstw + 65536 + v3;
        SB();
        PERIOD(sA, sB, pB0, pB1, pA0, pA1, k1, vm, { G1(pa_, da_, 0); G1(pa_, da_, 1); G1(pa_, da_, 2); }, { G1(pa_, da_, 3); G1(pb_, db_, 0); G1(pb_, db_, 1); });
        PERIOD(sB, sA, pA0, pA1, pB0, pB1, k1 + 4096, v0, { G1(pb_, db_, 2); G1(pb_, db_, 3); G1(pc_, dc_, 0); }, { G1(pc_, dc_, 1); G1(pc_, dc_, 2); G1(pc_, dc_, 3); });
        PERIOD(sA, sB, pB0, pB1, pA0, pA1, k2, v0 + 4096, { G1(pd_, dd_, 0); G1(pd_, dd_, 1); }, { G1(pd_, dd_, 2); G1(pd_, dd_, 3); });
        PERIOD(sB, sA, pA0, pA1, pB0, pB1, k2 + 4096, v1, (void)0, (void)0);
        vm = v1 + 4096; v0 = v2; v1 = v3;
        WAIT_BAR(0);
    }
    {
        PERIOD(sA, sB, pB0, pB1, pA0, pA1, 3 * 16384, vm, (void)0, (void)0);
        PERIOD(sB, sA, pA0, pA1, pB0, pB1, 3 * 16384 + 4096, v0, (void)0, (void)0);
        if (g == 1) {
#pragma unroll
            for (int r = 0; r < 16; ++r) sA[r] = -INFINITY;
        }
        PERIOD(sA, sB, pB0, pB1, pA0, pA1, 0, v0 + 4096, (void)0, (void)0);
#pragma unroll
        for (int r = 0; r < 16; ++r) sB[r] = -INFINITY;
        PERIOD(sB, sA, pA0, pA1, pB0, pB1, 0, v1, (void)0, (void)0);
    }
    float l;
    { const float la = __shfl(lsum[0], lane & 15), lb = __shfl(lsum[1], lane & 15); l = 0.5f * ((lane & 16) ? lb : la); }
    WAIT_BAR(0);
#undef DMA_K
#undef DMA_V
#undef DMA2
#undef PERIOD
#undef FIN
#undef EXP2
#undef LSUM
#undef G1
#undef NEXT5
#undef KF
#undef VF
    {
        bool bad = !(l < AT_BIG);
#pragma unroll
        for (int r = 0; r < 16; ++r) bad = bad || !(fabsf(o0[r]) < INFINITY) || !(fabsf(o1[r]) < INFINITY);
        if (__builtin_expect(__any(bad), 0)) {
            o0 = (f32x16){}; o1 = (f32x16){}; l = 0.f; float mrun = -INFINITY;
            for (int kt = 0; kt < TPB; ++kt) {
                if (((kt >> 1) & 1) != g) continue;
                const u32x4* kg = kv + (size_t)kt * 512 + lane;
                f32x16 sc = (f32x16){};
#pragma unroll
                for (int f = 0; f < 4; ++f) sc = MFMA(kg[f * 64], q[f], sc);
                float mx = sc[0];
#pragma unroll
                for (int r = 1; r < 16; ++r) mx = fmaxf(mx, sc[r]);
                mx = fmaxf(mx, xor32(mx));
                const float mn = fmaxf(mrun, mx), f = EX2(mrun - mn);
                mrun = mn; l *= f;
#pragma unroll
                for (int r = 0; r < 16; ++r) { o0[r] *= f; o1[r] *= f; sc[r] = EX2(sc[r] - mn); l += sc[r]; }
                const u32x4 p0 = pack8(sc, 0), p1 = pack8(sc, 1);
                o0 = MFMA(kg[256], p0, o0); o1 = MFMA(kg[256 + 128], p0, o1);
                o0 = MFMA(kg[256 + 64], p1, o0); o1 = MFMA(kg[256 + 192], p1, o1);
            }
            negm = -mrun;
        }
    }
    l += xor32(l);
    float* cf = (float*)lds;
    if (g == 1) {
        float* dst = cf + qi * 34 * 64 + lane;
#pragma unroll
        for (int r = 0; r < 16; ++r) { dst[r * 64] = o0[r]; dst[(16 + r) * 64] = o1[r]; }
        dst[32 * 64] = negm; dst[33 * 64] = l;
    }
    __syncthreads();
    if (g == 0) {
        const float* s = cf + qi * 34 * 64 + lane;
        const float negm1 = s[32 * 64], l1 = s[33 * 64];
        const float nm = fminf(negm, negm1);
        const float f0 = __builtin_amdgcn_exp2f(nm - negm), f1 = __builtin_amdgcn_exp2f(nm - negm1);
        const float inv = __builtin_amdgcn_rcpf(l * f0 + l1 * f1);
        const float c0 = f0 * inv, c1 = f1 * inv;
#pragma unroll
        for (int r = 0; r < 16; ++r) { o0[r] = o0[r] * c0 + s[r * 64] * c1; o1[r] = o1[r] * c0 + s[(16 + r) * 64] * c1; }
        if (qvalid) {
            const int b = bh >> 2, hd = bh & 3;
            u32x4* ao = ws + WS_AT + ((size_t)(b * TPB + qt) * 16 + 4 * hd) * 64 + lane;
            const u32x4 r0 = pack8(o0, 0), r1 = pack8(o0, 1), r2 = pack8(o1, 0), r3 = pack8(o1, 1);
            asm volatile("global_store_dwordx4 %0, %1, off sc1\n\tglobal_store_dwordx4 %0, %2, off offset:1024 sc1\n\t"
                         "global_store_dwordx4 %0, %3, off offset:2048 sc1\n\tglobal_store_dwordx4 %0, %4, off offset:3072 sc1\n\t"
                         "s_waitcnt vmcnt(0)" :: "v"(ao), "v"(r0), "v"(r1), "v"(r2), "v"(r3) : "memory");
            if (lane == 0) __hip_atomic_fetch_add((unsigned*)(ws + WS_FL) + (b * TPB + qt) * 16, 1u, __ATOMIC_RELAXED, __HIP_MEMORY_SCOPE_AGENT);
        }
    }
    {
        const u32x4* wp = ws_in + WS_WC + (size_t)w * NFC * 64 + lane;
        LD8(0); LD8(8); LD8(16); LD8(24);
    }
}

DI void stageC_body(const u32x4* ws, const uint16_t* __restrict__ dw, uint16_t* __restrict__ out, u32x4 (&ring)[32], u32x4* lds) {
    u32x4* fx = lds;
    u32x4* fy = lds + 16 * 64;
    u32x4* fh = lds + 32 * 64;
    float* st0 = (float*)(lds + 32 * 64 + 64 * 64);
    float* st1 = st0 + 512;
    float* st2 = st0 + 1024;
    u32x4* dwl = lds + 32 * 64 + 64 * 64 + 384;
    const int tile = blockIdx.x, tid = threadIdx.x, lane = tid & 63, h = lane >> 5, m = lane & 31;
    const int w = __builtin_amdgcn_readfirstlane(tid >> 6);

#define MFMA16(a, b, c) __builtin_amdgcn_mfma_f32_16x16x32_bf16(__builtin_bit_cast(bf16x8, (a)), __builtin_bit_cast(bf16x8, (b)), (c), 0, 0, 0)
    const int tt = tile % TPB;
    const int c16 = lane & 15, g4 = lane >> 4, hm = lane & 3;
    int toff = hm < 2 ? hm - 2 : 30 + hm;
    const int tl = tt * 32 + toff;
    const bool hvalid = (tl >= 0) && (tl < T_SEQ);
    if (!hvalid) toff = 0;
    const int gtok = tile * 32 + toff;
    u32x4* hal = fy;
    const u32x4* hrd = (c16 < 8 && (c16 >> 2) == (g4 & 1)) ? hal + hm * 32 + (g4 >> 1) : hal + 128;

    u32x4 in[4]; u32x2 xh;
    {
        const u32x4* x1 = ws + WS_X1;
        const size_t mb = (size_t)tile * 16 * 64 + lane;
        in[2] = x1[mb + (2 * w) * 64]; in[3] = x1[mb + (2 * w + 1) * 64];
        xh = *(const u32x2*)((const char*)(x1 + ((size_t)(gtok >> 5) * 16 + 2 * w + ((c16 >> 2) & 1)) * 64 + (g4 & 1) * 32 + (gtok & 31)) + 8 * (g4 >> 1));
    }
    u32x4 dwr;
    if (tid < 160) dwr = ((const u32x4*)dw)[tid];
    constexpr int NFX = NFC;
    const u32x4* wp = ws + WS_WC + (size_t)w * NFC * 64 + lane;
    if (w == 0) {
        const int tn = tt + lane - 1;
        const bool need = lane < 3 && tn >= 0 && tn < TPB;
        const unsigned* fl = (const unsigned*)(ws + WS_FL) + (need ? tile + lane - 1 : tile) * 16;
        for (unsigned spin = 0; spin < (1u << 24); ++spin) {
            const unsigned v = __hip_atomic_load(fl, __ATOMIC_RELAXED, __HIP_MEMORY_SCOPE_AGENT);
            if (__all(v >= 4u)) break;
            __builtin_amdgcn_s_sleep(1);
        }
    }
    __syncthreads();
    u32x4 hin;
    {
        const __amdgpu_buffer_rsrc_t rs = __builtin_amdgcn_make_buffer_rsrc((void*)(ws + WS_AT), 0, NTILE * 16 * 1024, 0x00020000);
        const int mo = (tile * 16 * 64 + lane + 2 * w * 64) * 16;
        in[0] = __builtin_bit_cast(u32x4, __builtin_amdgcn_raw_buffer_load_b128(rs, mo, 0, 16));
        in[1] = __builtin_bit_cast(u32x4, __builtin_amdgcn_raw_buffer_load_b128(rs, mo + 1024, 0, 16));
        const int ho = (((gtok >> 5) * 16 + 2 * w + ((lane >> 3) & 1)) * 64 + ((lane >> 2) & 1) * 32 + (gtok & 31)) * 16;
        hin = __builtin_bit_cast(u32x4, __builtin_amdgcn_raw_buffer_load_b128(rs, ho, 0, 16));
    }
    f32x16 xv; f32x4 yv;
    fx[(2 * w) * 64 + lane] = in[0]; fx[(2 * w + 1) * 64 + lane] = in[1];
    if (lane < 16) hal[hm * 32 + (2 * w + ((lane >> 3) & 1)) * 2 + ((lane >> 2) & 1)] = hin;
    if (tid >= 480) hal[128 + tid - 480] = (u32x4){0u, 0u, 0u, 0u};
    if (tid < 160) dwl[tid] = dwr;
    unpack16(xv, in[2], in[3]);
    yv = (f32x4){bflo(xh[0]), bfhi(xh[0]), bflo(xh[1]), bfhi(xh[1])};
    __syncthreads();
    {
        f32x16 a = (f32x16){}; f32x4 b = (f32x4){0.f, 0.f, 0.f, 0.f};
        u32x4 bx[2], by[2];
        bx[0] = fx[lane]; by[0] = hrd[0]; bx[1] = fx[64 + lane]; by[1] = hrd[2];
#pragma unroll
        for (int ks = 0; ks < 16; ++ks) {
            a = MFMA(ring[ks & 31], bx[ks & 1], a);
            b = MFMA16(ring[ks & 31], by[ks & 1], b);
            if (ks + 2 < 16) { bx[ks & 1] = fx[(ks + 2) * 64 + lane]; by[ks & 1] = hrd[2 * (ks + 2)]; }
            REFILL(ks, 0);
            SB();
        }
#pragma unroll
        for (int r = 0; r < 16; ++r) xv[r] = rbf(xv[r] + rbf(a[r]));
#pragma unroll
        for (int r = 0; r < 4; ++r) yv[r] = rbf(yv[r] + rbf(b[r]));
    }
    float sc, sh;
    ln_partial(xv, st0, w, lane);
    {
        float s_ = (yv[0] + yv[1]) + (yv[2] + yv[3]), q_ = (yv[0] * yv[0] + yv[1] * yv[1]) + (yv[2] * yv[2] + yv[3] * yv[3]);
        s_ += __shfl_xor(s_, 4); q_ += __shfl_xor(q_, 4);
        s_ += __shfl_xor(s_, 16); q_ += __shfl_xor(q_, 16);
        s_ += __shfl_xor(s_, 32); q_ += __shfl_xor(q_, 32);
        if (lane < 4) { st1[w * 8 + lane] = s_; st1[w * 8 + 4 + lane] = q_; }
    }
    __syncthreads();
    ln_finish(st0, lane, sc, sh);
    norm_store(xv, sc, sh, fx, w, lane);
    {
        float s_ = 0.f, q_ = 0.f;
#pragma unroll
        for (int i = 0; i < 8; ++i) { s_ += st1[i * 8 + hm]; q_ += st1[i * 8 + 4 + hm]; }
        const float m_ = s_ * (1.0f / 256.0f);
        const float sc2 = __builtin_amdgcn_rsqf(fmaxf(q_ * (1.0f / 256.0f) - m_ * m_, 0.f) + 1e-5f), sh2 = -m_ * sc2;
        const u32x2 o_ = {pack2(__builtin_fmaf(yv[0], sc2, sh2), __builtin_fmaf(yv[1], sc2, sh2)), pack2(__builtin_fmaf(yv[2], sc2, sh2), __builtin_fmaf(yv[3], sc2, sh2))};
        if (c16 < 8) *(u32x2*)((char*)(hal + hm * 32 + (2 * w + (c16 >> 2)) * 2 + (g4 & 1)) + 8 * (g4 >> 1)) = o_;
    }
    __syncthreads();
    {
        f32x16 a = (f32x16){}, b = (f32x16){}; f32x4 c = (f32x4){0.f, 0.f, 0.f, 0.f}, d = c;
        u32x4 bxn = fx[lane], byn = hrd[0];
#pragma unroll
        for (int ks = 0; ks < 16; ++ks) {
            const int F = 16 + 2 * ks;
            const u32x4 bx = bxn, by = byn;
            if (ks + 1 < 16) { bxn = fx[(ks + 1) * 64 + lane]; byn = hrd[2 * (ks + 1)]; }
            a = MFMA(ring[F & 31], bx, a); c = MFMA16(ring[F & 31], by, c); REFILL(F, 0);
            b = MFMA(ring[(F + 1) & 31], bx, b); d = MFMA16(ring[(F + 1) & 31], by, d); REFILL(F + 1, 0);
            SB();
        }
#pragma unroll
        for (int r = 0; r < 16; ++r) a[r] = a[r] * sigmoidf_(b[r]);
#pragma unroll
        for (int r = 0; r < 4; ++r) c[r] = hvalid ? c[r] * sigmoidf_(d[r]) : 0.f;
        f32x4* cs = (f32x4*)fh + w * (4 * 2 * 36);
#pragma unroll
        for (int g = 0; g < 4; ++g) cs[(g * 2 + h) * 36 + m + 2] = (f32x4){a[4 * g], a[4 * g + 1], a[4 * g + 2], a[4 * g + 3]};
        if (c16 < 8) cs[((2 * (c16 >> 2) + (g4 >> 1)) * 2 + (g4 & 1)) * 36 + (hm < 2 ? hm : 32 + hm)] = c;
        __syncthreads();
        const uint16_t* dp = (const uint16_t*)dwl + 32 * w + 4 * h;
#pragma unroll
        for (int g = 0; g < 4; ++g) {
            f32x4 acc = (f32x4){0.f, 0.f, 0.f, 0.f};
#pragma unroll
            for (int i = 0; i < 5; ++i) {
                const f32x4 hv = cs[(g * 2 + h) * 36 + m + i];
                const u32x2 t = *(const u32x2*)(dp + i * DM + 8 * g);
                acc[0] += hv[0] * bflo(t[0]); acc[1] += hv[1] * bfhi(t[0]); acc[2] += hv[2] * bflo(t[1]); acc[3] += hv[3] * bfhi(t[1]);
            }
#pragma unroll
            for (int i = 0; i < 4; ++i) a[4 * g + i] = siluf_(acc[i]);
        }
        fx[(2 * w) * 64 + lane] = pack8(a, 0);
        fx[(2 * w + 1) * 64 + lane] = pack8(a, 1);
    }
    __syncthreads();
    {
        f32x16 a;
        GEMM16(a, 48, 64, fx, false);
#pragma unroll
        for (int r = 0; r < 16; ++r) xv[r] = rbf(xv[r] + rbf(a[r]));
    }
    ln_partial(xv, st2, w, lane);
    __syncthreads();
    ln_finish(st2, lane, sc, sh);
    norm_store(xv, sc, sh, fx, w, lane);
    __syncthreads();
    {
        f32x16 acc;
        FFN(acc, 64);
#pragma unroll
        for (int r = 0; r < 16; ++r) xv[r] = rbf(__builtin_fmaf(0.5f, rbf(acc[r]), xv[r]));
    }
    ln_partial(xv, st0, w, lane);
    __syncthreads();
    ln_finish(st0, lane, sc, sh);
    {
        uint16_t* op = out + (size_t)(tile * 32 + m) * DM + 32 * w + 4 * h;
#pragma unroll
        for (int g = 0; g < 4; ++g) {
            u32x2 t;
            t[0] = pack2(__builtin_fmaf(xv[4 * g], sc, sh), __builtin_fmaf(xv[4 * g + 1], sc, sh));
            t[1] = pack2(__builtin_fmaf(xv[4 * g + 2], sc, sh), __builtin_fmaf(xv[4 * g + 3], sc, sh));
            *(u32x2*)(op + 8 * g) = t;
        }
    }
}

__global__ __launch_bounds__(512, 2) void k_attnC(u32x4* ws, const uint16_t* __restrict__ dw, uint16_t* __restrict__ out) {
    __shared__ u32x4 lds[9 * 1024];
    u32x4 ring[32];
    attn_body(ws, ws, ring, lds);
    if (blockIdx.x >= NTILE) return;
    __syncthreads();
    stageC_body(ws, dw, out, ring, lds);
}

extern "C" void kernel_launch(void* const* d_in, const int* in_sizes, int n_in,
                              void* d_out, int out_size, void* d_ws, size_t ws_size,
                              hipStream_t stream) {
    PackArgs p;
    p.w1a = (const uint16_t*)d_in[1]; p.w1b = (const uint16_t*)d_in[2]; p.wqkv = (const uint16_t*)d_in[3]; p.wo = (const uint16_t*)d_in[4];
    p.wp1 = (const uint16_t*)d_in[5]; p.wp2 = (const uint16_t*)d_in[7]; p.w2a = (const uint16_t*)d_in[8]; p.w2b = (const uint16_t*)d_in[9];
    p.ws = (u32x4*)d_ws;
    u32x4* ws = (u32x4*)d_ws;
    const int nchunk = 8 * (NFA + NFC) * 64;
    k_pack<<<(nchunk + 255) / 256, 256, 0, stream>>>(p);
    k_stageA<<<NTILE, 512, 0, stream>>>((const uint16_t*)d_in[0], ws);
    k_attnC<<<256, 512, 0, stream>>>(ws, (const uint16_t*)d_in[6], (uint16_t*)d_out);
}
```
